```python
import torch

def _softmax(x, axis=-1):
    return torch.softmax(x, dim=axis)
def _gelu(x):
    return torch.nn.functional.gelu(x, approximate="tanh")
def _silu(x):
    return torch.nn.functional.silu(x)
def _sigmoid(x):
    return torch.sigmoid(x)
def _layernorm(x, axis=-1, eps=1e-5):
    m = x.mean(dim=axis, keepdim=True)
    v = x.var(dim=axis, keepdim=True, unbiased=False)
    return (x - m) / torch.sqrt(v + eps)
def _scatter_add(tgt, idx, src, axis=0):
    return tgt.clone().index_add_(axis, idx, src)
def _segment_sum(src, seg, n):
    out = torch.zeros((n,) + src.shape[1:], dtype=src.dtype)
    return out.index_add_(0, seg, src)
def _pad(x, pw):
    flat = tuple(v for pair in reversed(pw) for v in pair)
    return torch.nn.functional.pad(x, flat)

kernel_name = 'standalone__geglu_ffn__prompt'

def setup_inputs():

    _w = (lambda a: (lambda t: t.to(torch.bfloat16) if t.is_floating_point() else t)(torch.as_tensor(a)))
    B, S, D, F = 1, 2048, 576, 4608
    return {
        "x": _w((lambda *s: torch.randn(*s, dtype=torch.bfloat16) * 0.05)(B, S, D)),
        "w_gate": _w((lambda *s: torch.randn(*s, dtype=torch.bfloat16) * 0.05)(D, F)),
        "w_up": _w((lambda *s: torch.randn(*s, dtype=torch.bfloat16) * 0.05)(D, F)),
        "w_down": _w((lambda *s: torch.randn(*s, dtype=torch.bfloat16) * 0.05)(F, D)),
    }

def reference(x, w_gate, w_up, w_down):
    return (_gelu(x @ w_gate) * (x @ w_up)) @ w_down
```

```cpp
#include <hip/hip_runtime.h>
#include <hip/hip_bf16.h>
typedef __bf16 bf16x8 __attribute__((ext_vector_type(8)));
typedef float f32x4 __attribute__((ext_vector_type(4)));
constexpr int S=2048, D=576, F=4608;
__global__ void k1(const __bf16* x, const __bf16* wg, const __bf16* wu, __bf16* h){
  int wave = (blockIdx.x*blockDim.x + threadIdx.x)>>6, lane = threadIdx.x&63;
  int tm = wave / (F/16), tn = wave % (F/16);
  f32x4 ag={0,0,0,0}, au={0,0,0,0};
  int r = lane&15, q = lane>>4;
  for(int k=0;k<D;k+=32){
    bf16x8 a = *(const bf16x8*)(x + (tm*16+r)*D + k + q*8);
    bf16x8 bg, bu;
    for(int j=0;j<8;++j){ bg[j]=wg[(k+q*8+j)*F + tn*16+r]; bu[j]=wu[(k+q*8+j)*F + tn*16+r]; }
    ag=__builtin_amdgcn_mfma_f32_16x16x32_bf16(a,bg,ag,0,0,0);
    au=__builtin_amdgcn_mfma_f32_16x16x32_bf16(a,bu,au,0,0,0);
  }
  for(int j=0;j<4;++j){
    float g=ag[j], u=au[j];
    float z = -2.0f*0.7978845608f*(g+0.044715f*g*g*g);
    float hh = g/(1.0f+__expf(z))*u;
    h[(tm*16+q*4+j)*F + tn*16+r] = (__bf16)hh;
  }
}
__global__ void k2(const __bf16* h, const __bf16* wd, __bf16* out){
  int wave = (blockIdx.x*blockDim.x + threadIdx.x)>>6, lane = threadIdx.x&63;
  int tm = wave / (D/16), tn = wave % (D/16);
  f32x4 a4={0,0,0,0};
  int r = lane&15, q = lane>>4;
  for(int k=0;k<F;k+=32){
    bf16x8 a = *(const bf16x8*)(h + (tm*16+r)*F + k + q*8);
    bf16x8 b;
    for(int j=0;j<8;++j){ b[j]=wd[(k+q*8+j)*D + tn*16+r]; }
    a4=__builtin_amdgcn_mfma_f32_16x16x32_bf16(a,b,a4,0,0,0);
  }
  for(int j=0;j<4;++j) out[(tm*16+q*4+j)*D + tn*16+r] = (__bf16)a4[j];
}
extern "C" void kernel_launch(void* const* d_in, const int* in_sizes, int n_in,
                              void* d_out, int out_size, void* d_ws, size_t ws_size,
                              hipStream_t stream) {
  const __bf16* x=(const __bf16*)d_in[0]; const __bf16* wg=(const __bf16*)d_in[1];
  const __bf16* wu=(const __bf16*)d_in[2]; const __bf16* wd=(const __bf16*)d_in[3];
  __bf16* h=(__bf16*)d_ws;
  k1<<<(S/16)*(F/16)/4,256,0,stream>>>(x,wg,wu,h);
  k2<<<(S/16)*(D/16)/4,256,0,stream>>>(h,wd,(__bf16*)d_out);
}
```

```cpp
#include <hip/hip_runtime.h>
#include <hip/hip_bf16.h>

typedef unsigned u32x16 __attribute__((ext_vector_type(16)));
typedef unsigned u32x8 __attribute__((ext_vector_type(8)));
typedef unsigned u32x4 __attribute__((ext_vector_type(4)));
typedef unsigned u32x2 __attribute__((ext_vector_type(2)));

constexpr int kS = 2048, kD = 576, kF = 4608;
constexpr unsigned WS_FLAGS = 0, WS_F2 = 256u << 10, WS_DBG = 512u << 10, WS_H = 1u << 20, WS_P = 24u << 20;
constexpr int NPX = 2;

__global__ __launch_bounds__(256, 1) void geglu_k1(const char* __restrict__ x, const char* __restrict__ wg,
                                                   const char* __restrict__ wu, const char* __restrict__ wd, char* __restrict__ ws,
                                                   char* __restrict__ out) {
  __shared__ __attribute__((aligned(1024))) char lds[163840];
  unsigned pfd = 0;
  {
    unsigned long long pc;
    unsigned li = (((blockIdx.x >> 3) * 4 + (threadIdx.x >> 6)) * 4 + ((threadIdx.x & 63u) < 3u ? (threadIdx.x & 63u) : 3u)) * 128u;
    asm volatile("s_getpc_b64 %1\n.Lgeglu_code_pc:\n v_min_u32 %2, .Lgeglu_code_end-.Lgeglu_code_pc-4, %2\n global_load_dword %0, %2, %1"
                 : "={v229}"(pfd), "=&s"(pc), "+v"(li) :: "memory");
    __builtin_amdgcn_sched_barrier(0);
  }
#define TPRE_OP
  const unsigned b = blockIdx.x, xcd = b & 7u, idx = b >> 3;
  const unsigned w = __builtin_amdgcn_readfirstlane(threadIdx.x >> 6), l = threadIdx.x & 63u;
  const unsigned p = idx & 1u;
  constexpr unsigned NS = 16 / NPX;
  const unsigned r = idx >> 1;
  const unsigned m = (xcd % (8 / NPX)) * NPX + (r / NS);
  const unsigned rl = r % NS;
  const unsigned rb = (xcd / (8 / NPX)) * NS + rl;
  const unsigned sl = 2 * m + p;
  const unsigned lb = (unsigned)(size_t)lds;
  u32x16 s0, s1; u32x8 s2; u32x16 v;
  const unsigned fullf = 576 * m + 320 * p, fullfp = 576 * m + 320 * (1 - p), fulld2 = 640 * p, halfd2 = 512 + 64 * p;
  s1[4] = rb * 128 * 1152;
  s1[5] = fullf * 2;
  s1[6] = (576 * m + 256 + 32 * p) * 2;
  s1[7] = fullf * 1152 + fulld2;
  s1[8] = fullfp * 1152 + fulld2;
  s1[9] = (576 * m + 256 + 16 * (w ^ (2 * p)) - 16 * w) * 1152 + fulld2;
  s1[10] = fullf * 1152 + halfd2;
  s1[11] = fullfp * 1152 + halfd2;
  s1[12] = (576 * m + 256) * 1152 + halfd2;
  s1[13] = lb + 6144 * w;
  s1[14] = lb + 4096 * w;
  s1[15] = WS_H + ((rb * 16 + sl) * 4 + w) * 18432;
  s2[0] = WS_H + ((rb * 16 + (sl ^ 1)) * 4 + w) * 18432;
  const unsigned fl = WS_FLAGS + ((rb * 8 + m) * 4 + w) * 384;
  s2[1] = fl + 128 * (1 + p);
  s2[2] = fl + 128 * (2 - p);
  s2[3] = fl;
  s2[4] = WS_P + ((rb * 2 + p) * 4 + w) * 18 * 8192 + m * 1024;
  s2[5] = 576 * m * 1152 + fulld2;
  s2[6] = WS_DBG + (b * 4 + w) * 64;
  const unsigned orow = (rb * 128 + 32 * w + 4 * m) * 1152;
  s2[7] = orow + 512 + 64 * p;
  u32x8 s3; u32x8 v2;
  const unsigned g2 = (rb * 2 + p) * 4 + w;
  s3[4] = WS_F2 + g2 * 256;
  s3[5] = WS_F2 + g2 * 256 + 128 + 4 * m;
  s3[6] = WS_P + g2 * 18 * 8192;
  s3[7] = orow + 640 * p;
  {
    const unsigned cc = l & 15u, hf = cc >> 3, hh = (cc >> 2) & 1u, rr = cc & 3u, rho = l >> 4;
    v2[0] = rho * 1024 + hf * 8192 + (32 * hh + rr + 4 * m) * 16;
    v2[1] = rr * 1152 + (16 * hf + 8 * (rho >> 1) + 4 * hh + 2 * (rho & 1u)) * 2;
    v2[2] = 128 + 4 * (l & 7u);
    v2[3] = v2[4] = v2[5] = v2[6] = v2[7] = 0;
  }
  const unsigned h = l >> 5, g1 = (l >> 4) & 1u, q = (l & 15u) >> 2, p4 = l & 3u, j = l & 31u;
  const unsigned swz = 16 * ((l & 7u) ^ (4 * ((l >> 4) & 1u)));
  v[0] = lb + h * 1024 + q * 128 + 16 * ((2 * g1 + (p4 >> 1)) ^ (4 * (q >> 1))) + 8 * (p4 & 1u);
  v[1] = lb + h * 512 + q * 64 + 32 * g1 + 8 * p4;
  v[2] = (16 * w + (l >> 3)) * 9216 + swz;
  v[3] = (16 * w + (l >> 3)) * 1152 + swz;
  v[4] = (16 * w + (l >> 2)) * 9216 + 16 * (l & 3u) + 1024;
  v[5] = (32 * w + (l >> 2)) * 1152 + 16 * (l & 3u);
  v[6] = (16 * (w ^ (2 * p)) + (l >> 2)) * 1152 + 16 * (l & 3u);
  v[7] = (32 * w + (l >> 3)) * 1152 + 16 * ((l & 7u) ^ (l >> 4));
  v[8] = (32 * w + 8 + (l >> 3)) * 1152 + 16 * ((l & 7u) ^ (4 + (l >> 4))) - 1024;
  v[9] = lb + 4096 * w + j * 128 + 16 * (h ^ ((j >> 1) & 7u));
  v[10] = l * 16;
  constexpr unsigned RPW = 576 / (4 * NS);
  const unsigned prow = (rl * 4 + w) * RPW + (l < RPW ? l : RPW - 1);
  v[11] = prow * 9216;
  v[12] = prow * 1152;
  v[13] = h;
  v[14] = (idx * 4 + w) * 4 + (l < 4 ? l : 3);
  v[15] = 0;
  __builtin_amdgcn_sched_barrier(0);
  const unsigned long long px = (unsigned long long)x, pg = (unsigned long long)wg - 4096, pu = (unsigned long long)wu - 4096,
                           pd = (unsigned long long)wd - 4096, pw = (unsigned long long)ws;
  s0[0] = (unsigned)px; s0[1] = (unsigned)(px >> 32) & 0xffffu; s0[2] = kS * kD * 2; s0[3] = 0x20000u;
  s0[4] = (unsigned)pg; s0[5] = (unsigned)(pg >> 32) & 0xffffu; s0[6] = kD * kF * 2 + 4096; s0[7] = 0x20000u;
  s0[8] = (unsigned)pu; s0[9] = (unsigned)(pu >> 32) & 0xffffu; s0[10] = kD * kF * 2 + 4096; s0[11] = 0x20000u;
  s0[12] = (unsigned)pd; s0[13] = (unsigned)(pd >> 32) & 0xffffu; s0[14] = kD * kF * 2 + 4096; s0[15] = 0x20000u;
  s1[0] = (unsigned)pw; s1[1] = (unsigned)(pw >> 32) & 0xffffu; s1[2] = 64u << 20; s1[3] = 0x20000u;
  const unsigned long long po = (unsigned long long)out;
  s3[0] = (unsigned)po; s3[1] = (unsigned)(po >> 32) & 0xffffu; s3[2] = kS * kD * 2; s3[3] = 0x20000u;
  asm volatile(
    "v_add_u32 v235, 16384, v247\n"
    "v_add_u32 v236, 16384, v248\n"
    "v_add_u32 v225, 72704, v242\n"
    "v_subrev_u32 v227, 1024, v244\n"
    "s_add_u32 m0, s66, 73728\n"
    "s_add_u32 s84, s56, 0\n"
    "buffer_load_dwordx4 v247, s[36:39], s84 offen lds\n"
    "buffer_load_dwordx4 v248, s[36:39], s84 offen offset:1024 lds\n"
    "buffer_load_dwordx4 v235, s[36:39], s84 offen offset:2048 lds\n"
    "buffer_load_dwordx4 v236, s[36:39], s84 offen offset:3072 lds\n"
    "s_add_u32 m0, s65, 0\n"
    "s_add_u32 s84, s57, 4096\n"
    "buffer_load_dwordx4 v242, s[40:43], s84 offen lds\n"
    "buffer_load_dwordx4 v225, s[40:43], s84 offen offset:1024 lds\n"
    "s_add_u32 s84, s57, 591872\n"
    "buffer_load_dwordx4 v242, s[40:43], s84 offen offset:2048 lds\n"
    "buffer_load_dwordx4 v225, s[40:43], s84 offen offset:3072 lds\n"
    "s_add_u32 m0, s65, 4096\n"
    "s_add_u32 s84, s57, 1183744\n"
    "buffer_load_dwordx4 v242, s[40:43], s84 offen lds\n"
    "buffer_load_dwordx4 v225, s[40:43], s84 offen offset:1024 lds\n"
    "buffer_load_dword v230, off, s[52:55], s71 sc1\n"
    "buffer_load_dword v239, off, s[52:55], s80 sc1\n"
    "s_add_u32 m0, s66, 90112\n"
    "s_add_u32 s84, s56, 128\n"
    "buffer_load_dwordx4 v247, s[36:39], s84 offen lds\n"
    "buffer_load_dwordx4 v248, s[36:39], s84 offen offset:1024 lds\n"
    "buffer_load_dwordx4 v235, s[36:39], s84 offen offset:2048 lds\n"
    "buffer_load_dwordx4 v236, s[36:39], s84 offen offset:3072 lds\n"
    "s_add_u32 m0, s65, 24576\n"
    "s_add_u32 s84, s57, 1773568\n"
    "buffer_load_dwordx4 v242, s[40:43], s84 offen lds\n"
    "buffer_load_dwordx4 v225, s[40:43], s84 offen offset:1024 lds\n"
    "s_add_u32 s84, s57, 2361344\n"
    "buffer_load_dwordx4 v242, s[40:43], s84 offen offset:2048 lds\n"
    "buffer_load_dwordx4 v225, s[40:43], s84 offen offset:3072 lds\n"
    "s_add_u32 m0, s65, 28672\n"
    "s_add_u32 s84, s57, 2953216\n"
    "buffer_load_dwordx4 v242, s[40:43], s84 offen lds\n"
    "buffer_load_dwordx4 v225, s[40:43], s84 offen offset:1024 lds\n"
    "s_add_u32 m0, s66, 106496\n"
    "s_add_u32 s84, s56, 256\n"
    "buffer_load_dwordx4 v247, s[36:39], s84 offen lds\n"
    "buffer_load_dwordx4 v248, s[36:39], s84 offen offset:1024 lds\n"
    "buffer_load_dwordx4 v235, s[36:39], s84 offen offset:2048 lds\n"
    "buffer_load_dwordx4 v236, s[36:39], s84 offen offset:3072 lds\n"
    "v_mov_b32 v216, v240\n"
    "v_add_u32 v217, 65536, v216\n"
    "v_add_u32 v218, 131072, v216\n"
    "v_xor_b32 v219, 64, v240\n"
    "v_add_u32 v220, 65536, v219\n"
    "v_add_u32 v221, 131072, v219\n"
    "v_mov_b32 v222, v241\n"
    "v_add_u32 v223, 65536, v241\n"
    "v_add_u32 v224, 131072, v241\n"
    "v_add_u32 v226, 8192, v243\n"
    "v_add_u32 v228, 17408, v245\n"
    "v_mov_b32 v231, 0xbdd2d3e8\n"
    "v_add_u32 v96, 73728, v249\n"
    "v_xor_b32 v97, 32, v249\n"
    "v_add_u32 v97, 73728, v97\n"
    "v_xor_b32 v98, 64, v249\n"
    "v_add_u32 v98, 73728, v98\n"
    "v_xor_b32 v99, 96, v249\n"
    "v_add_u32 v99, 73728, v99\n"
    "v_add_u32 v100, 65536, v96\n"
    "v_add_u32 v101, 65536, v97\n"
    "v_add_u32 v102, 65536, v98\n"
    "v_add_u32 v103, 65536, v99\n"
    "s_waitcnt vmcnt(22)\n"
    "ds_read_b128 a[0:3], v96 offset:0\n"
    "ds_read_b128 a[4:7], v97 offset:0\n"
    "ds_read_b128 a[8:11], v98 offset:0\n"
    "ds_read_b128 a[12:15], v99 offset:0\n"
    "s_waitcnt vmcnt(16)\n"
    "s_barrier\n"
    "ds_read_b64_tr_b16 v[168:169], v216 offset:0\n"
    "ds_read_b64_tr_b16 v[170:171], v216 offset:512\n"
    "ds_read_b64_tr_b16 v[172:173], v219 offset:0\n"
    "ds_read_b64_tr_b16 v[174:175], v219 offset:512\n"
    "ds_read_b64_tr_b16 v[176:177], v216 offset:6144\n"
    "ds_read_b64_tr_b16 v[178:179], v216 offset:6656\n"
    "ds_read_b64_tr_b16 v[180:181], v219 offset:6144\n"
    "ds_read_b64_tr_b16 v[182:183], v219 offset:6656\n"
    "ds_read_b64_tr_b16 v[184:185], v216 offset:12288\n"
    "ds_read_b64_tr_b16 v[186:187], v216 offset:12800\n"
    "ds_read_b64_tr_b16 v[188:189], v219 offset:12288\n"
    "ds_read_b64_tr_b16 v[190:191], v219 offset:12800\n"
    "ds_read_b64_tr_b16 v[192:193], v216 offset:18432\n"
    "ds_read_b64_tr_b16 v[194:195], v216 offset:18944\n"
    "s_waitcnt lgkmcnt(14)\n"
    "s_waitcnt lgkmcnt(6)\n"
    "v_mfma_f32_32x32x16_bf16 v[0:15], v[168:171], a[0:3], 0\n"
    "ds_read_b64_tr_b16 v[196:197], v219 offset:18432\n"
    "ds_read_b64_tr_b16 v[198:199], v219 offset:18944\n"
    "s_add_u32 m0, s66, 122880\n"
    "s_add_u32 s84, s56, 384\n"
    "buffer_load_dwordx4 v247, s[36:39], s84 offen lds\n"
    "v_mfma_f32_32x32x16_bf16 v[16:31], v[172:175], a[0:3], 0\n"
    "ds_read_b64_tr_b16 v[168:169], v216 offset:2048\n"
    "ds_read_b64_tr_b16 v[170:171], v216 offset:2560\n"
    "buffer_load_dwordx4 v248, s[36:39], s84 offen offset:1024 lds\n"
    "v_mfma_f32_32x32x16_bf16 v[0:15], v[176:179], a[4:7], v[0:15]\n"
    "ds_read_b64_tr_b16 v[172:173], v219 offset:2048\n"
    "ds_read_b64_tr_b16 v[174:175], v219 offset:2560\n"
    "buffer_load_dwordx4 v235, s[36:39], s84 offen offset:2048 lds\n"
    "v_mfma_f32_32x32x16_bf16 v[16:31], v[180:183], a[4:7], v[16:31]\n"
    "ds_read_b64_tr_b16 v[176:177], v216 offset:8192\n"
    "ds_read_b64_tr_b16 v[178:179], v216 offset:8704\n"
    "s_waitcnt vmcnt(13)\n"
    "ds_read_b128 a[16:19], v96 offset:16384\n"
    "ds_read_b128 a[20:23], v97 offset:16384\n"
    "ds_read_b128 a[24:27], v98 offset:16384\n"
    "ds_read_b128 a[28:31], v99 offset:16384\n"
    "s_waitcnt lgkmcnt(10)\n"
    "v_mfma_f32_32x32x16_bf16 v[0:15], v[184:187], a[8:11], v[0:15]\n"
    "ds_read_b64_tr_b16 v[180:181], v219 offset:8192\n"
    "ds_read_b64_tr_b16 v[182:183], v219 offset:8704\n"
    "buffer_load_dwordx4 v236, s[36:39], s84 offen offset:3072 lds\n"
    "s_add_u32 m0, s66, 139264\n"
    "s_add_u32 s84, s56, 512\n"
    "v_mfma_f32_32x32x16_bf16 v[16:31], v[188:191], a[8:11], v[16:31]\n"
    "ds_read_b64_tr_b16 v[184:185], v216 offset:14336\n"
    "ds_read_b64_tr_b16 v[186:187], v216 offset:14848\n"
    "buffer_load_dwordx4 v247, s[36:39], s84 offen lds\n"
    "v_mfma_f32_32x32x16_bf16 v[0:15], v[192:195], a[12:15], v[0:15]\n"
    "ds_read_b64_tr_b16 v[188:189], v219 offset:14336\n"
    "ds_read_b64_tr_b16 v[190:191], v219 offset:14848\n"
    "buffer_load_dwordx4 v248, s[36:39], s84 offen offset:1024 lds\n"
    "v_mfma_f32_32x32x16_bf16 v[16:31], v[196:199], a[12:15], v[16:31]\n"
    "ds_read_b64_tr_b16 v[192:193], v216 offset:20480\n"
    "ds_read_b64_tr_b16 v[194:195], v216 offset:20992\n"
    "buffer_load_dwordx4 v235, s[36:39], s84 offen offset:2048 lds\n"
    "s_waitcnt lgkmcnt(8)\n"
    "s_waitcnt lgkmcnt(6)\n"
    "v_mfma_f32_32x32x16_bf16 v[0:15], v[168:171], a[16:19], v[0:15]\n"
    "ds_read_b64_tr_b16 v[196:197], v219 offset:20480\n"
    "ds_read_b64_tr_b16 v[198:199], v219 offset:20992\n"
    "buffer_load_dwordx4 v236, s[36:39], s84 offen offset:3072 lds\n"
    "s_add_u32 m0, s65, 49152\n"
    "s_add_u32 s84, s57, 3543040\n"
    "v_mfma_f32_32x32x16_bf16 v[16:31], v[172:175], a[16:19], v[16:31]\n"
    "ds_read_b64_tr_b16 v[168:169], v216 offset:4096\n"
    "ds_read_b64_tr_b16 v[170:171], v216 offset:4608\n"
    "buffer_load_dwordx4 v242, s[40:43], s84 offen lds\n"
    "v_mfma_f32_32x32x16_bf16 v[0:15], v[176:179], a[20:23], v[0:15]\n"
    "ds_read_b64_tr_b16 v[172:173], v219 offset:4096\n"
    "ds_read_b64_tr_b16 v[174:175], v219 offset:4608\n"
    "s_waitcnt vmcnt(9)\n"
    "ds_read_b128 a[32:35], v96 offset:32768\n"
    "ds_read_b128 a[36:39], v97 offset:32768\n"
    "ds_read_b128 a[40:43], v98 offset:32768\n"
    "ds_read_b128 a[44:47], v99 offset:32768\n"
    "v_mfma_f32_32x32x16_bf16 v[16:31], v[180:183], a[20:23], v[16:31]\n"
    "ds_read_b64_tr_b16 v[176:177], v216 offset:10240\n"
    "ds_read_b64_tr_b16 v[178:179], v216 offset:10752\n"
    "buffer_load_dwordx4 v225, s[40:43], s84 offen offset:1024 lds\n"
    "s_add_u32 s84, s57, 4130816\n"
    "s_waitcnt lgkmcnt(10)\n"
    "v_mfma_f32_32x32x16_bf16 v[0:15], v[184:187], a[24:27], v[0:15]\n"
    "ds_read_b64_tr_b16 v[180:181], v219 offset:10240\n"
    "ds_read_b64_tr_b16 v[182:183], v219 offset:10752\n"
    "buffer_load_dwordx4 v242, s[40:43], s84 offen offset:2048 lds\n"
    "v_mfma_f32_32x32x16_bf16 v[16:31], v[188:191], a[24:27], v[16:31]\n"
    "ds_read_b64_tr_b16 v[184:185], v216 offset:16384\n"
    "ds_read_b64_tr_b16 v[186:187], v216 offset:16896\n"
    "buffer_load_dwordx4 v225, s[40:43], s84 offen offset:3072 lds\n"
    "s_add_u32 m0, s65, 53248\n"
    "s_add_u32 s84, s57, 4722688\n"
    "v_mfma_f32_32x32x16_bf16 v[0:15], v[192:195], a[28:31], v[0:15]\n"
    "ds_read_b64_tr_b16 v[188:189], v219 offset:16384\n"
    "ds_read_b64_tr_b16 v[190:191], v219 offset:16896\n"
    "buffer_load_dwordx4 v242, s[40:43], s84 offen lds\n"
    "v_mfma_f32_32x32x16_bf16 v[16:31], v[196:199], a[28:31], v[16:31]\n"
    "ds_read_b64_tr_b16 v[192:193], v216 offset:22528\n"
    "ds_read_b64_tr_b16 v[194:195], v216 offset:23040\n"
    "buffer_load_dwordx4 v225, s[40:43], s84 offen offset:1024 lds\n"
    "s_add_u32 m0, s66, 73728\n"
    "s_waitcnt lgkmcnt(10)\n"
    "s_waitcnt lgkmcnt(6)\n"
    "v_mfma_f32_32x32x16_bf16 v[0:15], v[168:171], a[32:35], v[0:15]\n"
    "s_barrier\n"
    "v_mfma_f32_32x32x16_bf16 v[16:31], v[172:175], a[32:35], v[16:31]\n"
    "ds_read_b64_tr_b16 v[196:197], v219 offset:22528\n"
    "ds_read_b64_tr_b16 v[198:199], v219 offset:23040\n"
    "ds_read_b64_tr_b16 v[168:169], v216 offset:24576\n"
    "ds_read_b64_tr_b16 v[170:171], v216 offset:25088\n"
    "s_waitcnt vmcnt(10)\n"
    "ds_read_b128 a[48:51], v96 offset:49152\n"
    "ds_read_b128 a[52:55], v97 offset:49152\n"
    "ds_read_b128 a[56:59], v98 offset:49152\n"
    "ds_read_b128 a[60:63], v99 offset:49152\n"
    "v_mfma_f32_32x32x16_bf16 v[0:15], v[176:179], a[36:39], v[0:15]\n"
    "ds_read_b64_tr_b16 v[172:173], v219 offset:24576\n"
    "ds_read_b64_tr_b16 v[174:175], v219 offset:25088\n"
    "s_add_u32 s84, s56, 640\n"
    "buffer_load_dwordx4 v247, s[36:39], s84 offen lds\n"
    "v_mfma_f32_32x32x16_bf16 v[16:31], v[180:183], a[36:39], v[16:31]\n"
    "ds_read_b64_tr_b16 v[176:177], v216 offset:30720\n"
    "ds_read_b64_tr_b16 v[178:179], v216 offset:31232\n"
    "buffer_load_dwordx4 v248, s[36:39], s84 offen offset:1024 lds\n"
    "s_waitcnt lgkmcnt(10)\n"
    "v_mfma_f32_32x32x16_bf16 v[0:15], v[184:187], a[40:43], v[0:15]\n"
    "ds_read_b64_tr_b16 v[180:181], v219 offset:30720\n"
    "ds_read_b64_tr_b16 v[182:183], v219 offset:31232\n"
    "buffer_load_dwordx4 v235, s[36:39], s84 offen offset:2048 lds\n"
    "v_mfma_f32_32x32x16_bf16 v[16:31], v[188:191], a[40:43], v[16:31]\n"
    "ds_read_b64_tr_b16 v[184:185], v216 offset:36864\n"
    "ds_read_b64_tr_b16 v[186:187], v216 offset:37376\n"
    "buffer_load_dwordx4 v236, s[36:39], s84 offen offset:3072 lds\n"
    "s_add_u32 m0, s66, 90112\n"
    "s_add_u32 s84, s56, 768\n"
    "v_mfma_f32_32x32x16_bf16 v[0:15], v[192:195], a[44:47], v[0:15]\n"
    "ds_read_b64_tr_b16 v[188:189], v219 offset:36864\n"
    "ds_read_b64_tr_b16 v[190:191], v219 offset:37376\n"
    "buffer_load_dwordx4 v247, s[36:39], s84 offen lds\n"
    "v_mfma_f32_32x32x16_bf16 v[16:31], v[196:199], a[44:47], v[16:31]\n"
    "ds_read_b64_tr_b16 v[192:193], v216 offset:43008\n"
    "ds_read_b64_tr_b16 v[194:195], v216 offset:43520\n"
    "buffer_load_dwordx4 v248, s[36:39], s84 offen offset:1024 lds\n"
    "s_waitcnt lgkmcnt(12)\n"
    "s_waitcnt lgkmcnt(6)\n"
    "v_mfma_f32_32x32x16_bf16 v[0:15], v[168:171], a[48:51], v[0:15]\n"
    "ds_read_b64_tr_b16 v[196:197], v219 offset:43008\n"
    "ds_read_b64_tr_b16 v[198:199], v219 offset:43520\n"
    "s_add_u32 s86, s57, 4096\n"
    "buffer_load_dword v255, v251, s[44:47], s86 offen\n"
    "v_mfma_f32_32x32x16_bf16 v[16:31], v[172:175], a[48:51], v[16:31]\n"
    "ds_read_b64_tr_b16 v[168:169], v216 offset:26624\n"
    "ds_read_b64_tr_b16 v[170:171], v216 offset:27136\n"
    "buffer_load_dwordx4 v235, s[36:39], s84 offen offset:2048 lds\n"
    "v_mfma_f32_32x32x16_bf16 v[0:15], v[176:179], a[52:55], v[0:15]\n"
    "ds_read_b64_tr_b16 v[172:173], v219 offset:26624\n"
    "ds_read_b64_tr_b16 v[174:175], v219 offset:27136\n"
    "s_waitcnt vmcnt(14)\n"
    "ds_read_b128 a[64:67], v100 offset:0\n"
    "ds_read_b128 a[68:71], v101 offset:0\n"
    "ds_read_b128 a[72:75], v102 offset:0\n"
    "ds_read_b128 a[76:79], v103 offset:0\n"
    "v_mfma_f32_32x32x16_bf16 v[16:31], v[180:183], a[52:55], v[16:31]\n"
    "ds_read_b64_tr_b16 v[176:177], v216 offset:32768\n"
    "ds_read_b64_tr_b16 v[178:179], v216 offset:33280\n"
    "buffer_load_dwordx4 v236, s[36:39], s84 offen offset:3072 lds\n"
    "s_add_u32 m0, s66, 106496\n"
    "s_waitcnt lgkmcnt(10)\n"
    "v_mfma_f32_32x32x16_bf16 v[0:15], v[184:187], a[56:59], v[0:15]\n"
    "ds_read_b64_tr_b16 v[180:181], v219 offset:32768\n"
    "ds_read_b64_tr_b16 v[182:183], v219 offset:33280\n"
    "s_add_u32 s84, s56, 896\n"
    "buffer_load_dwordx4 v247, s[36:39], s84 offen lds\n"
    "v_mfma_f32_32x32x16_bf16 v[16:31], v[188:191], a[56:59], v[16:31]\n"
    "ds_read_b64_tr_b16 v[184:185], v216 offset:38912\n"
    "ds_read_b64_tr_b16 v[186:187], v216 offset:39424\n"
    "buffer_load_dwordx4 v248, s[36:39], s84 offen offset:1024 lds\n"
    "v_mfma_f32_32x32x16_bf16 v[0:15], v[192:195], a[60:63], v[0:15]\n"
    "ds_read_b64_tr_b16 v[188:189], v219 offset:38912\n"
    "ds_read_b64_tr_b16 v[190:191], v219 offset:39424\n"
    "buffer_load_dwordx4 v235, s[36:39], s84 offen offset:2048 lds\n"
    "v_mfma_f32_32x32x16_bf16 v[16:31], v[196:199], a[60:63], v[16:31]\n"
    "ds_read_b64_tr_b16 v[192:193], v216 offset:45056\n"
    "ds_read_b64_tr_b16 v[194:195], v216 offset:45568\n"
    "buffer_load_dwordx4 v236, s[36:39], s84 offen offset:3072 lds\n"
    "s_add_u32 m0, s66, 122880\n"
    "s_waitcnt lgkmcnt(10)\n"
    "s_waitcnt lgkmcnt(6)\n"
    "v_mfma_f32_32x32x16_bf16 v[0:15], v[168:171], a[64:67], v[0:15]\n"
    "ds_read_b64_tr_b16 v[196:197], v219 offset:45056\n"
    "ds_read_b64_tr_b16 v[198:199], v219 offset:45568\n"
    "s_add_u32 s84, s56, 1024\n"
    "buffer_load_dwordx4 v247, s[36:39], s84 offen lds\n"
    "v_mfma_f32_32x32x16_bf16 v[16:31], v[172:175], a[64:67], v[16:31]\n"
    "ds_read_b64_tr_b16 v[168:169], v216 offset:28672\n"
    "ds_read_b64_tr_b16 v[170:171], v216 offset:29184\n"
    "buffer_load_dwordx4 v248, s[36:39], s84 offen offset:1024 lds\n"
    "v_mfma_f32_32x32x16_bf16 v[0:15], v[176:179], a[68:71], v[0:15]\n"
    "ds_read_b64_tr_b16 v[172:173], v219 offset:28672\n"
    "ds_read_b64_tr_b16 v[174:175], v219 offset:29184\n"
    "s_waitcnt vmcnt(11)\n"
    "ds_read_b128 a[80:83], v96 offset:0\n"
    "ds_read_b128 a[84:87], v97 offset:0\n"
    "ds_read_b128 a[88:91], v98 offset:0\n"
    "ds_read_b128 a[92:95], v99 offset:0\n"
    "v_mfma_f32_32x32x16_bf16 v[16:31], v[180:183], a[68:71], v[16:31]\n"
    "ds_read_b64_tr_b16 v[176:177], v216 offset:34816\n"
    "ds_read_b64_tr_b16 v[178:179], v216 offset:35328\n"
    "buffer_load_dwordx4 v235, s[36:39], s84 offen offset:2048 lds\n"
    "s_waitcnt lgkmcnt(10)\n"
    "v_mfma_f32_32x32x16_bf16 v[0:15], v[184:187], a[72:75], v[0:15]\n"
    "ds_read_b64_tr_b16 v[180:181], v219 offset:34816\n"
    "ds_read_b64_tr_b16 v[182:183], v219 offset:35328\n"
    "s_add_u32 s86, s57, 4224\n"
    "buffer_load_dword v255, v251, s[40:43], s86 offen\n"
    "v_mfma_f32_32x32x16_bf16 v[16:31], v[188:191], a[72:75], v[16:31]\n"
    "ds_read_b64_tr_b16 v[184:185], v216 offset:40960\n"
    "ds_read_b64_tr_b16 v[186:187], v216 offset:41472\n"
    "buffer_load_dwordx4 v236, s[36:39], s84 offen offset:3072 lds\n"
    "v_mfma_f32_32x32x16_bf16 v[0:15], v[192:195], a[76:79], v[0:15]\n"
    "ds_read_b64_tr_b16 v[188:189], v219 offset:40960\n"
    "ds_read_b64_tr_b16 v[190:191], v219 offset:41472\n"
    "v_mfma_f32_32x32x16_bf16 v[16:31], v[196:199], a[76:79], v[16:31]\n"
    "ds_read_b64_tr_b16 v[192:193], v216 offset:47104\n"
    "ds_read_b64_tr_b16 v[194:195], v216 offset:47616\n"
    "s_waitcnt lgkmcnt(10)\n"
    "s_waitcnt lgkmcnt(6)\n"
    "v_mfma_f32_32x32x16_bf16 v[0:15], v[168:171], a[80:83], v[0:15]\n"
    "s_barrier\n"
    "v_add_u32 v230, 8, v230\n"
    "v_add_u32 v239, 8, v239\n"
    "v_mfma_f32_32x32x16_bf16 v[16:31], v[172:175], a[80:83], v[16:31]\n"
    "ds_read_b64_tr_b16 v[196:197], v219 offset:47104\n"
    "ds_read_b64_tr_b16 v[198:199], v219 offset:47616\n"
    "ds_read_b64_tr_b16 v[168:169], v216 offset:49152\n"
    "ds_read_b64_tr_b16 v[170:171], v216 offset:49664\n"
    "s_add_u32 m0, s65, 0\n"
    "s_add_u32 s84, s57, 4096\n"
    "v_mfma_f32_32x32x16_bf16 v[0:15], v[176:179], a[84:87], v[0:15]\n"
    "ds_read_b64_tr_b16 v[172:173], v219 offset:49152\n"
    "ds_read_b64_tr_b16 v[174:175], v219 offset:49664\n"
    "s_waitcnt vmcnt(9)\n"
    "ds_read_b128 a[96:99], v96 offset:16384\n"
    "ds_read_b128 a[100:103], v97 offset:16384\n"
    "ds_read_b128 a[104:107], v98 offset:16384\n"
    "ds_read_b128 a[108:111], v99 offset:16384\n"
    "v_mfma_f32_32x32x16_bf16 v[16:31], v[180:183], a[84:87], v[16:31]\n"
    "ds_read_b64_tr_b16 v[176:177], v216 offset:55296\n"
    "ds_read_b64_tr_b16 v[178:179], v216 offset:55808\n"
    "buffer_load_dwordx4 v242, s[44:47], s84 offen lds\n"
    "s_waitcnt lgkmcnt(10)\n"
    "v_mfma_f32_32x32x16_bf16 v[0:15], v[184:187], a[88:91], v[0:15]\n"
    "ds_read_b64_tr_b16 v[180:181], v219 offset:55296\n"
    "ds_read_b64_tr_b16 v[182:183], v219 offset:55808\n"
    "buffer_load_dwordx4 v225, s[44:47], s84 offen offset:1024 lds\n"
    "s_add_u32 s84, s57, 591872\n"
    "v_mfma_f32_32x32x16_bf16 v[16:31], v[188:191], a[88:91], v[16:31]\n"
    "ds_read_b64_tr_b16 v[184:185], v216 offset:61440\n"
    "ds_read_b64_tr_b16 v[186:187], v216 offset:61952\n"
    "buffer_load_dwordx4 v242, s[44:47], s84 offen offset:2048 lds\n"
    "v_mfma_f32_32x32x16_bf16 v[0:15], v[192:195], a[92:95], v[0:15]\n"
    "ds_read_b64_tr_b16 v[188:189], v219 offset:61440\n"
    "ds_read_b64_tr_b16 v[190:191], v219 offset:61952\n"
    "buffer_load_dwordx4 v225, s[44:47], s84 offen offset:3072 lds\n"
    "s_add_u32 m0, s65, 4096\n"
    "s_add_u32 s84, s57, 1183744\n"
    "v_mfma_f32_32x32x16_bf16 v[16:31], v[196:199], a[92:95], v[16:31]\n"
    "ds_read_b64_tr_b16 v[192:193], v217 offset:2048\n"
    "ds_read_b64_tr_b16 v[194:195], v217 offset:2560\n"
    "buffer_load_dwordx4 v242, s[44:47], s84 offen lds\n"
    "s_waitcnt lgkmcnt(10)\n"
    "s_waitcnt lgkmcnt(6)\n"
    "v_mfma_f32_32x32x16_bf16 v[0:15], v[168:171], a[96:99], v[0:15]\n"
    "ds_read_b64_tr_b16 v[196:197], v220 offset:2048\n"
    "ds_read_b64_tr_b16 v[198:199], v220 offset:2560\n"
    "s_add_u32 s86, s57, 4224\n"
    "buffer_load_dword v255, v251, s[44:47], s86 offen\n"
    "v_mfma_f32_32x32x16_bf16 v[16:31], v[172:175], a[96:99], v[16:31]\n"
    "ds_read_b64_tr_b16 v[168:169], v216 offset:51200\n"
    "ds_read_b64_tr_b16 v[170:171], v216 offset:51712\n"
    "buffer_load_dwordx4 v225, s[44:47], s84 offen offset:1024 lds\n"
    "s_add_u32 m0, s65, 139264\n"
    "s_add_u32 s84, s57, 1773568\n"
    "v_mfma_f32_32x32x16_bf16 v[0:15], v[176:179], a[100:103], v[0:15]\n"
    "ds_read_b64_tr_b16 v[172:173], v219 offset:51200\n"
    "ds_read_b64_tr_b16 v[174:175], v219 offset:51712\n"
    "s_waitcnt vmcnt(12)\n"
    "ds_read_b128 a[112:115], v96 offset:32768\n"
    "ds_read_b128 a[116:119], v97 offset:32768\n"
    "ds_read_b128 a[120:123], v98 offset:32768\n"
    "ds_read_b128 a[124:127], v99 offset:32768\n"
    "v_mfma_f32_32x32x16_bf16 v[16:31], v[180:183], a[100:103], v[16:31]\n"
    "ds_read_b64_tr_b16 v[176:177], v216 offset:57344\n"
    "ds_read_b64_tr_b16 v[178:179], v216 offset:57856\n"
    "buffer_load_dwordx4 v242, s[44:47], s84 offen lds\n"
    "s_waitcnt lgkmcnt(10)\n"
    "v_mfma_f32_32x32x16_bf16 v[0:15], v[184:187], a[104:107], v[0:15]\n"
    "ds_read_b64_tr_b16 v[180:181], v219 offset:57344\n"
    "ds_read_b64_tr_b16 v[182:183], v219 offset:57856\n"
    "buffer_load_dwordx4 v225, s[44:47], s84 offen offset:1024 lds\n"
    "s_add_u32 s84, s57, 2361344\n"
    "v_mfma_f32_32x32x16_bf16 v[16:31], v[188:191], a[104:107], v[16:31]\n"
    "ds_read_b64_tr_b16 v[184:185], v216 offset:63488\n"
    "ds_read_b64_tr_b16 v[186:187], v216 offset:64000\n"
    "buffer_load_dwordx4 v242, s[44:47], s84 offen offset:2048 lds\n"
    "v_mfma_f32_32x32x16_bf16 v[0:15], v[192:195], a[108:111], v[0:15]\n"
    "ds_read_b64_tr_b16 v[188:189], v219 offset:63488\n"
    "ds_read_b64_tr_b16 v[190:191], v219 offset:64000\n"
    "buffer_load_dwordx4 v225, s[44:47], s84 offen offset:3072 lds\n"
    "s_add_u32 m0, s65, 143360\n"
    "s_add_u32 s84, s57, 2953216\n"
    "v_mfma_f32_32x32x16_bf16 v[16:31], v[196:199], a[108:111], v[16:31]\n"
    "ds_read_b64_tr_b16 v[192:193], v217 offset:4096\n"
    "ds_read_b64_tr_b16 v[194:195], v217 offset:4608\n"
    "buffer_load_dwordx4 v242, s[44:47], s84 offen lds\n"
    "s_waitcnt lgkmcnt(10)\n"
    "s_waitcnt lgkmcnt(6)\n"
    "v_mfma_f32_32x32x16_bf16 v[0:15], v[168:171], a[112:115], v[0:15]\n"
    "ds_read_b64_tr_b16 v[196:197], v220 offset:4096\n"
    "ds_read_b64_tr_b16 v[198:199], v220 offset:4608\n"
    "buffer_load_dwordx4 v225, s[44:47], s84 offen offset:1024 lds\n"
    "v_mfma_f32_32x32x16_bf16 v[16:31], v[172:175], a[112:115], v[16:31]\n"
    "ds_read_b64_tr_b16 v[168:169], v216 offset:53248\n"
    "ds_read_b64_tr_b16 v[170:171], v216 offset:53760\n"
    "v_mfma_f32_32x32x16_bf16 v[0:15], v[176:179], a[116:119], v[0:15]\n"
    "ds_read_b64_tr_b16 v[172:173], v219 offset:53248\n"
    "ds_read_b64_tr_b16 v[174:175], v219 offset:53760\n"
    "s_waitcnt vmcnt(13)\n"
    "ds_read_b128 a[128:131], v96 offset:49152\n"
    "ds_read_b128 a[132:135], v97 offset:49152\n"
    "ds_read_b128 a[136:139], v98 offset:49152\n"
    "ds_read_b128 a[140:143], v99 offset:49152\n"
    "v_mfma_f32_32x32x16_bf16 v[16:31], v[180:183], a[116:119], v[16:31]\n"
    "ds_read_b64_tr_b16 v[176:177], v216 offset:59392\n"
    "ds_read_b64_tr_b16 v[178:179], v216 offset:59904\n"
    "s_waitcnt lgkmcnt(10)\n"
    "v_mfma_f32_32x32x16_bf16 v[0:15], v[184:187], a[120:123], v[0:15]\n"
    "ds_read_b64_tr_b16 v[180:181], v219 offset:59392\n"
    "ds_read_b64_tr_b16 v[182:183], v219 offset:59904\n"
    "v_mfma_f32_32x32x16_bf16 v[16:31], v[188:191], a[120:123], v[16:31]\n"
    "ds_read_b64_tr_b16 v[184:185], v217 offset:0\n"
    "ds_read_b64_tr_b16 v[186:187], v217 offset:512\n"
    "v_mfma_f32_32x32x16_bf16 v[0:15], v[192:195], a[124:127], v[0:15]\n"
    "ds_read_b64_tr_b16 v[188:189], v220 offset:0\n"
    "ds_read_b64_tr_b16 v[190:191], v220 offset:512\n"
    "v_mfma_f32_32x32x16_bf16 v[16:31], v[196:199], a[124:127], v[16:31]\n"
    "ds_read_b64_tr_b16 v[192:193], v217 offset:6144\n"
    "ds_read_b64_tr_b16 v[194:195], v217 offset:6656\n"
    "s_waitcnt lgkmcnt(10)\n"
    "s_waitcnt lgkmcnt(6)\n"
    "v_mfma_f32_32x32x16_bf16 v[0:15], v[168:171], a[128:131], v[0:15]\n"
    "s_waitcnt vmcnt(6)\n"
    "s_barrier\n"
    "v_mfma_f32_32x32x16_bf16 v[16:31], v[172:175], a[128:131], v[16:31]\n"
    "ds_read_b64_tr_b16 v[196:197], v220 offset:6144\n"
    "ds_read_b64_tr_b16 v[198:199], v220 offset:6656\n"
    "ds_read_b64_tr_b16 v[168:169], v216 offset:0\n"
    "ds_read_b64_tr_b16 v[170:171], v216 offset:512\n"
    "s_add_u32 m0, s65, 24576\n"
    "s_add_u32 s84, s57, 3543040\n"
    "v_mfma_f32_32x32x16_bf16 v[0:15], v[176:179], a[132:135], v[0:15]\n"
    "ds_read_b64_tr_b16 v[172:173], v219 offset:0\n"
    "ds_read_b64_tr_b16 v[174:175], v219 offset:512\n"
    "buffer_load_dwordx4 v242, s[44:47], s84 offen lds\n"
    "v_mfma_f32_32x32x16_bf16 v[16:31], v[180:183], a[132:135], v[16:31]\n"
    "ds_read_b64_tr_b16 v[176:177], v216 offset:6144\n"
    "ds_read_b64_tr_b16 v[178:179], v216 offset:6656\n"
    "s_waitcnt lgkmcnt(6)\n"
    "v_mfma_f32_32x32x16_bf16 v[0:15], v[184:187], a[136:139], v[0:15]\n"
    "ds_read_b64_tr_b16 v[180:181], v219 offset:6144\n"
    "ds_read_b64_tr_b16 v[182:183], v219 offset:6656\n"
    "v_mfma_f32_32x32x16_bf16 v[16:31], v[188:191], a[136:139], v[16:31]\n"
    "ds_read_b64_tr_b16 v[184:185], v216 offset:12288\n"
    "ds_read_b64_tr_b16 v[186:187], v216 offset:12800\n"
    "buffer_load_dwordx4 v225, s[44:47], s84 offen offset:1024 lds\n"
    "s_add_u32 s84, s57, 4130816\n"
    "v_mfma_f32_32x32x16_bf16 v[0:15], v[192:195], a[140:143], v[0:15]\n"
    "ds_read_b64_tr_b16 v[188:189], v219 offset:12288\n"
    "ds_read_b64_tr_b16 v[190:191], v219 offset:12800\n"
    "v_mfma_f32_32x32x16_bf16 v[16:31], v[196:199], a[140:143], v[16:31]\n"
    "ds_read_b64_tr_b16 v[192:193], v216 offset:18432\n"
    "ds_read_b64_tr_b16 v[194:195], v216 offset:18944\n"
    "s_waitcnt lgkmcnt(6)\n"
    "v_mfma_f32_32x32x16_bf16 v[64:79], v[168:171], a[0:3], 0\n"
    "ds_read_b64_tr_b16 v[196:197], v219 offset:18432\n"
    "ds_read_b64_tr_b16 v[198:199], v219 offset:18944\n"
    "s_add_u32 s86, s58, 4096\n"
    "buffer_load_dword v255, v251, s[40:43], s86 offen\n"
    "v_mfma_f32_32x32x16_bf16 v[80:95], v[172:175], a[0:3], 0\n"
    "ds_read_b64_tr_b16 v[168:169], v216 offset:2048\n"
    "ds_read_b64_tr_b16 v[170:171], v216 offset:2560\n"
    "buffer_load_dwordx4 v242, s[44:47], s84 offen offset:2048 lds\n"
    "v_mfma_f32_32x32x16_bf16 v[64:79], v[176:179], a[4:7], v[64:79]\n"
    "ds_read_b64_tr_b16 v[172:173], v219 offset:2048\n"
    "ds_read_b64_tr_b16 v[174:175], v219 offset:2560\n"
    "v_mul_f32 v200, v0, v0\n"
    "v_mul_f32 v201, v1, v1\n"
    "v_mul_f32 v202, v2, v2\n"
    "v_mul_f32 v203, v3, v3\n"
    "v_mfma_f32_32x32x16_bf16 v[80:95], v[180:183], a[4:7], v[80:95]\n"
    "ds_read_b64_tr_b16 v[176:177], v216 offset:8192\n"
    "ds_read_b64_tr_b16 v[178:179], v216 offset:8704\n"
    "v_fmaak_f32 v200, v200, v231, 0xc0135761\n"
    "v_fmaak_f32 v201, v201, v231, 0xc0135761\n"
    "v_fmaak_f32 v202, v202, v231, 0xc0135761\n"
    "s_waitcnt lgkmcnt(6)\n"
    "v_mfma_f32_32x32x16_bf16 v[64:79], v[184:187], a[8:11], v[64:79]\n"
    "ds_read_b64_tr_b16 v[180:181], v219 offset:8192\n"
    "ds_read_b64_tr_b16 v[182:183], v219 offset:8704\n"
    "buffer_load_dwordx4 v225, s[44:47], s84 offen offset:3072 lds\n"
    "s_add_u32 m0, s65, 28672\n"
    "s_add_u32 s84, s57, 4722688\n"
    "v_mfma_f32_32x32x16_bf16 v[80:95], v[188:191], a[8:11], v[80:95]\n"
    "ds_read_b64_tr_b16 v[184:185], v216 offset:14336\n"
    "ds_read_b64_tr_b16 v[186:187], v216 offset:14848\n"
    "v_fmaak_f32 v203, v203, v231, 0xc0135761\n"
    "v_mul_f32 v200, v200, v0\n"
    "v_mul_f32 v201, v201, v1\n"
    "v_mul_f32 v202, v202, v2\n"
    "v_mfma_f32_32x32x16_bf16 v[64:79], v[192:195], a[12:15], v[64:79]\n"
    "ds_read_b64_tr_b16 v[188:189], v219 offset:14336\n"
    "ds_read_b64_tr_b16 v[190:191], v219 offset:14848\n"
    "v_mul_f32 v203, v203, v3\n"
    "v_exp_f32 v200, v200\n"
    "v_mfma_f32_32x32x16_bf16 v[80:95], v[196:199], a[12:15], v[80:95]\n"
    "ds_read_b64_tr_b16 v[192:193], v216 offset:20480\n"
    "ds_read_b64_tr_b16 v[194:195], v216 offset:20992\n"
    "buffer_load_dwordx4 v242, s[44:47], s84 offen lds\n"
    "s_waitcnt lgkmcnt(6)\n"
    "v_mfma_f32_32x32x16_bf16 v[64:79], v[168:171], a[16:19], v[64:79]\n"
    "ds_read_b64_tr_b16 v[196:197], v219 offset:20480\n"
    "ds_read_b64_tr_b16 v[198:199], v219 offset:20992\n"
    "v_exp_f32 v201, v201\n"
    "v_exp_f32 v202, v202\n"
    "v_mfma_f32_32x32x16_bf16 v[80:95], v[172:175], a[16:19], v[80:95]\n"
    "ds_read_b64_tr_b16 v[168:169], v216 offset:4096\n"
    "ds_read_b64_tr_b16 v[170:171], v216 offset:4608\n"
    "v_exp_f32 v203, v203\n"
    "v_add_f32 v200, 1.0, v200\n"
    "v_add_f32 v201, 1.0, v201\n"
    "v_mfma_f32_32x32x16_bf16 v[64:79], v[176:179], a[20:23], v[64:79]\n"
    "ds_read_b64_tr_b16 v[172:173], v219 offset:4096\n"
    "ds_read_b64_tr_b16 v[174:175], v219 offset:4608\n"
    "buffer_load_dwordx4 v225, s[44:47], s84 offen offset:1024 lds\n"
    "s_add_u32 m0, s65, 73728\n"
    "s_add_u32 s84, s57, 4224\n"
    "v_add_f32 v202, 1.0, v202\n"
    "v_mfma_f32_32x32x16_bf16 v[80:95], v[180:183], a[20:23], v[80:95]\n"
    "ds_read_b64_tr_b16 v[176:177], v216 offset:10240\n"
    "ds_read_b64_tr_b16 v[178:179], v216 offset:10752\n"
    "v_add_f32 v203, 1.0, v203\n"
    "v_rcp_f32 v200, v200\n"
    "s_waitcnt lgkmcnt(6)\n"
    "v_mfma_f32_32x32x16_bf16 v[64:79], v[184:187], a[24:27], v[64:79]\n"
    "ds_read_b64_tr_b16 v[180:181], v219 offset:10240\n"
    "ds_read_b64_tr_b16 v[182:183], v219 offset:10752\n"
    "v_rcp_f32 v201, v201\n"
    "v_rcp_f32 v202, v202\n"
    "v_mfma_f32_32x32x16_bf16 v[80:95], v[188:191], a[24:27], v[80:95]\n"
    "ds_read_b64_tr_b16 v[184:185], v216 offset:16384\n"
    "ds_read_b64_tr_b16 v[186:187], v216 offset:16896\n"
    "buffer_load_dwordx4 v242, s[40:43], s84 offen lds\n"
    "v_mfma_f32_32x32x16_bf16 v[64:79], v[192:195], a[28:31], v[64:79]\n"
    "ds_read_b64_tr_b16 v[188:189], v219 offset:16384\n"
    "ds_read_b64_tr_b16 v[190:191], v219 offset:16896\n"
    "v_rcp_f32 v203, v203\n"
    "v_mul_f32 v0, v0, v200\n"
    "v_mul_f32 v1, v1, v201\n"
    "v_mfma_f32_32x32x16_bf16 v[80:95], v[196:199], a[28:31], v[80:95]\n"
    "ds_read_b64_tr_b16 v[192:193], v216 offset:22528\n"
    "ds_read_b64_tr_b16 v[194:195], v216 offset:23040\n"
    "v_mul_f32 v2, v2, v202\n"
    "v_mul_f32 v3, v3, v203\n"
    "v_mul_f32 v204, v4, v4\n"
    "s_waitcnt lgkmcnt(6)\n"
    "v_mfma_f32_32x32x16_bf16 v[64:79], v[168:171], a[32:35], v[64:79]\n"
    "s_waitcnt vmcnt(8)\n"
    "s_barrier\n"
    "v_mfma_f32_32x32x16_bf16 v[80:95], v[172:175], a[32:35], v[80:95]\n"
    "ds_read_b64_tr_b16 v[196:197], v219 offset:22528\n"
    "ds_read_b64_tr_b16 v[198:199], v219 offset:23040\n"
    "ds_read_b64_tr_b16 v[168:169], v218 offset:8192\n"
    "ds_read_b64_tr_b16 v[170:171], v218 offset:8704\n"
    "buffer_load_dwordx4 v225, s[40:43], s84 offen offset:1024 lds\n"
    "v_mul_f32 v205, v5, v5\n"
    "v_mul_f32 v206, v6, v6\n"
    "v_mul_f32 v207, v7, v7\n"
    "v_fmaak_f32 v204, v204, v231, 0xc0135761\n"
    "v_fmaak_f32 v205, v205, v231, 0xc0135761\n"
    "v_mfma_f32_32x32x16_bf16 v[64:79], v[176:179], a[36:39], v[64:79]\n"
    "ds_read_b64_tr_b16 v[172:173], v221 offset:8192\n"
    "ds_read_b64_tr_b16 v[174:175], v221 offset:8704\n"
    "s_add_u32 s84, s57, 592000\n"
    "v_fmaak_f32 v206, v206, v231, 0xc0135761\n"
    "v_fmaak_f32 v207, v207, v231, 0xc0135761\n"
    "v_mul_f32 v204, v204, v4\n"
    "v_mfma_f32_32x32x16_bf16 v[80:95], v[180:183], a[36:39], v[80:95]\n"
    "ds_read_b64_tr_b16 v[176:177], v218 offset:14336\n"
    "ds_read_b64_tr_b16 v[178:179], v218 offset:14848\n"
    "v_mul_f32 v205, v205, v5\n"
    "v_mul_f32 v206, v206, v6\n"
    "v_mul_f32 v207, v207, v7\n"
    "s_waitcnt lgkmcnt(6)\n"
    "v_mfma_f32_32x32x16_bf16 v[64:79], v[184:187], a[40:43], v[64:79]\n"
    "ds_read_b64_tr_b16 v[180:181], v221 offset:14336\n"
    "ds_read_b64_tr_b16 v[182:183], v221 offset:14848\n"
    "buffer_load_dwordx4 v242, s[40:43], s84 offen offset:2048 lds\n"
    "v_exp_f32 v204, v204\n"
    "v_mfma_f32_32x32x16_bf16 v[80:95], v[188:191], a[40:43], v[80:95]\n"
    "ds_read_b64_tr_b16 v[184:185], v218 offset:20480\n"
    "ds_read_b64_tr_b16 v[186:187], v218 offset:20992\n"
    "v_exp_f32 v205, v205\n"
    "v_exp_f32 v206, v206\n"
    "v_mfma_f32_32x32x16_bf16 v[64:79], v[192:195], a[44:47], v[64:79]\n"
    "ds_read_b64_tr_b16 v[188:189], v221 offset:20480\n"
    "ds_read_b64_tr_b16 v[190:191], v221 offset:20992\n"
    "v_exp_f32 v207, v207\n"
    "v_add_f32 v204, 1.0, v204\n"
    "v_add_f32 v205, 1.0, v205\n"
    "v_mfma_f32_32x32x16_bf16 v[80:95], v[196:199], a[44:47], v[80:95]\n"
    "ds_read_b64_tr_b16 v[192:193], v218 offset:26624\n"
    "ds_read_b64_tr_b16 v[194:195], v218 offset:27136\n"
    "buffer_load_dwordx4 v225, s[40:43], s84 offen offset:3072 lds\n"
    "s_add_u32 m0, s65, 77824\n"
    "s_waitcnt lgkmcnt(6)\n"
    "v_mfma_f32_32x32x16_bf16 v[64:79], v[168:171], a[48:51], v[64:79]\n"
    "ds_read_b64_tr_b16 v[196:197], v221 offset:26624\n"
    "ds_read_b64_tr_b16 v[198:199], v221 offset:27136\n"
    "s_add_u32 s84, s57, 1183872\n"
    "v_add_f32 v206, 1.0, v206\n"
    "v_add_f32 v207, 1.0, v207\n"
    "v_mfma_f32_32x32x16_bf16 v[80:95], v[172:175], a[48:51], v[80:95]\n"
    "ds_read_b64_tr_b16 v[168:169], v218 offset:10240\n"
    "ds_read_b64_tr_b16 v[170:171], v218 offset:10752\n"
    "v_rcp_f32 v204, v204\n"
    "v_rcp_f32 v205, v205\n"
    "v_mfma_f32_32x32x16_bf16 v[64:79], v[176:179], a[52:55], v[64:79]\n"
    "ds_read_b64_tr_b16 v[172:173], v221 offset:10240\n"
    "ds_read_b64_tr_b16 v[174:175], v221 offset:10752\n"
    "buffer_load_dwordx4 v242, s[40:43], s84 offen lds\n"
    "v_rcp_f32 v206, v206\n"
    "v_mfma_f32_32x32x16_bf16 v[80:95], v[180:183], a[52:55], v[80:95]\n"
    "ds_read_b64_tr_b16 v[176:177], v218 offset:16384\n"
    "ds_read_b64_tr_b16 v[178:179], v218 offset:16896\n"
    "v_rcp_f32 v207, v207\n"
    "v_mul_f32 v4, v4, v204\n"
    "s_waitcnt lgkmcnt(6)\n"
    "v_mfma_f32_32x32x16_bf16 v[64:79], v[184:187], a[56:59], v[64:79]\n"
    "ds_read_b64_tr_b16 v[180:181], v221 offset:16384\n"
    "ds_read_b64_tr_b16 v[182:183], v221 offset:16896\n"
    "v_mul_f32 v5, v5, v205\n"
    "v_mul_f32 v6, v6, v206\n"
    "v_mul_f32 v7, v7, v207\n"
    "v_mul_f32 v200, v8, v8\n"
    "v_mfma_f32_32x32x16_bf16 v[80:95], v[188:191], a[56:59], v[80:95]\n"
    "ds_read_b64_tr_b16 v[184:185], v218 offset:22528\n"
    "ds_read_b64_tr_b16 v[186:187], v218 offset:23040\n"
    "buffer_load_dwordx4 v225, s[40:43], s84 offen offset:1024 lds\n"
    "s_add_u32 m0, s65, 98304\n"
    "s_add_u32 s84, s57, 1773696\n"
    "v_mul_f32 v201, v9, v9\n"
    "v_mfma_f32_32x32x16_bf16 v[64:79], v[192:195], a[60:63], v[64:79]\n"
    "ds_read_b64_tr_b16 v[188:189], v221 offset:22528\n"
    "ds_read_b64_tr_b16 v[190:191], v221 offset:23040\n"
    "v_mul_f32 v202, v10, v10\n"
    "v_mul_f32 v203, v11, v11\n"
    "v_fmaak_f32 v200, v200, v231, 0xc0135761\n"
    "v_fmaak_f32 v201, v201, v231, 0xc0135761\n"
    "v_mfma_f32_32x32x16_bf16 v[80:95], v[196:199], a[60:63], v[80:95]\n"
    "ds_read_b64_tr_b16 v[192:193], v218 offset:28672\n"
    "ds_read_b64_tr_b16 v[194:195], v218 offset:29184\n"
    "v_fmaak_f32 v202, v202, v231, 0xc0135761\n"
    "v_fmaak_f32 v203, v203, v231, 0xc0135761\n"
    "v_mul_f32 v200, v200, v8\n"
    "s_waitcnt lgkmcnt(6)\n"
    "v_mfma_f32_32x32x16_bf16 v[64:79], v[168:171], a[64:67], v[64:79]\n"
    "ds_read_b64_tr_b16 v[196:197], v221 offset:28672\n"
    "ds_read_b64_tr_b16 v[198:199], v221 offset:29184\n"
    "buffer_load_dwordx4 v242, s[40:43], s84 offen lds\n"
    "v_mul_f32 v201, v201, v9\n"
    "v_mfma_f32_32x32x16_bf16 v[80:95], v[172:175], a[64:67], v[80:95]\n"
    "ds_read_b64_tr_b16 v[168:169], v218 offset:12288\n"
    "ds_read_b64_tr_b16 v[170:171], v218 offset:12800\n"
    "v_mul_f32 v202, v202, v10\n"
    "v_mul_f32 v203, v203, v11\n"
    "v_exp_f32 v200, v200\n"
    "v_mfma_f32_32x32x16_bf16 v[64:79], v[176:179], a[68:71], v[64:79]\n"
    "ds_read_b64_tr_b16 v[172:173], v221 offset:12288\n"
    "ds_read_b64_tr_b16 v[174:175], v221 offset:12800\n"
    "v_exp_f32 v201, v201\n"
    "v_exp_f32 v202, v202\n"
    "v_mfma_f32_32x32x16_bf16 v[80:95], v[180:183], a[68:71], v[80:95]\n"
    "ds_read_b64_tr_b16 v[176:177], v218 offset:18432\n"
    "ds_read_b64_tr_b16 v[178:179], v218 offset:18944\n"
    "buffer_load_dwordx4 v225, s[40:43], s84 offen offset:1024 lds\n"
    "s_add_u32 s84, s57, 2361472\n"
    "s_waitcnt lgkmcnt(6)\n"
    "v_mfma_f32_32x32x16_bf16 v[64:79], v[184:187], a[72:75], v[64:79]\n"
    "ds_read_b64_tr_b16 v[180:181], v221 offset:18432\n"
    "ds_read_b64_tr_b16 v[182:183], v221 offset:18944\n"
    "v_exp_f32 v203, v203\n"
    "v_add_f32 v200, 1.0, v200\n"
    "v_add_f32 v201, 1.0, v201\n"
    "v_mfma_f32_32x32x16_bf16 v[80:95], v[188:191], a[72:75], v[80:95]\n"
    "ds_read_b64_tr_b16 v[184:185], v218 offset:24576\n"
    "ds_read_b64_tr_b16 v[186:187], v218 offset:25088\n"
    "v_add_f32 v202, 1.0, v202\n"
    "v_add_f32 v203, 1.0, v203\n"
    "v_rcp_f32 v200, v200\n"
    "v_mfma_f32_32x32x16_bf16 v[64:79], v[192:195], a[76:79], v[64:79]\n"
    "ds_read_b64_tr_b16 v[188:189], v221 offset:24576\n"
    "ds_read_b64_tr_b16 v[190:191], v221 offset:25088\n"
    "buffer_load_dwordx4 v242, s[40:43], s84 offen offset:2048 lds\n"
    "v_mfma_f32_32x32x16_bf16 v[80:95], v[196:199], a[76:79], v[80:95]\n"
    "ds_read_b64_tr_b16 v[192:193], v218 offset:30720\n"
    "ds_read_b64_tr_b16 v[194:195], v218 offset:31232\n"
    "v_rcp_f32 v201, v201\n"
    "s_waitcnt lgkmcnt(6)\n"
    "v_mfma_f32_32x32x16_bf16 v[64:79], v[168:171], a[80:83], v[64:79]\n"
    "s_waitcnt vmcnt(9)\n"
    "s_barrier\n"
    "v_mfma_f32_32x32x16_bf16 v[80:95], v[172:175], a[80:83], v[80:95]\n"
    "ds_read_b64_tr_b16 v[196:197], v221 offset:30720\n"
    "ds_read_b64_tr_b16 v[198:199], v221 offset:31232\n"
    "ds_read_b64_tr_b16 v[168:169], v216 offset:24576\n"
    "ds_read_b64_tr_b16 v[170:171], v216 offset:25088\n"
    "buffer_load_dwordx4 v225, s[40:43], s84 offen offset:3072 lds\n"
    "v_rcp_f32 v202, v202\n"
    "v_rcp_f32 v203, v203\n"
    "v_mul_f32 v8, v8, v200\n"
    "v_mfma_f32_32x32x16_bf16 v[64:79], v[176:179], a[84:87], v[64:79]\n"
    "ds_read_b64_tr_b16 v[172:173], v219 offset:24576\n"
    "ds_read_b64_tr_b16 v[174:175], v219 offset:25088\n"
    "s_add_u32 m0, s65, 102400\n"
    "s_add_u32 s84, s57, 2953344\n"
    "v_mul_f32 v9, v9, v201\n"
    "v_mul_f32 v10, v10, v202\n"
    "v_mul_f32 v11, v11, v203\n"
    "v_mfma_f32_32x32x16_bf16 v[80:95], v[180:183], a[84:87], v[80:95]\n"
    "ds_read_b64_tr_b16 v[176:177], v216 offset:30720\n"
    "ds_read_b64_tr_b16 v[178:179], v216 offset:31232\n"
    "v_mul_f32 v204, v12, v12\n"
    "v_mul_f32 v205, v13, v13\n"
    "v_mul_f32 v206, v14, v14\n"
    "s_waitcnt lgkmcnt(6)\n"
    "v_mfma_f32_32x32x16_bf16 v[64:79], v[184:187], a[88:91], v[64:79]\n"
    "ds_read_b64_tr_b16 v[180:181], v219 offset:30720\n"
    "ds_read_b64_tr_b16 v[182:183], v219 offset:31232\n"
    "buffer_load_dwordx4 v242, s[40:43], s84 offen lds\n"
    "v_mul_f32 v207, v15, v15\n"
    "v_fmaak_f32 v204, v204, v231, 0xc0135761\n"
    "v_mfma_f32_32x32x16_bf16 v[80:95], v[188:191], a[88:91], v[80:95]\n"
    "ds_read_b64_tr_b16 v[184:185], v216 offset:36864\n"
    "ds_read_b64_tr_b16 v[186:187], v216 offset:37376\n"
    "v_fmaak_f32 v205, v205, v231, 0xc0135761\n"
    "v_fmaak_f32 v206, v206, v231, 0xc0135761\n"
    "v_fmaak_f32 v207, v207, v231, 0xc0135761\n"
    "v_mul_f32 v204, v204, v12\n"
    "v_mfma_f32_32x32x16_bf16 v[64:79], v[192:195], a[92:95], v[64:79]\n"
    "ds_read_b64_tr_b16 v[188:189], v219 offset:36864\n"
    "ds_read_b64_tr_b16 v[190:191], v219 offset:37376\n"
    "v_mul_f32 v205, v205, v13\n"
    "v_mul_f32 v206, v206, v14\n"
    "v_mul_f32 v207, v207, v15\n"
    "v_mfma_f32_32x32x16_bf16 v[80:95], v[196:199], a[92:95], v[80:95]\n"
    "ds_read_b64_tr_b16 v[192:193], v216 offset:43008\n"
    "ds_read_b64_tr_b16 v[194:195], v216 offset:43520\n"
    "buffer_load_dwordx4 v225, s[40:43], s84 offen offset:1024 lds\n"
    "s_add_u32 m0, s65, 49152\n"
    "s_waitcnt lgkmcnt(6)\n"
    "v_mfma_f32_32x32x16_bf16 v[64:79], v[168:171], a[96:99], v[64:79]\n"
    "ds_read_b64_tr_b16 v[196:197], v219 offset:43008\n"
    "ds_read_b64_tr_b16 v[198:199], v219 offset:43520\n"
    "s_add_u32 s84, s57, 3543168\n"
    "v_exp_f32 v204, v204\n"
    "v_exp_f32 v205, v205\n"
    "v_mfma_f32_32x32x16_bf16 v[80:95], v[172:175], a[96:99], v[80:95]\n"
    "ds_read_b64_tr_b16 v[168:169], v216 offset:26624\n"
    "ds_read_b64_tr_b16 v[170:171], v216 offset:27136\n"
    "v_exp_f32 v206, v206\n"
    "v_exp_f32 v207, v207\n"
    "v_mfma_f32_32x32x16_bf16 v[64:79], v[176:179], a[100:103], v[64:79]\n"
    "ds_read_b64_tr_b16 v[172:173], v219 offset:26624\n"
    "ds_read_b64_tr_b16 v[174:175], v219 offset:27136\n"
    "buffer_load_dwordx4 v242, s[40:43], s84 offen lds\n"
    "v_add_f32 v204, 1.0, v204\n"
    "v_mfma_f32_32x32x16_bf16 v[80:95], v[180:183], a[100:103], v[80:95]\n"
    "ds_read_b64_tr_b16 v[176:177], v216 offset:32768\n"
    "ds_read_b64_tr_b16 v[178:179], v216 offset:33280\n"
    "v_add_f32 v205, 1.0, v205\n"
    "v_add_f32 v206, 1.0, v206\n"
    "v_add_f32 v207, 1.0, v207\n"
    "s_waitcnt lgkmcnt(6)\n"
    "v_mfma_f32_32x32x16_bf16 v[64:79], v[184:187], a[104:107], v[64:79]\n"
    "ds_read_b64_tr_b16 v[180:181], v219 offset:32768\n"
    "ds_read_b64_tr_b16 v[182:183], v219 offset:33280\n"
    "v_rcp_f32 v204, v204\n"
    "v_rcp_f32 v205, v205\n"
    "v_mfma_f32_32x32x16_bf16 v[80:95], v[188:191], a[104:107], v[80:95]\n"
    "ds_read_b64_tr_b16 v[184:185], v216 offset:38912\n"
    "ds_read_b64_tr_b16 v[186:187], v216 offset:39424\n"
    "buffer_load_dwordx4 v225, s[40:43], s84 offen offset:1024 lds\n"
    "s_add_u32 s84, s57, 4130944\n"
    "v_mfma_f32_32x32x16_bf16 v[64:79], v[192:195], a[108:111], v[64:79]\n"
    "ds_read_b64_tr_b16 v[188:189], v219 offset:38912\n"
    "ds_read_b64_tr_b16 v[190:191], v219 offset:39424\n"
    "v_rcp_f32 v206, v206\n"
    "v_rcp_f32 v207, v207\n"
    "v_mfma_f32_32x32x16_bf16 v[80:95], v[196:199], a[108:111], v[80:95]\n"
    "ds_read_b64_tr_b16 v[192:193], v216 offset:45056\n"
    "ds_read_b64_tr_b16 v[194:195], v216 offset:45568\n"
    "v_mul_f32 v12, v12, v204\n"
    "v_mul_f32 v13, v13, v205\n"
    "v_mul_f32 v14, v14, v206\n"
    "s_waitcnt lgkmcnt(6)\n"
    "v_mfma_f32_32x32x16_bf16 v[64:79], v[168:171], a[112:115], v[64:79]\n"
    "ds_read_b64_tr_b16 v[196:197], v219 offset:45056\n"
    "ds_read_b64_tr_b16 v[198:199], v219 offset:45568\n"
    "buffer_load_dwordx4 v242, s[40:43], s84 offen offset:2048 lds\n"
    "v_mul_f32 v15, v15, v207\n"
    "v_mfma_f32_32x32x16_bf16 v[80:95], v[172:175], a[112:115], v[80:95]\n"
    "ds_read_b64_tr_b16 v[168:169], v216 offset:28672\n"
    "ds_read_b64_tr_b16 v[170:171], v216 offset:29184\n"
    "v_mul_f32 v200, v16, v16\n"
    "v_mul_f32 v201, v17, v17\n"
    "v_mul_f32 v202, v18, v18\n"
    "v_mul_f32 v203, v19, v19\n"
    "v_mfma_f32_32x32x16_bf16 v[64:79], v[176:179], a[116:119], v[64:79]\n"
    "ds_read_b64_tr_b16 v[172:173], v219 offset:28672\n"
    "ds_read_b64_tr_b16 v[174:175], v219 offset:29184\n"
    "v_fmaak_f32 v200, v200, v231, 0xc0135761\n"
    "v_fmaak_f32 v201, v201, v231, 0xc0135761\n"
    "v_fmaak_f32 v202, v202, v231, 0xc0135761\n"
    "v_fmaak_f32 v203, v203, v231, 0xc0135761\n"
    "v_mfma_f32_32x32x16_bf16 v[80:95], v[180:183], a[116:119], v[80:95]\n"
    "ds_read_b64_tr_b16 v[176:177], v216 offset:34816\n"
    "ds_read_b64_tr_b16 v[178:179], v216 offset:35328\n"
    "buffer_load_dwordx4 v225, s[40:43], s84 offen offset:3072 lds\n"
    "s_add_u32 m0, s65, 53248\n"
    "s_waitcnt lgkmcnt(6)\n"
    "v_mfma_f32_32x32x16_bf16 v[64:79], v[184:187], a[120:123], v[64:79]\n"
    "ds_read_b64_tr_b16 v[180:181], v219 offset:34816\n"
    "ds_read_b64_tr_b16 v[182:183], v219 offset:35328\n"
    "s_add_u32 s84, s57, 4722816\n"
    "v_mul_f32 v200, v200, v16\n"
    "v_mul_f32 v201, v201, v17\n"
    "v_mul_f32 v202, v202, v18\n"
    "v_mfma_f32_32x32x16_bf16 v[80:95], v[188:191], a[120:123], v[80:95]\n"
    "ds_read_b64_tr_b16 v[184:185], v216 offset:40960\n"
    "ds_read_b64_tr_b16 v[186:187], v216 offset:41472\n"
    "v_mul_f32 v203, v203, v19\n"
    "v_exp_f32 v200, v200\n"
    "v_mfma_f32_32x32x16_bf16 v[64:79], v[192:195], a[124:127], v[64:79]\n"
    "ds_read_b64_tr_b16 v[188:189], v219 offset:40960\n"
    "ds_read_b64_tr_b16 v[190:191], v219 offset:41472\n"
    "buffer_load_dwordx4 v242, s[40:43], s84 offen lds\n"
    "v_exp_f32 v201, v201\n"
    "v_mfma_f32_32x32x16_bf16 v[80:95], v[196:199], a[124:127], v[80:95]\n"
    "ds_read_b64_tr_b16 v[192:193], v216 offset:47104\n"
    "ds_read_b64_tr_b16 v[194:195], v216 offset:47616\n"
    "v_exp_f32 v202, v202\n"
    "s_waitcnt lgkmcnt(6)\n"
    "v_mfma_f32_32x32x16_bf16 v[64:79], v[168:171], a[128:131], v[64:79]\n"
    "s_waitcnt vmcnt(11)\n"
    "s_barrier\n"
    "v_mfma_f32_32x32x16_bf16 v[80:95], v[172:175], a[128:131], v[80:95]\n"
    "ds_read_b64_tr_b16 v[196:197], v219 offset:47104\n"
    "ds_read_b64_tr_b16 v[198:199], v219 offset:47616\n"
    "ds_read_b64_tr_b16 v[168:169], v217 offset:8192\n"
    "ds_read_b64_tr_b16 v[170:171], v217 offset:8704\n"
    "buffer_load_dwordx4 v225, s[40:43], s84 offen offset:1024 lds\n"
    "v_exp_f32 v203, v203\n"
    "v_add_f32 v200, 1.0, v200\n"
    "v_add_f32 v201, 1.0, v201\n"
    "v_add_f32 v202, 1.0, v202\n"
    "v_add_f32 v203, 1.0, v203\n"
    "v_mfma_f32_32x32x16_bf16 v[64:79], v[176:179], a[132:135], v[64:79]\n"
    "ds_read_b64_tr_b16 v[172:173], v220 offset:8192\n"
    "ds_read_b64_tr_b16 v[174:175], v220 offset:8704\n"
    "s_add_u32 m0, s65, 0\n"
    "s_add_u32 s84, s57, 4224\n"
    "v_rcp_f32 v200, v200\n"
    "v_mfma_f32_32x32x16_bf16 v[80:95], v[180:183], a[132:135], v[80:95]\n"
    "ds_read_b64_tr_b16 v[176:177], v217 offset:14336\n"
    "ds_read_b64_tr_b16 v[178:179], v217 offset:14848\n"
    "v_rcp_f32 v201, v201\n"
    "s_waitcnt lgkmcnt(6)\n"
    "v_mfma_f32_32x32x16_bf16 v[64:79], v[184:187], a[136:139], v[64:79]\n"
    "ds_read_b64_tr_b16 v[180:181], v220 offset:14336\n"
    "ds_read_b64_tr_b16 v[182:183], v220 offset:14848\n"
    "buffer_load_dwordx4 v242, s[44:47], s84 offen lds\n"
    "v_rcp_f32 v202, v202\n"
    "v_rcp_f32 v203, v203\n"
    "v_mfma_f32_32x32x16_bf16 v[80:95], v[188:191], a[136:139], v[80:95]\n"
    "ds_read_b64_tr_b16 v[184:185], v217 offset:20480\n"
    "ds_read_b64_tr_b16 v[186:187], v217 offset:20992\n"
    "v_mul_f32 v16, v16, v200\n"
    "v_mul_f32 v17, v17, v201\n"
    "v_mul_f32 v18, v18, v202\n"
    "v_mul_f32 v19, v19, v203\n"
    "v_mfma_f32_32x32x16_bf16 v[64:79], v[192:195], a[140:143], v[64:79]\n"
    "ds_read_b64_tr_b16 v[188:189], v220 offset:20480\n"
    "ds_read_b64_tr_b16 v[190:191], v220 offset:20992\n"
    "v_mul_f32 v204, v20, v20\n"
    "v_mul_f32 v205, v21, v21\n"
    "v_mul_f32 v206, v22, v22\n"
    "v_mul_f32 v207, v23, v23\n"
    "v_mfma_f32_32x32x16_bf16 v[80:95], v[196:199], a[140:143], v[80:95]\n"
    "ds_read_b64_tr_b16 v[192:193], v217 offset:26624\n"
    "ds_read_b64_tr_b16 v[194:195], v217 offset:27136\n"
    "buffer_load_dwordx4 v225, s[44:47], s84 offen offset:1024 lds\n"
    "s_add_u32 s84, s57, 592000\n"
    "s_waitcnt lgkmcnt(6)\n"
    "v_mfma_f32_32x32x16_bf16 v[32:47], v[168:171], a[0:3], 0\n"
    "ds_read_b64_tr_b16 v[196:197], v220 offset:26624\n"
    "ds_read_b64_tr_b16 v[198:199], v220 offset:27136\n"
    "s_add_u32 s86, s58, 4096\n"
    "buffer_load_dword v255, v251, s[44:47], s86 offen\n"
    "v_fmaak_f32 v204, v204, v231, 0xc0135761\n"
    "v_fmaak_f32 v205, v205, v231, 0xc0135761\n"
    "v_mfma_f32_32x32x16_bf16 v[48:63], v[172:175], a[0:3], 0\n"
    "ds_read_b64_tr_b16 v[168:169], v217 offset:10240\n"
    "ds_read_b64_tr_b16 v[170:171], v217 offset:10752\n"
    "v_fmaak_f32 v206, v206, v231, 0xc0135761\n"
    "v_fmaak_f32 v207, v207, v231, 0xc0135761\n"
    "v_mul_f32 v204, v204, v20\n"
    "v_mul_f32 v205, v205, v21\n"
    "v_mfma_f32_32x32x16_bf16 v[32:47], v[176:179], a[4:7], v[32:47]\n"
    "ds_read_b64_tr_b16 v[172:173], v220 offset:10240\n"
    "ds_read_b64_tr_b16 v[174:175], v220 offset:10752\n"
    "buffer_load_dwordx4 v242, s[44:47], s84 offen offset:2048 lds\n"
    "v_mul_f32 v206, v206, v22\n"
    "v_mfma_f32_32x32x16_bf16 v[48:63], v[180:183], a[4:7], v[48:63]\n"
    "ds_read_b64_tr_b16 v[176:177], v217 offset:16384\n"
    "ds_read_b64_tr_b16 v[178:179], v217 offset:16896\n"
    "v_mul_f32 v207, v207, v23\n"
    "v_exp_f32 v204, v204\n"
    "s_waitcnt lgkmcnt(6)\n"
    "v_mfma_f32_32x32x16_bf16 v[32:47], v[184:187], a[8:11], v[32:47]\n"
    "ds_read_b64_tr_b16 v[180:181], v220 offset:16384\n"
    "ds_read_b64_tr_b16 v[182:183], v220 offset:16896\n"
    "v_exp_f32 v205, v205\n"
    "v_exp_f32 v206, v206\n"
    "v_mfma_f32_32x32x16_bf16 v[48:63], v[188:191], a[8:11], v[48:63]\n"
    "ds_read_b64_tr_b16 v[184:185], v217 offset:22528\n"
    "ds_read_b64_tr_b16 v[186:187], v217 offset:23040\n"
    "buffer_load_dwordx4 v225, s[44:47], s84 offen offset:3072 lds\n"
    "s_add_u32 m0, s65, 4096\n"
    "s_add_u32 s84, s57, 1183872\n"
    "v_mfma_f32_32x32x16_bf16 v[32:47], v[192:195], a[12:15], v[32:47]\n"
    "ds_read_b64_tr_b16 v[188:189], v220 offset:22528\n"
    "ds_read_b64_tr_b16 v[190:191], v220 offset:23040\n"
    "v_exp_f32 v207, v207\n"
    "v_add_f32 v204, 1.0, v204\n"
    "v_add_f32 v205, 1.0, v205\n"
    "v_mfma_f32_32x32x16_bf16 v[48:63], v[196:199], a[12:15], v[48:63]\n"
    "ds_read_b64_tr_b16 v[192:193], v217 offset:28672\n"
    "ds_read_b64_tr_b16 v[194:195], v217 offset:29184\n"
    "v_add_f32 v206, 1.0, v206\n"
    "v_add_f32 v207, 1.0, v207\n"
    "s_waitcnt lgkmcnt(6)\n"
    "v_mfma_f32_32x32x16_bf16 v[32:47], v[168:171], a[16:19], v[32:47]\n"
    "ds_read_b64_tr_b16 v[196:197], v220 offset:28672\n"
    "ds_read_b64_tr_b16 v[198:199], v220 offset:29184\n"
    "buffer_load_dwordx4 v242, s[44:47], s84 offen lds\n"
    "v_rcp_f32 v204, v204\n"
    "v_mfma_f32_32x32x16_bf16 v[48:63], v[172:175], a[16:19], v[48:63]\n"
    "ds_read_b64_tr_b16 v[168:169], v217 offset:12288\n"
    "ds_read_b64_tr_b16 v[170:171], v217 offset:12800\n"
    "v_rcp_f32 v205, v205\n"
    "v_rcp_f32 v206, v206\n"
    "v_mfma_f32_32x32x16_bf16 v[32:47], v[176:179], a[20:23], v[32:47]\n"
    "ds_read_b64_tr_b16 v[172:173], v220 offset:12288\n"
    "ds_read_b64_tr_b16 v[174:175], v220 offset:12800\n"
    "v_rcp_f32 v207, v207\n"
    "v_mul_f32 v20, v20, v204\n"
    "v_mul_f32 v21, v21, v205\n"
    "v_mfma_f32_32x32x16_bf16 v[48:63], v[180:183], a[20:23], v[48:63]\n"
    "ds_read_b64_tr_b16 v[176:177], v217 offset:18432\n"
    "ds_read_b64_tr_b16 v[178:179], v217 offset:18944\n"
    "buffer_load_dwordx4 v225, s[44:47], s84 offen offset:1024 lds\n"
    "s_add_u32 m0, s65, 139264\n"
    "s_waitcnt lgkmcnt(6)\n"
    "v_mfma_f32_32x32x16_bf16 v[32:47], v[184:187], a[24:27], v[32:47]\n"
    "ds_read_b64_tr_b16 v[180:181], v220 offset:18432\n"
    "ds_read_b64_tr_b16 v[182:183], v220 offset:18944\n"
    "s_add_u32 s84, s57, 1773696\n"
    "v_mul_f32 v22, v22, v206\n"
    "v_mul_f32 v23, v23, v207\n"
    "v_mul_f32 v200, v24, v24\n"
    "v_mfma_f32_32x32x16_bf16 v[48:63], v[188:191], a[24:27], v[48:63]\n"
    "ds_read_b64_tr_b16 v[184:185], v217 offset:24576\n"
    "ds_read_b64_tr_b16 v[186:187], v217 offset:25088\n"
    "v_mul_f32 v201, v25, v25\n"
    "v_mul_f32 v202, v26, v26\n"
    "v_mul_f32 v203, v27, v27\n"
    "v_fmaak_f32 v200, v200, v231, 0xc0135761\n"
    "v_mfma_f32_32x32x16_bf16 v[32:47], v[192:195], a[28:31], v[32:47]\n"
    "ds_read_b64_tr_b16 v[188:189], v220 offset:24576\n"
    "ds_read_b64_tr_b16 v[190:191], v220 offset:25088\n"
    "buffer_load_dwordx4 v242, s[44:47], s84 offen lds\n"
    "v_fmaak_f32 v201, v201, v231, 0xc0135761\n"
    "v_mfma_f32_32x32x16_bf16 v[48:63], v[196:199], a[28:31], v[48:63]\n"
    "ds_read_b64_tr_b16 v[192:193], v217 offset:30720\n"
    "ds_read_b64_tr_b16 v[194:195], v217 offset:31232\n"
    "v_fmaak_f32 v202, v202, v231, 0xc0135761\n"
    "v_fmaak_f32 v203, v203, v231, 0xc0135761\n"
    "v_mul_f32 v200, v200, v24\n"
    "s_waitcnt lgkmcnt(6)\n"
    "v_mfma_f32_32x32x16_bf16 v[32:47], v[168:171], a[32:35], v[32:47]\n"
    "s_waitcnt vmcnt(14)\n"
    "s_barrier\n"
    "v_mfma_f32_32x32x16_bf16 v[48:63], v[172:175], a[32:35], v[48:63]\n"
    "ds_read_b64_tr_b16 v[196:197], v220 offset:30720\n"
    "ds_read_b64_tr_b16 v[198:199], v220 offset:31232\n"
    "ds_read_b64_tr_b16 v[168:169], v217 offset:32768\n"
    "ds_read_b64_tr_b16 v[170:171], v217 offset:33280\n"
    "buffer_load_dwordx4 v225, s[44:47], s84 offen offset:1024 lds\n"
    "v_mul_f32 v201, v201, v25\n"
    "v_mul_f32 v202, v202, v26\n"
    "v_mul_f32 v203, v203, v27\n"
    "v_exp_f32 v200, v200\n"
    "v_mfma_f32_32x32x16_bf16 v[32:47], v[176:179], a[36:39], v[32:47]\n"
    "ds_read_b64_tr_b16 v[172:173], v220 offset:32768\n"
    "ds_read_b64_tr_b16 v[174:175], v220 offset:33280\n"
    "s_add_u32 s84, s57, 2361472\n"
    "v_exp_f32 v201, v201\n"
    "v_mfma_f32_32x32x16_bf16 v[48:63], v[180:183], a[36:39], v[48:63]\n"
    "ds_read_b64_tr_b16 v[176:177], v217 offset:38912\n"
    "ds_read_b64_tr_b16 v[178:179], v217 offset:39424\n"
    "v_exp_f32 v202, v202\n"
    "s_waitcnt lgkmcnt(6)\n"
    "v_mfma_f32_32x32x16_bf16 v[32:47], v[184:187], a[40:43], v[32:47]\n"
    "ds_read_b64_tr_b16 v[180:181], v220 offset:38912\n"
    "ds_read_b64_tr_b16 v[182:183], v220 offset:39424\n"
    "buffer_load_dwordx4 v242, s[44:47], s84 offen offset:2048 lds\n"
    "v_exp_f32 v203, v203\n"
    "v_add_f32 v200, 1.0, v200\n"
    "v_mfma_f32_32x32x16_bf16 v[48:63], v[188:191], a[40:43], v[48:63]\n"
    "ds_read_b64_tr_b16 v[184:185], v217 offset:45056\n"
    "ds_read_b64_tr_b16 v[186:187], v217 offset:45568\n"
    "v_add_f32 v201, 1.0, v201\n"
    "v_add_f32 v202, 1.0, v202\n"
    "v_add_f32 v203, 1.0, v203\n"
    "v_mfma_f32_32x32x16_bf16 v[32:47], v[192:195], a[44:47], v[32:47]\n"
    "ds_read_b64_tr_b16 v[188:189], v220 offset:45056\n"
    "ds_read_b64_tr_b16 v[190:191], v220 offset:45568\n"
    "v_rcp_f32 v200, v200\n"
    "v_rcp_f32 v201, v201\n"
    "v_mfma_f32_32x32x16_bf16 v[48:63], v[196:199], a[44:47], v[48:63]\n"
    "ds_read_b64_tr_b16 v[192:193], v217 offset:51200\n"
    "ds_read_b64_tr_b16 v[194:195], v217 offset:51712\n"
    "buffer_load_dwordx4 v225, s[44:47], s84 offen offset:3072 lds\n"
    "s_add_u32 m0, s65, 143360\n"
    "s_waitcnt lgkmcnt(6)\n"
    "v_mfma_f32_32x32x16_bf16 v[32:47], v[168:171], a[48:51], v[32:47]\n"
    "ds_read_b64_tr_b16 v[196:197], v220 offset:51200\n"
    "ds_read_b64_tr_b16 v[198:199], v220 offset:51712\n"
    "s_add_u32 s84, s57, 2953344\n"
    "v_rcp_f32 v202, v202\n"
    "v_mfma_f32_32x32x16_bf16 v[48:63], v[172:175], a[48:51], v[48:63]\n"
    "ds_read_b64_tr_b16 v[168:169], v217 offset:34816\n"
    "ds_read_b64_tr_b16 v[170:171], v217 offset:35328\n"
    "v_rcp_f32 v203, v203\n"
    "v_mul_f32 v24, v24, v200\n"
    "v_mul_f32 v25, v25, v201\n"
    "v_mfma_f32_32x32x16_bf16 v[32:47], v[176:179], a[52:55], v[32:47]\n"
    "ds_read_b64_tr_b16 v[172:173], v220 offset:34816\n"
    "ds_read_b64_tr_b16 v[174:175], v220 offset:35328\n"
    "buffer_load_dwordx4 v242, s[44:47], s84 offen lds\n"
    "v_mul_f32 v26, v26, v202\n"
    "v_mul_f32 v27, v27, v203\n"
    "v_mfma_f32_32x32x16_bf16 v[48:63], v[180:183], a[52:55], v[48:63]\n"
    "ds_read_b64_tr_b16 v[176:177], v217 offset:40960\n"
    "ds_read_b64_tr_b16 v[178:179], v217 offset:41472\n"
    "v_mul_f32 v204, v28, v28\n"
    "v_mul_f32 v205, v29, v29\n"
    "v_mul_f32 v206, v30, v30\n"
    "s_waitcnt lgkmcnt(6)\n"
    "v_mfma_f32_32x32x16_bf16 v[32:47], v[184:187], a[56:59], v[32:47]\n"
    "ds_read_b64_tr_b16 v[180:181], v220 offset:40960\n"
    "ds_read_b64_tr_b16 v[182:183], v220 offset:41472\n"
    "v_mul_f32 v207, v31, v31\n"
    "v_fmaak_f32 v204, v204, v231, 0xc0135761\n"
    "v_fmaak_f32 v205, v205, v231, 0xc0135761\n"
    "v_fmaak_f32 v206, v206, v231, 0xc0135761\n"
    "v_mfma_f32_32x32x16_bf16 v[48:63], v[188:191], a[56:59], v[48:63]\n"
    "ds_read_b64_tr_b16 v[184:185], v217 offset:47104\n"
    "ds_read_b64_tr_b16 v[186:187], v217 offset:47616\n"
    "buffer_load_dwordx4 v225, s[44:47], s84 offen offset:1024 lds\n"
    "s_add_u32 m0, s65, 24576\n"
    "s_add_u32 s84, s57, 3543168\n"
    "v_fmaak_f32 v207, v207, v231, 0xc0135761\n"
    "v_mfma_f32_32x32x16_bf16 v[32:47], v[192:195], a[60:63], v[32:47]\n"
    "ds_read_b64_tr_b16 v[188:189], v220 offset:47104\n"
    "ds_read_b64_tr_b16 v[190:191], v220 offset:47616\n"
    "v_mul_f32 v204, v204, v28\n"
    "v_mul_f32 v205, v205, v29\n"
    "v_mul_f32 v206, v206, v30\n"
    "v_mul_f32 v207, v207, v31\n"
    "v_mfma_f32_32x32x16_bf16 v[48:63], v[196:199], a[60:63], v[48:63]\n"
    "ds_read_b64_tr_b16 v[192:193], v217 offset:53248\n"
    "ds_read_b64_tr_b16 v[194:195], v217 offset:53760\n"
    "v_exp_f32 v204, v204\n"
    "s_waitcnt lgkmcnt(6)\n"
    "v_mfma_f32_32x32x16_bf16 v[32:47], v[168:171], a[64:67], v[32:47]\n"
    "ds_read_b64_tr_b16 v[196:197], v220 offset:53248\n"
    "ds_read_b64_tr_b16 v[198:199], v220 offset:53760\n"
    "buffer_load_dwordx4 v242, s[44:47], s84 offen lds\n"
    "v_exp_f32 v205, v205\n"
    "v_mfma_f32_32x32x16_bf16 v[48:63], v[172:175], a[64:67], v[48:63]\n"
    "ds_read_b64_tr_b16 v[168:169], v217 offset:36864\n"
    "ds_read_b64_tr_b16 v[170:171], v217 offset:37376\n"
    "v_exp_f32 v206, v206\n"
    "v_exp_f32 v207, v207\n"
    "v_mfma_f32_32x32x16_bf16 v[32:47], v[176:179], a[68:71], v[32:47]\n"
    "ds_read_b64_tr_b16 v[172:173], v220 offset:36864\n"
    "ds_read_b64_tr_b16 v[174:175], v220 offset:37376\n"
    "v_add_f32 v204, 1.0, v204\n"
    "v_add_f32 v205, 1.0, v205\n"
    "v_add_f32 v206, 1.0, v206\n"
    "v_add_f32 v207, 1.0, v207\n"
    "v_mfma_f32_32x32x16_bf16 v[48:63], v[180:183], a[68:71], v[48:63]\n"
    "ds_read_b64_tr_b16 v[176:177], v217 offset:43008\n"
    "ds_read_b64_tr_b16 v[178:179], v217 offset:43520\n"
    "buffer_load_dwordx4 v225, s[44:47], s84 offen offset:1024 lds\n"
    "s_add_u32 s84, s57, 4130944\n"
    "s_waitcnt lgkmcnt(6)\n"
    "v_mfma_f32_32x32x16_bf16 v[32:47], v[184:187], a[72:75], v[32:47]\n"
    "ds_read_b64_tr_b16 v[180:181], v220 offset:43008\n"
    "ds_read_b64_tr_b16 v[182:183], v220 offset:43520\n"
    "v_rcp_f32 v204, v204\n"
    "v_rcp_f32 v205, v205\n"
    "v_mfma_f32_32x32x16_bf16 v[48:63], v[188:191], a[72:75], v[48:63]\n"
    "ds_read_b64_tr_b16 v[184:185], v217 offset:49152\n"
    "ds_read_b64_tr_b16 v[186:187], v217 offset:49664\n"
    "v_rcp_f32 v206, v206\n"
    "v_rcp_f32 v207, v207\n"
    "v_mfma_f32_32x32x16_bf16 v[32:47], v[192:195], a[76:79], v[32:47]\n"
    "ds_read_b64_tr_b16 v[188:189], v220 offset:49152\n"
    "ds_read_b64_tr_b16 v[190:191], v220 offset:49664\n"
    "buffer_load_dwordx4 v242, s[44:47], s84 offen offset:2048 lds\n"
    "v_mul_f32 v28, v28, v204\n"
    "v_mfma_f32_32x32x16_bf16 v[48:63], v[196:199], a[76:79], v[48:63]\n"
    "ds_read_b64_tr_b16 v[192:193], v217 offset:55296\n"
    "ds_read_b64_tr_b16 v[194:195], v217 offset:55808\n"
    "v_mul_f32 v29, v29, v205\n"
    "v_mul_f32 v30, v30, v206\n"
    "v_mul_f32 v31, v31, v207\n"
    "s_waitcnt lgkmcnt(6)\n"
    "v_mfma_f32_32x32x16_bf16 v[32:47], v[168:171], a[80:83], v[32:47]\n"
    "s_waitcnt vmcnt(16)\n"
    "s_barrier\n"
    "v_mfma_f32_32x32x16_bf16 v[48:63], v[172:175], a[80:83], v[48:63]\n"
    "ds_read_b64_tr_b16 v[196:197], v220 offset:55296\n"
    "ds_read_b64_tr_b16 v[198:199], v220 offset:55808\n"
    "ds_read_b64_tr_b16 v[168:169], v216 offset:49152\n"
    "ds_read_b64_tr_b16 v[170:171], v216 offset:49664\n"
    "buffer_load_dwordx4 v225, s[44:47], s84 offen offset:3072 lds\n"
    "v_mul_f32 v0, v0, v64\n"
    "v_mfma_f32_32x32x16_bf16 v[32:47], v[176:179], a[84:87], v[32:47]\n"
    "ds_read_b64_tr_b16 v[172:173], v219 offset:49152\n"
    "ds_read_b64_tr_b16 v[174:175], v219 offset:49664\n"
    "s_add_u32 m0, s65, 28672\n"
    "s_add_u32 s84, s57, 4722816\n"
    "v_mul_f32 v1, v1, v65\n"
    "v_mul_f32 v2, v2, v66\n"
    "v_mul_f32 v3, v3, v67\n"
    "v_mfma_f32_32x32x16_bf16 v[48:63], v[180:183], a[84:87], v[48:63]\n"
    "ds_read_b64_tr_b16 v[176:177], v216 offset:55296\n"
    "ds_read_b64_tr_b16 v[178:179], v216 offset:55808\n"
    "v_mul_f32 v4, v4, v68\n"
    "v_mul_f32 v5, v5, v69\n"
    "v_mul_f32 v6, v6, v70\n"
    "s_waitcnt lgkmcnt(6)\n"
    "v_mfma_f32_32x32x16_bf16 v[32:47], v[184:187], a[88:91], v[32:47]\n"
    "ds_read_b64_tr_b16 v[180:181], v219 offset:55296\n"
    "ds_read_b64_tr_b16 v[182:183], v219 offset:55808\n"
    "buffer_load_dwordx4 v242, s[44:47], s84 offen lds\n"
    "v_mul_f32 v7, v7, v71\n"
    "v_cvt_pk_bf16_f32 v96, v0, v1\n"
    "v_mfma_f32_32x32x16_bf16 v[48:63], v[188:191], a[88:91], v[48:63]\n"
    "ds_read_b64_tr_b16 v[184:185], v216 offset:61440\n"
    "ds_read_b64_tr_b16 v[186:187], v216 offset:61952\n"
    "v_cvt_pk_bf16_f32 v97, v2, v3\n"
    "v_cvt_pk_bf16_f32 v98, v4, v5\n"
    "v_cvt_pk_bf16_f32 v99, v6, v7\n"
    "v_mfma_f32_32x32x16_bf16 v[32:47], v[192:195], a[92:95], v[32:47]\n"
    "ds_read_b64_tr_b16 v[188:189], v219 offset:61440\n"
    "ds_read_b64_tr_b16 v[190:191], v219 offset:61952\n"
    "s_add_u32 s85, s67, 0\n"
    "buffer_store_dwordx4 v[96:99], v250, s[52:55], s85 offen sc1\n"
    "v_mfma_f32_32x32x16_bf16 v[48:63], v[196:199], a[92:95], v[48:63]\n"
    "ds_read_b64_tr_b16 v[192:193], v217 offset:2048\n"
    "ds_read_b64_tr_b16 v[194:195], v217 offset:2560\n"
    "buffer_load_dwordx4 v225, s[44:47], s84 offen offset:1024 lds\n"
    "s_add_u32 m0, s65, 73728\n"
    "s_waitcnt lgkmcnt(6)\n"
    "v_mfma_f32_32x32x16_bf16 v[32:47], v[168:171], a[96:99], v[32:47]\n"
    "ds_read_b64_tr_b16 v[196:197], v220 offset:2048\n"
    "ds_read_b64_tr_b16 v[198:199], v220 offset:2560\n"
    "s_add_u32 s84, s58, 3072\n"
    "v_mul_f32 v8, v8, v72\n"
    "v_mul_f32 v9, v9, v73\n"
    "v_mul_f32 v10, v10, v74\n"
    "v_mfma_f32_32x32x16_bf16 v[48:63], v[172:175], a[96:99], v[48:63]\n"
    "ds_read_b64_tr_b16 v[168:169], v216 offset:51200\n"
    "ds_read_b64_tr_b16 v[170:171], v216 offset:51712\n"
    "v_mul_f32 v11, v11, v75\n"
    "v_mul_f32 v12, v12, v76\n"
    "v_mul_f32 v13, v13, v77\n"
    "v_mul_f32 v14, v14, v78\n"
    "v_mfma_f32_32x32x16_bf16 v[32:47], v[176:179], a[100:103], v[32:47]\n"
    "ds_read_b64_tr_b16 v[172:173], v219 offset:51200\n"
    "ds_read_b64_tr_b16 v[174:175], v219 offset:51712\n"
    "buffer_load_dwordx4 v244, s[40:43], s84 offen lds\n"
    "v_mul_f32 v15, v15, v79\n"
    "v_mfma_f32_32x32x16_bf16 v[48:63], v[180:183], a[100:103], v[48:63]\n"
    "ds_read_b64_tr_b16 v[176:177], v216 offset:57344\n"
    "ds_read_b64_tr_b16 v[178:179], v216 offset:57856\n"
    "v_cvt_pk_bf16_f32 v100, v8, v9\n"
    "v_cvt_pk_bf16_f32 v101, v10, v11\n"
    "v_cvt_pk_bf16_f32 v102, v12, v13\n"
    "s_waitcnt lgkmcnt(6)\n"
    "v_mfma_f32_32x32x16_bf16 v[32:47], v[184:187], a[104:107], v[32:47]\n"
    "ds_read_b64_tr_b16 v[180:181], v219 offset:57344\n"
    "ds_read_b64_tr_b16 v[182:183], v219 offset:57856\n"
    "v_cvt_pk_bf16_f32 v103, v14, v15\n"
    "v_mfma_f32_32x32x16_bf16 v[48:63], v[188:191], a[104:107], v[48:63]\n"
    "ds_read_b64_tr_b16 v[184:185], v216 offset:63488\n"
    "ds_read_b64_tr_b16 v[186:187], v216 offset:64000\n"
    "buffer_load_dwordx4 v227, s[44:47], s84 offen offset:1024 lds\n"
    "s_add_u32 s84, s58, 590848\n"
    "v_mfma_f32_32x32x16_bf16 v[32:47], v[192:195], a[108:111], v[32:47]\n"
    "ds_read_b64_tr_b16 v[188:189], v219 offset:63488\n"
    "ds_read_b64_tr_b16 v[190:191], v219 offset:64000\n"
    "s_add_u32 s85, s67, 1024\n"
    "buffer_store_dwordx4 v[100:103], v250, s[52:55], s85 offen sc1\n"
    "v_mul_f32 v16, v16, v80\n"
    "v_mul_f32 v17, v17, v81\n"
    "v_mfma_f32_32x32x16_bf16 v[48:63], v[196:199], a[108:111], v[48:63]\n"
    "ds_read_b64_tr_b16 v[192:193], v217 offset:4096\n"
    "ds_read_b64_tr_b16 v[194:195], v217 offset:4608\n"
    "v_mul_f32 v18, v18, v82\n"
    "v_mul_f32 v19, v19, v83\n"
    "v_mul_f32 v20, v20, v84\n"
    "s_waitcnt lgkmcnt(6)\n"
    "v_mfma_f32_32x32x16_bf16 v[32:47], v[168:171], a[112:115], v[32:47]\n"
    "ds_read_b64_tr_b16 v[196:197], v220 offset:4096\n"
    "ds_read_b64_tr_b16 v[198:199], v220 offset:4608\n"
    "buffer_load_dwordx4 v244, s[40:43], s84 offen offset:2048 lds\n"
    "v_mul_f32 v21, v21, v85\n"
    "v_mfma_f32_32x32x16_bf16 v[48:63], v[172:175], a[112:115], v[48:63]\n"
    "ds_read_b64_tr_b16 v[168:169], v216 offset:53248\n"
    "ds_read_b64_tr_b16 v[170:171], v216 offset:53760\n"
    "buffer_load_dwordx4 v227, s[44:47], s84 offen offset:3072 lds\n"
    "s_add_u32 m0, s65, 77824\n"
    "s_add_u32 s84, s58, 1182720\n"
    "v_mul_f32 v22, v22, v86\n"
    "v_mul_f32 v23, v23, v87\n"
    "v_cvt_pk_bf16_f32 v104, v16, v17\n"
    "v_mfma_f32_32x32x16_bf16 v[32:47], v[176:179], a[116:119], v[32:47]\n"
    "ds_read_b64_tr_b16 v[172:173], v219 offset:53248\n"
    "ds_read_b64_tr_b16 v[174:175], v219 offset:53760\n"
    "buffer_load_dwordx4 v244, s[40:43], s84 offen lds\n"
    "v_cvt_pk_bf16_f32 v105, v18, v19\n"
    "v_cvt_pk_bf16_f32 v106, v20, v21\n"
    "v_mfma_f32_32x32x16_bf16 v[48:63], v[180:183], a[116:119], v[48:63]\n"
    "ds_read_b64_tr_b16 v[176:177], v216 offset:59392\n"
    "ds_read_b64_tr_b16 v[178:179], v216 offset:59904\n"
    "buffer_load_dwordx4 v227, s[44:47], s84 offen offset:1024 lds\n"
    "v_cvt_pk_bf16_f32 v107, v22, v23\n"
    "s_waitcnt lgkmcnt(6)\n"
    "v_mfma_f32_32x32x16_bf16 v[32:47], v[184:187], a[120:123], v[32:47]\n"
    "ds_read_b64_tr_b16 v[180:181], v219 offset:59392\n"
    "ds_read_b64_tr_b16 v[182:183], v219 offset:59904\n"
    "s_add_u32 s85, s67, 2048\n"
    "buffer_store_dwordx4 v[104:107], v250, s[52:55], s85 offen sc1\n"
    "v_mfma_f32_32x32x16_bf16 v[48:63], v[188:191], a[120:123], v[48:63]\n"
    "ds_read_b64_tr_b16 v[184:185], v217 offset:0\n"
    "ds_read_b64_tr_b16 v[186:187], v217 offset:512\n"
    "v_mul_f32 v24, v24, v88\n"
    "v_mul_f32 v25, v25, v89\n"
    "v_mul_f32 v26, v26, v90\n"
    "v_mul_f32 v27, v27, v91\n"
    "v_mfma_f32_32x32x16_bf16 v[32:47], v[192:195], a[124:127], v[32:47]\n"
    "ds_read_b64_tr_b16 v[188:189], v220 offset:0\n"
    "ds_read_b64_tr_b16 v[190:191], v220 offset:512\n"
    "v_mul_f32 v28, v28, v92\n"
    "v_mul_f32 v29, v29, v93\n"
    "v_mul_f32 v30, v30, v94\n"
    "v_mul_f32 v31, v31, v95\n"
    "v_mfma_f32_32x32x16_bf16 v[48:63], v[196:199], a[124:127], v[48:63]\n"
    "ds_read_b64_tr_b16 v[192:193], v217 offset:6144\n"
    "ds_read_b64_tr_b16 v[194:195], v217 offset:6656\n"
    "v_cvt_pk_bf16_f32 v108, v24, v25\n"
    "v_cvt_pk_bf16_f32 v109, v26, v27\n"
    "v_cvt_pk_bf16_f32 v110, v28, v29\n"
    "s_waitcnt lgkmcnt(6)\n"
    "v_mfma_f32_32x32x16_bf16 v[32:47], v[168:171], a[128:131], v[32:47]\n"
    "s_waitcnt vmcnt(21)\n"
    "s_barrier\n"
    "v_mfma_f32_32x32x16_bf16 v[48:63], v[172:175], a[128:131], v[48:63]\n"
    "ds_read_b64_tr_b16 v[196:197], v220 offset:6144\n"
    "ds_read_b64_tr_b16 v[198:199], v220 offset:6656\n"
    "ds_read_b64_tr_b16 v[168:169], v216 offset:0\n"
    "ds_read_b64_tr_b16 v[170:171], v216 offset:512\n"
    "s_add_u32 m0, s65, 98304\n"
    "s_add_u32 s84, s58, 1772544\n"
    "v_mfma_f32_32x32x16_bf16 v[32:47], v[176:179], a[132:135], v[32:47]\n"
    "ds_read_b64_tr_b16 v[172:173], v219 offset:0\n"
    "ds_read_b64_tr_b16 v[174:175], v219 offset:512\n"
    "buffer_load_dwordx4 v244, s[40:43], s84 offen lds\n"
    "v_cvt_pk_bf16_f32 v111, v30, v31\n"
    "v_mfma_f32_32x32x16_bf16 v[48:63], v[180:183], a[132:135], v[48:63]\n"
    "ds_read_b64_tr_b16 v[176:177], v216 offset:6144\n"
    "ds_read_b64_tr_b16 v[178:179], v216 offset:6656\n"
    "buffer_load_dwordx4 v227, s[44:47], s84 offen offset:1024 lds\n"
    "s_add_u32 s84, s58, 2360320\n"
    "s_waitcnt lgkmcnt(6)\n"
    "v_mfma_f32_32x32x16_bf16 v[32:47], v[184:187], a[136:139], v[32:47]\n"
    "ds_read_b64_tr_b16 v[180:181], v219 offset:6144\n"
    "ds_read_b64_tr_b16 v[182:183], v219 offset:6656\n"
    "buffer_load_dwordx4 v244, s[40:43], s84 offen offset:2048 lds\n"
    "v_mfma_f32_32x32x16_bf16 v[48:63], v[188:191], a[136:139], v[48:63]\n"
    "ds_read_b64_tr_b16 v[184:185], v216 offset:12288\n"
    "ds_read_b64_tr_b16 v[186:187], v216 offset:12800\n"
    "buffer_load_dwordx4 v227, s[44:47], s84 offen offset:3072 lds\n"
    "s_add_u32 m0, s65, 102400\n"
    "s_add_u32 s84, s58, 2952192\n"
    "v_mfma_f32_32x32x16_bf16 v[32:47], v[192:195], a[140:143], v[32:47]\n"
    "ds_read_b64_tr_b16 v[188:189], v219 offset:12288\n"
    "ds_read_b64_tr_b16 v[190:191], v219 offset:12800\n"
    "buffer_load_dwordx4 v244, s[40:43], s84 offen lds\n"
    "v_mfma_f32_32x32x16_bf16 v[48:63], v[196:199], a[140:143], v[48:63]\n"
    "ds_read_b64_tr_b16 v[192:193], v216 offset:18432\n"
    "ds_read_b64_tr_b16 v[194:195], v216 offset:18944\n"
    "buffer_load_dwordx4 v227, s[44:47], s84 offen offset:1024 lds\n"
    "s_add_u32 s85, s67, 3072\n"
    "buffer_store_dwordx4 v[108:111], v250, s[52:55], s85 offen sc1\n"
    "s_nop 1\n"
    "s_waitcnt lgkmcnt(6)\n"
    "v_mfma_f32_32x32x16_bf16 v[64:79], v[168:171], a[0:3], 0\n"
    "ds_read_b64_tr_b16 v[196:197], v219 offset:18432\n"
    "ds_read_b64_tr_b16 v[198:199], v219 offset:18944\n"
    "s_add_u32 s86, s57, 4352\n"
    "buffer_load_dword v255, v251, s[40:43], s86 offen\n"
    "v_mfma_f32_32x32x16_bf16 v[80:95], v[172:175], a[0:3], 0\n"
    "ds_read_b64_tr_b16 v[168:169], v216 offset:2048\n"
    "ds_read_b64_tr_b16 v[170:171], v216 offset:2560\n"
    "v_mfma_f32_32x32x16_bf16 v[64:79], v[176:179], a[4:7], v[64:79]\n"
    "ds_read_b64_tr_b16 v[172:173], v219 offset:2048\n"
    "ds_read_b64_tr_b16 v[174:175], v219 offset:2560\n"
    "v_mul_f32 v200, v32, v32\n"
    "v_mul_f32 v201, v33, v33\n"
    "v_mul_f32 v202, v34, v34\n"
    "v_mul_f32 v203, v35, v35\n"
    "v_mfma_f32_32x32x16_bf16 v[80:95], v[180:183], a[4:7], v[80:95]\n"
    "ds_read_b64_tr_b16 v[176:177], v216 offset:8192\n"
    "ds_read_b64_tr_b16 v[178:179], v216 offset:8704\n"
    "v_fmaak_f32 v200, v200, v231, 0xc0135761\n"
    "v_fmaak_f32 v201, v201, v231, 0xc0135761\n"
    "v_fmaak_f32 v202, v202, v231, 0xc0135761\n"
    "s_waitcnt lgkmcnt(6)\n"
    "v_mfma_f32_32x32x16_bf16 v[64:79], v[184:187], a[8:11], v[64:79]\n"
    "ds_read_b64_tr_b16 v[180:181], v219 offset:8192\n"
    "ds_read_b64_tr_b16 v[182:183], v219 offset:8704\n"
    "v_fmaak_f32 v203, v203, v231, 0xc0135761\n"
    "v_mul_f32 v200, v200, v32\n"
    "v_mul_f32 v201, v201, v33\n"
    "v_mul_f32 v202, v202, v34\n"
    "v_mfma_f32_32x32x16_bf16 v[80:95], v[188:191], a[8:11], v[80:95]\n"
    "ds_read_b64_tr_b16 v[184:185], v216 offset:14336\n"
    "ds_read_b64_tr_b16 v[186:187], v216 offset:14848\n"
    "v_mul_f32 v203, v203, v35\n"
    "v_exp_f32 v200, v200\n"
    "v_mfma_f32_32x32x16_bf16 v[64:79], v[192:195], a[12:15], v[64:79]\n"
    "ds_read_b64_tr_b16 v[188:189], v219 offset:14336\n"
    "ds_read_b64_tr_b16 v[190:191], v219 offset:14848\n"
    "v_exp_f32 v201, v201\n"
    "v_exp_f32 v202, v202\n"
    "v_mfma_f32_32x32x16_bf16 v[80:95], v[196:199], a[12:15], v[80:95]\n"
    "ds_read_b64_tr_b16 v[192:193], v216 offset:20480\n"
    "ds_read_b64_tr_b16 v[194:195], v216 offset:20992\n"
    "v_exp_f32 v203, v203\n"
    "v_add_f32 v200, 1.0, v200\n"
    "s_waitcnt lgkmcnt(6)\n"
    "v_mfma_f32_32x32x16_bf16 v[64:79], v[168:171], a[16:19], v[64:79]\n"
    "ds_read_b64_tr_b16 v[196:197], v219 offset:20480\n"
    "ds_read_b64_tr_b16 v[198:199], v219 offset:20992\n"
    "v_add_f32 v201, 1.0, v201\n"
    "v_add_f32 v202, 1.0, v202\n"
    "v_add_f32 v203, 1.0, v203\n"
    "v_mfma_f32_32x32x16_bf16 v[80:95], v[172:175], a[16:19], v[80:95]\n"
    "ds_read_b64_tr_b16 v[168:169], v216 offset:4096\n"
    "ds_read_b64_tr_b16 v[170:171], v216 offset:4608\n"
    "v_rcp_f32 v200, v200\n"
    "v_rcp_f32 v201, v201\n"
    "v_mfma_f32_32x32x16_bf16 v[64:79], v[176:179], a[20:23], v[64:79]\n"
    "ds_read_b64_tr_b16 v[172:173], v219 offset:4096\n"
    "ds_read_b64_tr_b16 v[174:175], v219 offset:4608\n"
    "v_rcp_f32 v202, v202\n"
    "v_rcp_f32 v203, v203\n"
    "v_mfma_f32_32x32x16_bf16 v[80:95], v[180:183], a[20:23], v[80:95]\n"
    "ds_read_b64_tr_b16 v[176:177], v216 offset:10240\n"
    "ds_read_b64_tr_b16 v[178:179], v216 offset:10752\n"
    "v_mul_f32 v32, v32, v200\n"
    "v_mul_f32 v33, v33, v201\n"
    "v_mul_f32 v34, v34, v202\n"
    "s_waitcnt lgkmcnt(6)\n"
    "v_mfma_f32_32x32x16_bf16 v[64:79], v[184:187], a[24:27], v[64:79]\n"
    "ds_read_b64_tr_b16 v[180:181], v219 offset:10240\n"
    "ds_read_b64_tr_b16 v[182:183], v219 offset:10752\n"
    "v_mul_f32 v35, v35, v203\n"
    "v_mul_f32 v204, v36, v36\n"
    "v_mul_f32 v205, v37, v37\n"
    "v_mul_f32 v206, v38, v38\n"
    "v_mfma_f32_32x32x16_bf16 v[80:95], v[188:191], a[24:27], v[80:95]\n"
    "ds_read_b64_tr_b16 v[184:185], v216 offset:16384\n"
    "ds_read_b64_tr_b16 v[186:187], v216 offset:16896\n"
    "v_mul_f32 v207, v39, v39\n"
    "v_fmaak_f32 v204, v204, v231, 0xc0135761\n"
    "v_fmaak_f32 v205, v205, v231, 0xc0135761\n"
    "v_fmaak_f32 v206, v206, v231, 0xc0135761\n"
    "v_mfma_f32_32x32x16_bf16 v[64:79], v[192:195], a[28:31], v[64:79]\n"
    "ds_read_b64_tr_b16 v[188:189], v219 offset:16384\n"
    "ds_read_b64_tr_b16 v[190:191], v219 offset:16896\n"
    "v_fmaak_f32 v207, v207, v231, 0xc0135761\n"
    "v_mul_f32 v204, v204, v36\n"
    "v_mul_f32 v205, v205, v37\n"
    "v_mul_f32 v206, v206, v38\n"
    "v_mfma_f32_32x32x16_bf16 v[80:95], v[196:199], a[28:31], v[80:95]\n"
    "ds_read_b64_tr_b16 v[192:193], v216 offset:22528\n"
    "ds_read_b64_tr_b16 v[194:195], v216 offset:23040\n"
    "v_mul_f32 v207, v207, v39\n"
    "v_exp_f32 v204, v204\n"
    "s_waitcnt lgkmcnt(6)\n"
    "v_mfma_f32_32x32x16_bf16 v[64:79], v[168:171], a[32:35], v[64:79]\n"
    "s_waitcnt vmcnt(23)\n"
    "s_barrier\n"
    "v_mfma_f32_32x32x16_bf16 v[80:95], v[172:175], a[32:35], v[80:95]\n"
    "ds_read_b64_tr_b16 v[196:197], v219 offset:22528\n"
    "ds_read_b64_tr_b16 v[198:199], v219 offset:23040\n"
    "ds_read_b64_tr_b16 v[168:169], v218 offset:8192\n"
    "ds_read_b64_tr_b16 v[170:171], v218 offset:8704\n"
    "s_add_u32 m0, s65, 49152\n"
    "s_add_u32 s84, s58, 3542016\n"
    "v_mfma_f32_32x32x16_bf16 v[64:79], v[176:179], a[36:39], v[64:79]\n"
    "ds_read_b64_tr_b16 v[172:173], v221 offset:8192\n"
    "ds_read_b64_tr_b16 v[174:175], v221 offset:8704\n"
    "buffer_load_dwordx4 v244, s[40:43], s84 offen lds\n"
    "v_mfma_f32_32x32x16_bf16 v[80:95], v[180:183], a[36:39], v[80:95]\n"
    "ds_read_b64_tr_b16 v[176:177], v218 offset:14336\n"
    "ds_read_b64_tr_b16 v[178:179], v218 offset:14848\n"
    "buffer_load_dwordx4 v227, s[44:47], s84 offen offset:1024 lds\n"
    "s_add_u32 s84, s58, 4129792\n"
    "v_exp_f32 v205, v205\n"
    "s_waitcnt lgkmcnt(6)\n"
    "v_mfma_f32_32x32x16_bf16 v[64:79], v[184:187], a[40:43], v[64:79]\n"
    "ds_read_b64_tr_b16 v[180:181], v221 offset:14336\n"
    "ds_read_b64_tr_b16 v[182:183], v221 offset:14848\n"
    "buffer_load_dwordx4 v244, s[40:43], s84 offen offset:2048 lds\n"
    "v_exp_f32 v206, v206\n"
    "v_mfma_f32_32x32x16_bf16 v[80:95], v[188:191], a[40:43], v[80:95]\n"
    "ds_read_b64_tr_b16 v[184:185], v218 offset:20480\n"
    "ds_read_b64_tr_b16 v[186:187], v218 offset:20992\n"
    "buffer_load_dwordx4 v227, s[44:47], s84 offen offset:3072 lds\n"
    "s_add_u32 m0, s65, 53248\n"
    "s_add_u32 s84, s58, 4721664\n"
    "v_exp_f32 v207, v207\n"
    "v_add_f32 v204, 1.0, v204\n"
    "v_mfma_f32_32x32x16_bf16 v[64:79], v[192:195], a[44:47], v[64:79]\n"
    "ds_read_b64_tr_b16 v[188:189], v221 offset:20480\n"
    "ds_read_b64_tr_b16 v[190:191], v221 offset:20992\n"
    "buffer_load_dwordx4 v244, s[40:43], s84 offen lds\n"
    "v_add_f32 v205, 1.0, v205\n"
    "v_add_f32 v206, 1.0, v206\n"
    "v_add_f32 v207, 1.0, v207\n"
    "v_mfma_f32_32x32x16_bf16 v[80:95], v[196:199], a[44:47], v[80:95]\n"
    "ds_read_b64_tr_b16 v[192:193], v218 offset:26624\n"
    "ds_read_b64_tr_b16 v[194:195], v218 offset:27136\n"
    "buffer_load_dwordx4 v227, s[44:47], s84 offen offset:1024 lds\n"
    "v_rcp_f32 v204, v204\n"
    "s_waitcnt lgkmcnt(6)\n"
    "v_mfma_f32_32x32x16_bf16 v[64:79], v[168:171], a[48:51], v[64:79]\n"
    "ds_read_b64_tr_b16 v[196:197], v221 offset:26624\n"
    "ds_read_b64_tr_b16 v[198:199], v221 offset:27136\n"
    "v_rcp_f32 v205, v205\n"
    "v_rcp_f32 v206, v206\n"
    "v_mfma_f32_32x32x16_bf16 v[80:95], v[172:175], a[48:51], v[80:95]\n"
    "ds_read_b64_tr_b16 v[168:169], v218 offset:10240\n"
    "ds_read_b64_tr_b16 v[170:171], v218 offset:10752\n"
    "v_rcp_f32 v207, v207\n"
    "v_mul_f32 v36, v36, v204\n"
    "v_mul_f32 v37, v37, v205\n"
    "v_mfma_f32_32x32x16_bf16 v[64:79], v[176:179], a[52:55], v[64:79]\n"
    "ds_read_b64_tr_b16 v[172:173], v221 offset:10240\n"
    "ds_read_b64_tr_b16 v[174:175], v221 offset:10752\n"
    "v_mul_f32 v38, v38, v206\n"
    "v_mul_f32 v39, v39, v207\n"
    "v_mul_f32 v200, v40, v40\n"
    "v_mul_f32 v201, v41, v41\n"
    "v_mfma_f32_32x32x16_bf16 v[80:95], v[180:183], a[52:55], v[80:95]\n"
    "ds_read_b64_tr_b16 v[176:177], v218 offset:16384\n"
    "ds_read_b64_tr_b16 v[178:179], v218 offset:16896\n"
    "v_mul_f32 v202, v42, v42\n"
    "v_mul_f32 v203, v43, v43\n"
    "v_fmaak_f32 v200, v200, v231, 0xc0135761\n"
    "s_waitcnt lgkmcnt(6)\n"
    "v_mfma_f32_32x32x16_bf16 v[64:79], v[184:187], a[56:59], v[64:79]\n"
    "ds_read_b64_tr_b16 v[180:181], v221 offset:16384\n"
    "ds_read_b64_tr_b16 v[182:183], v221 offset:16896\n"
    "v_fmaak_f32 v201, v201, v231, 0xc0135761\n"
    "v_fmaak_f32 v202, v202, v231, 0xc0135761\n"
    "v_fmaak_f32 v203, v203, v231, 0xc0135761\n"
    "v_mul_f32 v200, v200, v40\n"
    "v_mfma_f32_32x32x16_bf16 v[80:95], v[188:191], a[56:59], v[80:95]\n"
    "ds_read_b64_tr_b16 v[184:185], v218 offset:22528\n"
    "ds_read_b64_tr_b16 v[186:187], v218 offset:23040\n"
    "v_mul_f32 v201, v201, v41\n"
    "v_mul_f32 v202, v202, v42\n"
    "v_mul_f32 v203, v203, v43\n"
    "v_mfma_f32_32x32x16_bf16 v[64:79], v[192:195], a[60:63], v[64:79]\n"
    "ds_read_b64_tr_b16 v[188:189], v221 offset:22528\n"
    "ds_read_b64_tr_b16 v[190:191], v221 offset:23040\n"
    "v_exp_f32 v200, v200\n"
    "v_exp_f32 v201, v201\n"
    "v_mfma_f32_32x32x16_bf16 v[80:95], v[196:199], a[60:63], v[80:95]\n"
    "ds_read_b64_tr_b16 v[192:193], v218 offset:28672\n"
    "ds_read_b64_tr_b16 v[194:195], v218 offset:29184\n"
    "v_exp_f32 v202, v202\n"
    "s_waitcnt lgkmcnt(6)\n"
    "v_mfma_f32_32x32x16_bf16 v[64:79], v[168:171], a[64:67], v[64:79]\n"
    "ds_read_b64_tr_b16 v[196:197], v221 offset:28672\n"
    "ds_read_b64_tr_b16 v[198:199], v221 offset:29184\n"
    "v_exp_f32 v203, v203\n"
    "v_add_f32 v200, 1.0, v200\n"
    "v_add_f32 v201, 1.0, v201\n"
    "v_mfma_f32_32x32x16_bf16 v[80:95], v[172:175], a[64:67], v[80:95]\n"
    "ds_read_b64_tr_b16 v[168:169], v218 offset:12288\n"
    "ds_read_b64_tr_b16 v[170:171], v218 offset:12800\n"
    "v_add_f32 v202, 1.0, v202\n"
    "v_add_f32 v203, 1.0, v203\n"
    "v_rcp_f32 v200, v200\n"
    "v_mfma_f32_32x32x16_bf16 v[64:79], v[176:179], a[68:71], v[64:79]\n"
    "ds_read_b64_tr_b16 v[172:173], v221 offset:12288\n"
    "ds_read_b64_tr_b16 v[174:175], v221 offset:12800\n"
    "v_rcp_f32 v201, v201\n"
    "v_rcp_f32 v202, v202\n"
    "v_mfma_f32_32x32x16_bf16 v[80:95], v[180:183], a[68:71], v[80:95]\n"
    "ds_read_b64_tr_b16 v[176:177], v218 offset:18432\n"
    "ds_read_b64_tr_b16 v[178:179], v218 offset:18944\n"
    "v_rcp_f32 v203, v203\n"
    "v_mul_f32 v40, v40, v200\n"
    "s_waitcnt lgkmcnt(6)\n"
    "v_mfma_f32_32x32x16_bf16 v[64:79], v[184:187], a[72:75], v[64:79]\n"
    "ds_read_b64_tr_b16 v[180:181], v221 offset:18432\n"
    "ds_read_b64_tr_b16 v[182:183], v221 offset:18944\n"
    "v_mul_f32 v41, v41, v201\n"
    "v_mul_f32 v42, v42, v202\n"
    "v_mul_f32 v43, v43, v203\n"
    "v_mul_f32 v204, v44, v44\n"
    "v_mfma_f32_32x32x16_bf16 v[80:95], v[188:191], a[72:75], v[80:95]\n"
    "ds_read_b64_tr_b16 v[184:185], v218 offset:24576\n"
    "ds_read_b64_tr_b16 v[186:187], v218 offset:25088\n"
    "v_mul_f32 v205, v45, v45\n"
    "v_mul_f32 v206, v46, v46\n"
    "v_mul_f32 v207, v47, v47\n"
    "v_fmaak_f32 v204, v204, v231, 0xc0135761\n"
    "v_mfma_f32_32x32x16_bf16 v[64:79], v[192:195], a[76:79], v[64:79]\n"
    "ds_read_b64_tr_b16 v[188:189], v221 offset:24576\n"
    "ds_read_b64_tr_b16 v[190:191], v221 offset:25088\n"
    "v_fmaak_f32 v205, v205, v231, 0xc0135761\n"
    "v_fmaak_f32 v206, v206, v231, 0xc0135761\n"
    "v_fmaak_f32 v207, v207, v231, 0xc0135761\n"
    "v_mul_f32 v204, v204, v44\n"
    "v_mfma_f32_32x32x16_bf16 v[80:95], v[196:199], a[76:79], v[80:95]\n"
    "ds_read_b64_tr_b16 v[192:193], v218 offset:30720\n"
    "ds_read_b64_tr_b16 v[194:195], v218 offset:31232\n"
    "v_mul_f32 v205, v205, v45\n"
    "v_mul_f32 v206, v206, v46\n"
    "v_mul_f32 v207, v207, v47\n"
    "s_waitcnt lgkmcnt(6)\n"
    "v_mfma_f32_32x32x16_bf16 v[64:79], v[168:171], a[80:83], v[64:79]\n"
    "s_waitcnt vmcnt(22)\n"
    "s_barrier\n"
    "v_mfma_f32_32x32x16_bf16 v[80:95], v[172:175], a[80:83], v[80:95]\n"
    "ds_read_b64_tr_b16 v[196:197], v221 offset:30720\n"
    "ds_read_b64_tr_b16 v[198:199], v221 offset:31232\n"
    "ds_read_b64_tr_b16 v[168:169], v216 offset:24576\n"
    "ds_read_b64_tr_b16 v[170:171], v216 offset:25088\n"
    "s_add_u32 m0, s65, 0\n"
    "s_add_u32 s84, s57, 4352\n"
    "v_mfma_f32_32x32x16_bf16 v[64:79], v[176:179], a[84:87], v[64:79]\n"
    "ds_read_b64_tr_b16 v[172:173], v219 offset:24576\n"
    "ds_read_b64_tr_b16 v[174:175], v219 offset:25088\n"
    "buffer_load_dwordx4 v242, s[40:43], s84 offen lds\n"
    "v_mfma_f32_32x32x16_bf16 v[80:95], v[180:183], a[84:87], v[80:95]\n"
    "ds_read_b64_tr_b16 v[176:177], v216 offset:30720\n"
    "ds_read_b64_tr_b16 v[178:179], v216 offset:31232\n"
    "buffer_load_dwordx4 v225, s[40:43], s84 offen offset:1024 lds\n"
    "s_add_u32 s84, s57, 592128\n"
    "s_waitcnt lgkmcnt(6)\n"
    "v_mfma_f32_32x32x16_bf16 v[64:79], v[184:187], a[88:91], v[64:79]\n"
    "ds_read_b64_tr_b16 v[180:181], v219 offset:30720\n"
    "ds_read_b64_tr_b16 v[182:183], v219 offset:31232\n"
    "buffer_load_dwordx4 v242, s[40:43], s84 offen offset:2048 lds\n"
    "v_exp_f32 v204, v204\n"
    "v_exp_f32 v205, v205\n"
    "v_mfma_f32_32x32x16_bf16 v[80:95], v[188:191], a[88:91], v[80:95]\n"
    "ds_read_b64_tr_b16 v[184:185], v216 offset:36864\n"
    "ds_read_b64_tr_b16 v[186:187], v216 offset:37376\n"
    "buffer_load_dwordx4 v225, s[40:43], s84 offen offset:3072 lds\n"
    "s_add_u32 m0, s65, 4096\n"
    "s_add_u32 s84, s57, 1184000\n"
    "v_exp_f32 v206, v206\n"
    "v_mfma_f32_32x32x16_bf16 v[64:79], v[192:195], a[92:95], v[64:79]\n"
    "ds_read_b64_tr_b16 v[188:189], v219 offset:36864\n"
    "ds_read_b64_tr_b16 v[190:191], v219 offset:37376\n"
    "buffer_load_dwordx4 v242, s[40:43], s84 offen lds\n"
    "v_exp_f32 v207, v207\n"
    "v_add_f32 v204, 1.0, v204\n"
    "v_mfma_f32_32x32x16_bf16 v[80:95], v[196:199], a[92:95], v[80:95]\n"
    "ds_read_b64_tr_b16 v[192:193], v216 offset:43008\n"
    "ds_read_b64_tr_b16 v[194:195], v216 offset:43520\n"
    "buffer_load_dwordx4 v225, s[40:43], s84 offen offset:1024 lds\n"
    "v_add_f32 v205, 1.0, v205\n"
    "v_add_f32 v206, 1.0, v206\n"
    "s_waitcnt lgkmcnt(6)\n"
    "v_mfma_f32_32x32x16_bf16 v[64:79], v[168:171], a[96:99], v[64:79]\n"
    "ds_read_b64_tr_b16 v[196:197], v219 offset:43008\n"
    "ds_read_b64_tr_b16 v[198:199], v219 offset:43520\n"
    "v_add_f32 v207, 1.0, v207\n"
    "v_rcp_f32 v204, v204\n"
    "v_mfma_f32_32x32x16_bf16 v[80:95], v[172:175], a[96:99], v[80:95]\n"
    "ds_read_b64_tr_b16 v[168:169], v216 offset:26624\n"
    "ds_read_b64_tr_b16 v[170:171], v216 offset:27136\n"
    "v_rcp_f32 v205, v205\n"
    "v_rcp_f32 v206, v206\n"
    "v_mfma_f32_32x32x16_bf16 v[64:79], v[176:179], a[100:103], v[64:79]\n"
    "ds_read_b64_tr_b16 v[172:173], v219 offset:26624\n"
    "ds_read_b64_tr_b16 v[174:175], v219 offset:27136\n"
    "v_rcp_f32 v207, v207\n"
    "v_mul_f32 v44, v44, v204\n"
    "v_mul_f32 v45, v45, v205\n"
    "v_mfma_f32_32x32x16_bf16 v[80:95], v[180:183], a[100:103], v[80:95]\n"
    "ds_read_b64_tr_b16 v[176:177], v216 offset:32768\n"
    "ds_read_b64_tr_b16 v[178:179], v216 offset:33280\n"
    "v_mul_f32 v46, v46, v206\n"
    "v_mul_f32 v47, v47, v207\n"
    "v_mul_f32 v200, v48, v48\n"
    "s_waitcnt lgkmcnt(6)\n"
    "v_mfma_f32_32x32x16_bf16 v[64:79], v[184:187], a[104:107], v[64:79]\n"
    "ds_read_b64_tr_b16 v[180:181], v219 offset:32768\n"
    "ds_read_b64_tr_b16 v[182:183], v219 offset:33280\n"
    "v_mul_f32 v201, v49, v49\n"
    "v_mul_f32 v202, v50, v50\n"
    "v_mul_f32 v203, v51, v51\n"
    "v_fmaak_f32 v200, v200, v231, 0xc0135761\n"
    "v_mfma_f32_32x32x16_bf16 v[80:95], v[188:191], a[104:107], v[80:95]\n"
    "ds_read_b64_tr_b16 v[184:185], v216 offset:38912\n"
    "ds_read_b64_tr_b16 v[186:187], v216 offset:39424\n"
    "v_fmaak_f32 v201, v201, v231, 0xc0135761\n"
    "v_fmaak_f32 v202, v202, v231, 0xc0135761\n"
    "v_fmaak_f32 v203, v203, v231, 0xc0135761\n"
    "v_mul_f32 v200, v200, v48\n"
    "v_mfma_f32_32x32x16_bf16 v[64:79], v[192:195], a[108:111], v[64:79]\n"
    "ds_read_b64_tr_b16 v[188:189], v219 offset:38912\n"
    "ds_read_b64_tr_b16 v[190:191], v219 offset:39424\n"
    "v_mul_f32 v201, v201, v49\n"
    "v_mul_f32 v202, v202, v50\n"
    "v_mul_f32 v203, v203, v51\n"
    "v_mfma_f32_32x32x16_bf16 v[80:95], v[196:199], a[108:111], v[80:95]\n"
    "ds_read_b64_tr_b16 v[192:193], v216 offset:45056\n"
    "ds_read_b64_tr_b16 v[194:195], v216 offset:45568\n"
    "v_exp_f32 v200, v200\n"
    "s_waitcnt lgkmcnt(6)\n"
    "v_mfma_f32_32x32x16_bf16 v[64:79], v[168:171], a[112:115], v[64:79]\n"
    "ds_read_b64_tr_b16 v[196:197], v219 offset:45056\n"
    "ds_read_b64_tr_b16 v[198:199], v219 offset:45568\n"
    "v_exp_f32 v201, v201\n"
    "v_exp_f32 v202, v202\n"
    "v_mfma_f32_32x32x16_bf16 v[80:95], v[172:175], a[112:115], v[80:95]\n"
    "ds_read_b64_tr_b16 v[168:169], v216 offset:28672\n"
    "ds_read_b64_tr_b16 v[170:171], v216 offset:29184\n"
    "v_exp_f32 v203, v203\n"
    "v_add_f32 v200, 1.0, v200\n"
    "v_add_f32 v201, 1.0, v201\n"
    "v_mfma_f32_32x32x16_bf16 v[64:79], v[176:179], a[116:119], v[64:79]\n"
    "ds_read_b64_tr_b16 v[172:173], v219 offset:28672\n"
    "ds_read_b64_tr_b16 v[174:175], v219 offset:29184\n"
    "v_add_f32 v202, 1.0, v202\n"
    "v_add_f32 v203, 1.0, v203\n"
    "v_rcp_f32 v200, v200\n"
    "v_mfma_f32_32x32x16_bf16 v[80:95], v[180:183], a[116:119], v[80:95]\n"
    "ds_read_b64_tr_b16 v[176:177], v216 offset:34816\n"
    "ds_read_b64_tr_b16 v[178:179], v216 offset:35328\n"
    "v_rcp_f32 v201, v201\n"
    "s_waitcnt lgkmcnt(6)\n"
    "v_mfma_f32_32x32x16_bf16 v[64:79], v[184:187], a[120:123], v[64:79]\n"
    "ds_read_b64_tr_b16 v[180:181], v219 offset:34816\n"
    "ds_read_b64_tr_b16 v[182:183], v219 offset:35328\n"
    "v_rcp_f32 v202, v202\n"
    "v_rcp_f32 v203, v203\n"
    "v_mfma_f32_32x32x16_bf16 v[80:95], v[188:191], a[120:123], v[80:95]\n"
    "ds_read_b64_tr_b16 v[184:185], v216 offset:40960\n"
    "ds_read_b64_tr_b16 v[186:187], v216 offset:41472\n"
    "v_mul_f32 v48, v48, v200\n"
    "v_mul_f32 v49, v49, v201\n"
    "v_mul_f32 v50, v50, v202\n"
    "v_mul_f32 v51, v51, v203\n"
    "v_mfma_f32_32x32x16_bf16 v[64:79], v[192:195], a[124:127], v[64:79]\n"
    "ds_read_b64_tr_b16 v[188:189], v219 offset:40960\n"
    "ds_read_b64_tr_b16 v[190:191], v219 offset:41472\n"
    "v_mul_f32 v204, v52, v52\n"
    "v_mul_f32 v205, v53, v53\n"
    "v_mul_f32 v206, v54, v54\n"
    "v_mul_f32 v207, v55, v55\n"
    "v_mfma_f32_32x32x16_bf16 v[80:95], v[196:199], a[124:127], v[80:95]\n"
    "ds_read_b64_tr_b16 v[192:193], v216 offset:47104\n"
    "ds_read_b64_tr_b16 v[194:195], v216 offset:47616\n"
    "v_fmaak_f32 v204, v204, v231, 0xc0135761\n"
    "v_fmaak_f32 v205, v205, v231, 0xc0135761\n"
    "v_fmaak_f32 v206, v206, v231, 0xc0135761\n"
    "s_waitcnt lgkmcnt(6)\n"
    "v_mfma_f32_32x32x16_bf16 v[64:79], v[168:171], a[128:131], v[64:79]\n"
    "s_waitcnt vmcnt(21)\n"
    "s_barrier\n"
    "v_mfma_f32_32x32x16_bf16 v[80:95], v[172:175], a[128:131], v[80:95]\n"
    "ds_read_b64_tr_b16 v[196:197], v219 offset:47104\n"
    "ds_read_b64_tr_b16 v[198:199], v219 offset:47616\n"
    "ds_read_b64_tr_b16 v[168:169], v223 offset:8192\n"
    "ds_read_b64_tr_b16 v[170:171], v223 offset:8448\n"
    "s_add_u32 m0, s65, 139264\n"
    "s_add_u32 s84, s57, 1773824\n"
    "v_mfma_f32_32x32x16_bf16 v[64:79], v[176:179], a[132:135], v[64:79]\n"
    "ds_read_b64_tr_b16 v[172:173], v223 offset:9216\n"
    "ds_read_b64_tr_b16 v[174:175], v223 offset:9472\n"
    "buffer_load_dwordx4 v242, s[40:43], s84 offen lds\n"
    "v_fmaak_f32 v207, v207, v231, 0xc0135761\n"
    "v_mfma_f32_32x32x16_bf16 v[80:95], v[180:183], a[132:135], v[80:95]\n"
    "ds_read_b64_tr_b16 v[176:177], v223 offset:14336\n"
    "ds_read_b64_tr_b16 v[178:179], v223 offset:14592\n"
    "buffer_load_dwordx4 v225, s[40:43], s84 offen offset:1024 lds\n"
    "s_add_u32 s84, s57, 2361600\n"
    "s_waitcnt lgkmcnt(6)\n"
    "v_mfma_f32_32x32x16_bf16 v[64:79], v[184:187], a[136:139], v[64:79]\n"
    "ds_read_b64_tr_b16 v[180:181], v223 offset:15360\n"
    "ds_read_b64_tr_b16 v[182:183], v223 offset:15616\n"
    "buffer_load_dwordx4 v242, s[40:43], s84 offen offset:2048 lds\n"
    "v_mul_f32 v204, v204, v52\n"
    "v_mul_f32 v205, v205, v53\n"
    "v_mfma_f32_32x32x16_bf16 v[80:95], v[188:191], a[136:139], v[80:95]\n"
    "ds_read_b64_tr_b16 v[184:185], v223 offset:20480\n"
    "ds_read_b64_tr_b16 v[186:187], v223 offset:20736\n"
    "buffer_load_dwordx4 v225, s[40:43], s84 offen offset:3072 lds\n"
    "s_add_u32 m0, s65, 143360\n"
    "s_add_u32 s84, s57, 2953472\n"
    "v_mul_f32 v206, v206, v54\n"
    "v_mul_f32 v207, v207, v55\n"
    "v_mfma_f32_32x32x16_bf16 v[64:79], v[192:195], a[140:143], v[64:79]\n"
    "ds_read_b64_tr_b16 v[188:189], v223 offset:21504\n"
    "ds_read_b64_tr_b16 v[190:191], v223 offset:21760\n"
    "buffer_load_dwordx4 v242, s[40:43], s84 offen lds\n"
    "v_exp_f32 v204, v204\n"
    "v_mfma_f32_32x32x16_bf16 v[80:95], v[196:199], a[140:143], v[80:95]\n"
    "ds_read_b64_tr_b16 v[192:193], v223 offset:26624\n"
    "ds_read_b64_tr_b16 v[194:195], v223 offset:26880\n"
    "buffer_load_dwordx4 v225, s[40:43], s84 offen offset:1024 lds\n"
    "v_exp_f32 v205, v205\n"
    "s_waitcnt lgkmcnt(6)\n"
    "v_mfma_f32_32x32x16_bf16 v[0:15], v[168:171], a[0:3], 0\n"
    "ds_read_b64_tr_b16 v[196:197], v223 offset:27648\n"
    "ds_read_b64_tr_b16 v[198:199], v223 offset:27904\n"
    "s_add_u32 s86, s57, 4352\n"
    "buffer_load_dword v255, v251, s[44:47], s86 offen\n"
    "s_add_u32 s86, s57, 4480\n"
    "buffer_load_dword v255, v251, s[40:43], s86 offen\n"
    "v_exp_f32 v206, v206\n"
    "v_exp_f32 v207, v207\n"
    "v_mfma_f32_32x32x16_bf16 v[16:31], v[172:175], a[0:3], 0\n"
    "ds_read_b64_tr_b16 v[168:169], v223 offset:10240\n"
    "ds_read_b64_tr_b16 v[170:171], v223 offset:10496\n"
    "v_add_f32 v204, 1.0, v204\n"
    "v_add_f32 v205, 1.0, v205\n"
    "v_add_f32 v206, 1.0, v206\n"
    "v_add_f32 v207, 1.0, v207\n"
    "v_mfma_f32_32x32x16_bf16 v[0:15], v[176:179], a[4:7], v[0:15]\n"
    "ds_read_b64_tr_b16 v[172:173], v223 offset:11264\n"
    "ds_read_b64_tr_b16 v[174:175], v223 offset:11520\n"
    "v_rcp_f32 v204, v204\n"
    "v_rcp_f32 v205, v205\n"
    "v_mfma_f32_32x32x16_bf16 v[16:31], v[180:183], a[4:7], v[16:31]\n"
    "ds_read_b64_tr_b16 v[176:177], v223 offset:16384\n"
    "ds_read_b64_tr_b16 v[178:179], v223 offset:16640\n"
    "v_rcp_f32 v206, v206\n"
    "s_waitcnt lgkmcnt(6)\n"
    "v_mfma_f32_32x32x16_bf16 v[0:15], v[184:187], a[8:11], v[0:15]\n"
    "ds_read_b64_tr_b16 v[180:181], v223 offset:17408\n"
    "ds_read_b64_tr_b16 v[182:183], v223 offset:17664\n"
    "v_rcp_f32 v207, v207\n"
    "v_mul_f32 v52, v52, v204\n"
    "v_mul_f32 v53, v53, v205\n"
    "v_mfma_f32_32x32x16_bf16 v[16:31], v[188:191], a[8:11], v[16:31]\n"
    "ds_read_b64_tr_b16 v[184:185], v223 offset:22528\n"
    "ds_read_b64_tr_b16 v[186:187], v223 offset:22784\n"
    "v_mul_f32 v54, v54, v206\n"
    "v_mul_f32 v55, v55, v207\n"
    "v_mul_f32 v200, v56, v56\n"
    "v_mul_f32 v201, v57, v57\n"
    "v_mfma_f32_32x32x16_bf16 v[0:15], v[192:195], a[12:15], v[0:15]\n"
    "ds_read_b64_tr_b16 v[188:189], v223 offset:23552\n"
    "ds_read_b64_tr_b16 v[190:191], v223 offset:23808\n"
    "v_mul_f32 v202, v58, v58\n"
    "v_mul_f32 v203, v59, v59\n"
    "v_fmaak_f32 v200, v200, v231, 0xc0135761\n"
    "v_fmaak_f32 v201, v201, v231, 0xc0135761\n"
    "v_mfma_f32_32x32x16_bf16 v[16:31], v[196:199], a[12:15], v[16:31]\n"
    "ds_read_b64_tr_b16 v[192:193], v223 offset:28672\n"
    "ds_read_b64_tr_b16 v[194:195], v223 offset:28928\n"
    "v_fmaak_f32 v202, v202, v231, 0xc0135761\n"
    "v_fmaak_f32 v203, v203, v231, 0xc0135761\n"
    "v_mul_f32 v200, v200, v56\n"
    "s_waitcnt lgkmcnt(6)\n"
    "v_mfma_f32_32x32x16_bf16 v[0:15], v[168:171], a[16:19], v[0:15]\n"
    "ds_read_b64_tr_b16 v[196:197], v223 offset:29696\n"
    "ds_read_b64_tr_b16 v[198:199], v223 offset:29952\n"
    "v_mul_f32 v201, v201, v57\n"
    "v_mul_f32 v202, v202, v58\n"
    "v_mul_f32 v203, v203, v59\n"
    "v_mfma_f32_32x32x16_bf16 v[16:31], v[172:175], a[16:19], v[16:31]\n"
    "ds_read_b64_tr_b16 v[168:169], v223 offset:12288\n"
    "ds_read_b64_tr_b16 v[170:171], v223 offset:12544\n"
    "v_exp_f32 v200, v200\n"
    "v_exp_f32 v201, v201\n"
    "v_mfma_f32_32x32x16_bf16 v[0:15], v[176:179], a[20:23], v[0:15]\n"
    "ds_read_b64_tr_b16 v[172:173], v223 offset:13312\n"
    "ds_read_b64_tr_b16 v[174:175], v223 offset:13568\n"
    "v_exp_f32 v202, v202\n"
    "v_exp_f32 v203, v203\n"
    "v_mfma_f32_32x32x16_bf16 v[16:31], v[180:183], a[20:23], v[16:31]\n"
    "ds_read_b64_tr_b16 v[176:177], v223 offset:18432\n"
    "ds_read_b64_tr_b16 v[178:179], v223 offset:18688\n"
    "v_add_f32 v200, 1.0, v200\n"
    "v_add_f32 v201, 1.0, v201\n"
    "v_add_f32 v202, 1.0, v202\n"
    "s_waitcnt lgkmcnt(6)\n"
    "v_mfma_f32_32x32x16_bf16 v[0:15], v[184:187], a[24:27], v[0:15]\n"
    "ds_read_b64_tr_b16 v[180:181], v223 offset:19456\n"
    "ds_read_b64_tr_b16 v[182:183], v223 offset:19712\n"
    "v_add_f32 v203, 1.0, v203\n"
    "v_rcp_f32 v200, v200\n"
    "v_mfma_f32_32x32x16_bf16 v[16:31], v[188:191], a[24:27], v[16:31]\n"
    "ds_read_b64_tr_b16 v[184:185], v223 offset:24576\n"
    "ds_read_b64_tr_b16 v[186:187], v223 offset:24832\n"
    "v_rcp_f32 v201, v201\n"
    "v_rcp_f32 v202, v202\n"
    "v_mfma_f32_32x32x16_bf16 v[0:15], v[192:195], a[28:31], v[0:15]\n"
    "ds_read_b64_tr_b16 v[188:189], v223 offset:25600\n"
    "ds_read_b64_tr_b16 v[190:191], v223 offset:25856\n"
    "v_rcp_f32 v203, v203\n"
    "v_mul_f32 v56, v56, v200\n"
    "v_mul_f32 v57, v57, v201\n"
    "v_mfma_f32_32x32x16_bf16 v[16:31], v[196:199], a[28:31], v[16:31]\n"
    "ds_read_b64_tr_b16 v[192:193], v223 offset:30720\n"
    "ds_read_b64_tr_b16 v[194:195], v223 offset:30976\n"
    "v_mul_f32 v58, v58, v202\n"
    "v_mul_f32 v59, v59, v203\n"
    "v_mul_f32 v204, v60, v60\n"
    "s_waitcnt lgkmcnt(6)\n"
    "v_mfma_f32_32x32x16_bf16 v[0:15], v[168:171], a[32:35], v[0:15]\n"
    "s_waitcnt vmcnt(22)\n"
    "s_barrier\n"
    "v_mfma_f32_32x32x16_bf16 v[16:31], v[172:175], a[32:35], v[16:31]\n"
    "ds_read_b64_tr_b16 v[196:197], v223 offset:31744\n"
    "ds_read_b64_tr_b16 v[198:199], v223 offset:32000\n"
    "ds_read_b64_tr_b16 v[168:169], v223 offset:32768\n"
    "ds_read_b64_tr_b16 v[170:171], v223 offset:33024\n"
    "s_add_u32 m0, s65, 24576\n"
    "s_add_u32 s84, s57, 3543296\n"
    "v_mfma_f32_32x32x16_bf16 v[0:15], v[176:179], a[36:39], v[0:15]\n"
    "ds_read_b64_tr_b16 v[172:173], v223 offset:33792\n"
    "ds_read_b64_tr_b16 v[174:175], v223 offset:34048\n"
    "buffer_load_dwordx4 v242, s[40:43], s84 offen lds\n"
    "v_mul_f32 v205, v61, v61\n"
    "v_mfma_f32_32x32x16_bf16 v[16:31], v[180:183], a[36:39], v[16:31]\n"
    "ds_read_b64_tr_b16 v[176:177], v223 offset:38912\n"
    "ds_read_b64_tr_b16 v[178:179], v223 offset:39168\n"
    "buffer_load_dwordx4 v225, s[40:43], s84 offen offset:1024 lds\n"
    "s_add_u32 s84, s57, 4131072\n"
    "s_waitcnt lgkmcnt(6)\n"
    "v_mfma_f32_32x32x16_bf16 v[0:15], v[184:187], a[40:43], v[0:15]\n"
    "ds_read_b64_tr_b16 v[180:181], v223 offset:39936\n"
    "ds_read_b64_tr_b16 v[182:183], v223 offset:40192\n"
    "buffer_load_dwordx4 v242, s[40:43], s84 offen offset:2048 lds\n"
    "v_mul_f32 v206, v62, v62\n"
    "v_mfma_f32_32x32x16_bf16 v[16:31], v[188:191], a[40:43], v[16:31]\n"
    "ds_read_b64_tr_b16 v[184:185], v223 offset:45056\n"
    "ds_read_b64_tr_b16 v[186:187], v223 offset:45312\n"
    "buffer_load_dwordx4 v225, s[40:43], s84 offen offset:3072 lds\n"
    "s_add_u32 m0, s65, 28672\n"
    "s_add_u32 s84, s57, 4722944\n"
    "v_mul_f32 v207, v63, v63\n"
    "v_fmaak_f32 v204, v204, v231, 0xc0135761\n"
    "v_mfma_f32_32x32x16_bf16 v[0:15], v[192:195], a[44:47], v[0:15]\n"
    "ds_read_b64_tr_b16 v[188:189], v223 offset:46080\n"
    "ds_read_b64_tr_b16 v[190:191], v223 offset:46336\n"
    "buffer_load_dwordx4 v242, s[40:43], s84 offen lds\n"
    "v_fmaak_f32 v205, v205, v231, 0xc0135761\n"
    "v_fmaak_f32 v206, v206, v231, 0xc0135761\n"
    "v_mfma_f32_32x32x16_bf16 v[16:31], v[196:199], a[44:47], v[16:31]\n"
    "ds_read_b64_tr_b16 v[192:193], v223 offset:51200\n"
    "ds_read_b64_tr_b16 v[194:195], v223 offset:51456\n"
    "buffer_load_dwordx4 v225, s[40:43], s84 offen offset:1024 lds\n"
    "v_fmaak_f32 v207, v207, v231, 0xc0135761\n"
    "v_mul_f32 v204, v204, v60\n"
    "s_waitcnt lgkmcnt(6)\n"
    "v_mfma_f32_32x32x16_bf16 v[0:15], v[168:171], a[48:51], v[0:15]\n"
    "ds_read_b64_tr_b16 v[196:197], v223 offset:52224\n"
    "ds_read_b64_tr_b16 v[198:199], v223 offset:52480\n"
    "v_mul_f32 v205, v205, v61\n"
    "v_mul_f32 v206, v206, v62\n"
    "v_mul_f32 v207, v207, v63\n"
    "v_mfma_f32_32x32x16_bf16 v[16:31], v[172:175], a[48:51], v[16:31]\n"
    "ds_read_b64_tr_b16 v[168:169], v223 offset:34816\n"
    "ds_read_b64_tr_b16 v[170:171], v223 offset:35072\n"
    "v_exp_f32 v204, v204\n"
    "v_exp_f32 v205, v205\n"
    "v_mfma_f32_32x32x16_bf16 v[0:15], v[176:179], a[52:55], v[0:15]\n"
    "ds_read_b64_tr_b16 v[172:173], v223 offset:35840\n"
    "ds_read_b64_tr_b16 v[174:175], v223 offset:36096\n"
    "v_exp_f32 v206, v206\n"
    "v_exp_f32 v207, v207\n"
    "v_mfma_f32_32x32x16_bf16 v[16:31], v[180:183], a[52:55], v[16:31]\n"
    "ds_read_b64_tr_b16 v[176:177], v223 offset:40960\n"
    "ds_read_b64_tr_b16 v[178:179], v223 offset:41216\n"
    "v_add_f32 v204, 1.0, v204\n"
    "v_add_f32 v205, 1.0, v205\n"
    "v_add_f32 v206, 1.0, v206\n"
    "s_waitcnt lgkmcnt(6)\n"
    "v_mfma_f32_32x32x16_bf16 v[0:15], v[184:187], a[56:59], v[0:15]\n"
    "ds_read_b64_tr_b16 v[180:181], v223 offset:41984\n"
    "ds_read_b64_tr_b16 v[182:183], v223 offset:42240\n"
    "v_add_f32 v207, 1.0, v207\n"
    "v_rcp_f32 v204, v204\n"
    "v_mfma_f32_32x32x16_bf16 v[16:31], v[188:191], a[56:59], v[16:31]\n"
    "ds_read_b64_tr_b16 v[184:185], v223 offset:47104\n"
    "ds_read_b64_tr_b16 v[186:187], v223 offset:47360\n"
    "v_rcp_f32 v205, v205\n"
    "v_rcp_f32 v206, v206\n"
    "v_mfma_f32_32x32x16_bf16 v[0:15], v[192:195], a[60:63], v[0:15]\n"
    "ds_read_b64_tr_b16 v[188:189], v223 offset:48128\n"
    "ds_read_b64_tr_b16 v[190:191], v223 offset:48384\n"
    "v_rcp_f32 v207, v207\n"
    "v_mul_f32 v60, v60, v204\n"
    "v_mul_f32 v61, v61, v205\n"
    "v_mfma_f32_32x32x16_bf16 v[16:31], v[196:199], a[60:63], v[16:31]\n"
    "ds_read_b64_tr_b16 v[192:193], v223 offset:53248\n"
    "ds_read_b64_tr_b16 v[194:195], v223 offset:53504\n"
    "v_mul_f32 v62, v62, v206\n"
    "v_mul_f32 v63, v63, v207\n"
    "v_mul_f32 v32, v32, v64\n"
    "s_waitcnt lgkmcnt(6)\n"
    "v_mfma_f32_32x32x16_bf16 v[0:15], v[168:171], a[64:67], v[0:15]\n"
    "ds_read_b64_tr_b16 v[196:197], v223 offset:54272\n"
    "ds_read_b64_tr_b16 v[198:199], v223 offset:54528\n"
    "v_mul_f32 v33, v33, v65\n"
    "v_mul_f32 v34, v34, v66\n"
    "v_mul_f32 v35, v35, v67\n"
    "v_mul_f32 v36, v36, v68\n"
    "v_mfma_f32_32x32x16_bf16 v[16:31], v[172:175], a[64:67], v[16:31]\n"
    "ds_read_b64_tr_b16 v[168:169], v223 offset:36864\n"
    "ds_read_b64_tr_b16 v[170:171], v223 offset:37120\n"
    "v_mul_f32 v37, v37, v69\n"
    "v_mul_f32 v38, v38, v70\n"
    "v_mul_f32 v39, v39, v71\n"
    "v_cvt_pk_bf16_f32 v112, v32, v33\n"
    "v_mfma_f32_32x32x16_bf16 v[0:15], v[176:179], a[68:71], v[0:15]\n"
    "ds_read_b64_tr_b16 v[172:173], v223 offset:37888\n"
    "ds_read_b64_tr_b16 v[174:175], v223 offset:38144\n"
    "v_cvt_pk_bf16_f32 v113, v34, v35\n"
    "v_cvt_pk_bf16_f32 v114, v36, v37\n"
    "v_cvt_pk_bf16_f32 v115, v38, v39\n"
    "v_mfma_f32_32x32x16_bf16 v[16:31], v[180:183], a[68:71], v[16:31]\n"
    "ds_read_b64_tr_b16 v[176:177], v223 offset:43008\n"
    "ds_read_b64_tr_b16 v[178:179], v223 offset:43264\n"
    "s_waitcnt lgkmcnt(6)\n"
    "v_mfma_f32_32x32x16_bf16 v[0:15], v[184:187], a[72:75], v[0:15]\n"
    "ds_read_b64_tr_b16 v[180:181], v223 offset:44032\n"
    "ds_read_b64_tr_b16 v[182:183], v223 offset:44288\n"
    "s_add_u32 s85, s67, 4096\n"
    "buffer_store_dwordx4 v[112:115], v250, s[52:55], s85 offen sc1\n"
    "v_mfma_f32_32x32x16_bf16 v[16:31], v[188:191], a[72:75], v[16:31]\n"
    "ds_read_b64_tr_b16 v[184:185], v223 offset:49152\n"
    "ds_read_b64_tr_b16 v[186:187], v223 offset:49408\n"
    "v_mul_f32 v40, v40, v72\n"
    "v_mul_f32 v41, v41, v73\n"
    "v_mul_f32 v42, v42, v74\n"
    "v_mul_f32 v43, v43, v75\n"
    "v_mfma_f32_32x32x16_bf16 v[0:15], v[192:195], a[76:79], v[0:15]\n"
    "ds_read_b64_tr_b16 v[188:189], v223 offset:50176\n"
    "ds_read_b64_tr_b16 v[190:191], v223 offset:50432\n"
    "v_mul_f32 v44, v44, v76\n"
    "v_mul_f32 v45, v45, v77\n"
    "v_mul_f32 v46, v46, v78\n"
    "v_mul_f32 v47, v47, v79\n"
    "v_mfma_f32_32x32x16_bf16 v[16:31], v[196:199], a[76:79], v[16:31]\n"
    "ds_read_b64_tr_b16 v[192:193], v223 offset:55296\n"
    "ds_read_b64_tr_b16 v[194:195], v223 offset:55552\n"
    "v_cvt_pk_bf16_f32 v116, v40, v41\n"
    "v_cvt_pk_bf16_f32 v117, v42, v43\n"
    "v_cvt_pk_bf16_f32 v118, v44, v45\n"
    "s_waitcnt lgkmcnt(6)\n"
    "v_mfma_f32_32x32x16_bf16 v[0:15], v[168:171], a[80:83], v[0:15]\n"
    "s_waitcnt vmcnt(21)\n"
    "s_barrier\n"
    "v_mfma_f32_32x32x16_bf16 v[16:31], v[172:175], a[80:83], v[16:31]\n"
    "ds_read_b64_tr_b16 v[196:197], v223 offset:56320\n"
    "ds_read_b64_tr_b16 v[198:199], v223 offset:56576\n"
    "ds_read_b64_tr_b16 v[168:169], v222 offset:49152\n"
    "ds_read_b64_tr_b16 v[170:171], v222 offset:49408\n"
    "s_add_u32 m0, s65, 73728\n"
    "s_add_u32 s84, s57, 4352\n"
    "v_mfma_f32_32x32x16_bf16 v[0:15], v[176:179], a[84:87], v[0:15]\n"
    "ds_read_b64_tr_b16 v[172:173], v222 offset:50176\n"
    "ds_read_b64_tr_b16 v[174:175], v222 offset:50432\n"
    "buffer_load_dwordx4 v242, s[44:47], s84 offen lds\n"
    "v_cvt_pk_bf16_f32 v119, v46, v47\n"
    "v_mfma_f32_32x32x16_bf16 v[16:31], v[180:183], a[84:87], v[16:31]\n"
    "ds_read_b64_tr_b16 v[176:177], v222 offset:55296\n"
    "ds_read_b64_tr_b16 v[178:179], v222 offset:55552\n"
    "buffer_load_dwordx4 v225, s[44:47], s84 offen offset:1024 lds\n"
    "s_add_u32 s84, s57, 592128\n"
    "s_waitcnt lgkmcnt(6)\n"
    "v_mfma_f32_32x32x16_bf16 v[0:15], v[184:187], a[88:91], v[0:15]\n"
    "ds_read_b64_tr_b16 v[180:181], v222 offset:56320\n"
    "ds_read_b64_tr_b16 v[182:183], v222 offset:56576\n"
    "buffer_load_dwordx4 v242, s[44:47], s84 offen offset:2048 lds\n"
    "v_mfma_f32_32x32x16_bf16 v[16:31], v[188:191], a[88:91], v[16:31]\n"
    "ds_read_b64_tr_b16 v[184:185], v222 offset:61440\n"
    "ds_read_b64_tr_b16 v[186:187], v222 offset:61696\n"
    "buffer_load_dwordx4 v225, s[44:47], s84 offen offset:3072 lds\n"
    "s_add_u32 m0, s65, 77824\n"
    "s_add_u32 s84, s57, 1184000\n"
    "v_mfma_f32_32x32x16_bf16 v[0:15], v[192:195], a[92:95], v[0:15]\n"
    "ds_read_b64_tr_b16 v[188:189], v222 offset:62464\n"
    "ds_read_b64_tr_b16 v[190:191], v222 offset:62720\n"
    "buffer_load_dwordx4 v242, s[44:47], s84 offen lds\n"
    "v_mfma_f32_32x32x16_bf16 v[16:31], v[196:199], a[92:95], v[16:31]\n"
    "ds_read_b64_tr_b16 v[192:193], v223 offset:2048\n"
    "ds_read_b64_tr_b16 v[194:195], v223 offset:2304\n"
    "buffer_load_dwordx4 v225, s[44:47], s84 offen offset:1024 lds\n"
    "s_waitcnt lgkmcnt(6)\n"
    "v_mfma_f32_32x32x16_bf16 v[0:15], v[168:171], a[96:99], v[0:15]\n"
    "ds_read_b64_tr_b16 v[196:197], v223 offset:3072\n"
    "ds_read_b64_tr_b16 v[198:199], v223 offset:3328\n"
    "s_add_u32 s85, s67, 5120\n"
    "buffer_store_dwordx4 v[116:119], v250, s[52:55], s85 offen sc1\n"
    "v_mul_f32 v48, v48, v80\n"
    "v_mfma_f32_32x32x16_bf16 v[16:31], v[172:175], a[96:99], v[16:31]\n"
    "ds_read_b64_tr_b16 v[168:169], v222 offset:51200\n"
    "ds_read_b64_tr_b16 v[170:171], v222 offset:51456\n"
    "v_mul_f32 v49, v49, v81\n"
    "v_mul_f32 v50, v50, v82\n"
    "v_mul_f32 v51, v51, v83\n"
    "v_mul_f32 v52, v52, v84\n"
    "v_mfma_f32_32x32x16_bf16 v[0:15], v[176:179], a[100:103], v[0:15]\n"
    "ds_read_b64_tr_b16 v[172:173], v222 offset:52224\n"
    "ds_read_b64_tr_b16 v[174:175], v222 offset:52480\n"
    "v_mul_f32 v53, v53, v85\n"
    "v_mul_f32 v54, v54, v86\n"
    "v_mul_f32 v55, v55, v87\n"
    "v_cvt_pk_bf16_f32 v120, v48, v49\n"
    "v_mfma_f32_32x32x16_bf16 v[16:31], v[180:183], a[100:103], v[16:31]\n"
    "ds_read_b64_tr_b16 v[176:177], v222 offset:57344\n"
    "ds_read_b64_tr_b16 v[178:179], v222 offset:57600\n"
    "v_cvt_pk_bf16_f32 v121, v50, v51\n"
    "v_cvt_pk_bf16_f32 v122, v52, v53\n"
    "v_cvt_pk_bf16_f32 v123, v54, v55\n"
    "s_waitcnt lgkmcnt(6)\n"
    "v_mfma_f32_32x32x16_bf16 v[0:15], v[184:187], a[104:107], v[0:15]\n"
    "ds_read_b64_tr_b16 v[180:181], v222 offset:58368\n"
    "ds_read_b64_tr_b16 v[182:183], v222 offset:58624\n"
    "s_add_u32 s85, s67, 6144\n"
    "buffer_store_dwordx4 v[120:123], v250, s[52:55], s85 offen sc1\n"
    "v_mfma_f32_32x32x16_bf16 v[16:31], v[188:191], a[104:107], v[16:31]\n"
    "ds_read_b64_tr_b16 v[184:185], v222 offset:63488\n"
    "ds_read_b64_tr_b16 v[186:187], v222 offset:63744\n"
    "v_mul_f32 v56, v56, v88\n"
    "v_mul_f32 v57, v57, v89\n"
    "v_mul_f32 v58, v58, v90\n"
    "v_mul_f32 v59, v59, v91\n"
    "v_mfma_f32_32x32x16_bf16 v[0:15], v[192:195], a[108:111], v[0:15]\n"
    "ds_read_b64_tr_b16 v[188:189], v222 offset:64512\n"
    "ds_read_b64_tr_b16 v[190:191], v222 offset:64768\n"
    "v_mul_f32 v60, v60, v92\n"
    "v_mul_f32 v61, v61, v93\n"
    "v_mul_f32 v62, v62, v94\n"
    "v_mul_f32 v63, v63, v95\n"
    "v_mfma_f32_32x32x16_bf16 v[16:31], v[196:199], a[108:111], v[16:31]\n"
    "ds_read_b64_tr_b16 v[192:193], v223 offset:4096\n"
    "ds_read_b64_tr_b16 v[194:195], v223 offset:4352\n"
    "v_cvt_pk_bf16_f32 v124, v56, v57\n"
    "v_cvt_pk_bf16_f32 v125, v58, v59\n"
    "v_cvt_pk_bf16_f32 v126, v60, v61\n"
    "s_waitcnt lgkmcnt(6)\n"
    "v_mfma_f32_32x32x16_bf16 v[0:15], v[168:171], a[112:115], v[0:15]\n"
    "ds_read_b64_tr_b16 v[196:197], v223 offset:5120\n"
    "ds_read_b64_tr_b16 v[198:199], v223 offset:5376\n"
    "v_cvt_pk_bf16_f32 v127, v62, v63\n"
    "v_mfma_f32_32x32x16_bf16 v[16:31], v[172:175], a[112:115], v[16:31]\n"
    "ds_read_b64_tr_b16 v[168:169], v222 offset:53248\n"
    "ds_read_b64_tr_b16 v[170:171], v222 offset:53504\n"
    "s_add_u32 s85, s67, 7168\n"
    "buffer_store_dwordx4 v[124:127], v250, s[52:55], s85 offen sc1\n"
    "v_mfma_f32_32x32x16_bf16 v[0:15], v[176:179], a[116:119], v[0:15]\n"
    "ds_read_b64_tr_b16 v[172:173], v222 offset:54272\n"
    "ds_read_b64_tr_b16 v[174:175], v222 offset:54528\n"
    "v_mfma_f32_32x32x16_bf16 v[16:31], v[180:183], a[116:119], v[16:31]\n"
    "ds_read_b64_tr_b16 v[176:177], v222 offset:59392\n"
    "ds_read_b64_tr_b16 v[178:179], v222 offset:59648\n"
    "s_waitcnt lgkmcnt(6)\n"
    "v_mfma_f32_32x32x16_bf16 v[0:15], v[184:187], a[120:123], v[0:15]\n"
    "ds_read_b64_tr_b16 v[180:181], v222 offset:60416\n"
    "ds_read_b64_tr_b16 v[182:183], v222 offset:60672\n"
    "v_mfma_f32_32x32x16_bf16 v[16:31], v[188:191], a[120:123], v[16:31]\n"
    "ds_read_b64_tr_b16 v[184:185], v223 offset:0\n"
    "ds_read_b64_tr_b16 v[186:187], v223 offset:256\n"
    "v_mfma_f32_32x32x16_bf16 v[0:15], v[192:195], a[124:127], v[0:15]\n"
    "ds_read_b64_tr_b16 v[188:189], v223 offset:1024\n"
    "ds_read_b64_tr_b16 v[190:191], v223 offset:1280\n"
    "v_mfma_f32_32x32x16_bf16 v[16:31], v[196:199], a[124:127], v[16:31]\n"
    "ds_read_b64_tr_b16 v[192:193], v223 offset:6144\n"
    "ds_read_b64_tr_b16 v[194:195], v223 offset:6400\n"
    "s_waitcnt lgkmcnt(6)\n"
    "v_mfma_f32_32x32x16_bf16 v[0:15], v[168:171], a[128:131], v[0:15]\n"
    "s_waitcnt vmcnt(24)\n"
    "s_barrier\n"
    "v_mfma_f32_32x32x16_bf16 v[16:31], v[172:175], a[128:131], v[16:31]\n"
    "ds_read_b64_tr_b16 v[196:197], v223 offset:7168\n"
    "ds_read_b64_tr_b16 v[198:199], v223 offset:7424\n"
    "ds_read_b64_tr_b16 v[168:169], v216 offset:0\n"
    "ds_read_b64_tr_b16 v[170:171], v216 offset:512\n"
    "s_add_u32 m0, s65, 98304\n"
    "s_add_u32 s84, s57, 1773824\n"
    "v_mfma_f32_32x32x16_bf16 v[0:15], v[176:179], a[132:135], v[0:15]\n"
    "ds_read_b64_tr_b16 v[172:173], v219 offset:0\n"
    "ds_read_b64_tr_b16 v[174:175], v219 offset:512\n"
    "buffer_load_dwordx4 v242, s[44:47], s84 offen lds\n"
    "v_mfma_f32_32x32x16_bf16 v[16:31], v[180:183], a[132:135], v[16:31]\n"
    "ds_read_b64_tr_b16 v[176:177], v216 offset:6144\n"
    "ds_read_b64_tr_b16 v[178:179], v216 offset:6656\n"
    "buffer_load_dwordx4 v225, s[44:47], s84 offen offset:1024 lds\n"
    "s_add_u32 s84, s57, 2361600\n"
    "s_waitcnt lgkmcnt(6)\n"
    "v_mfma_f32_32x32x16_bf16 v[0:15], v[184:187], a[136:139], v[0:15]\n"
    "ds_read_b64_tr_b16 v[180:181], v219 offset:6144\n"
    "ds_read_b64_tr_b16 v[182:183], v219 offset:6656\n"
    "buffer_load_dwordx4 v242, s[44:47], s84 offen offset:2048 lds\n"
    "v_mfma_f32_32x32x16_bf16 v[16:31], v[188:191], a[136:139], v[16:31]\n"
    "ds_read_b64_tr_b16 v[184:185], v216 offset:12288\n"
    "ds_read_b64_tr_b16 v[186:187], v216 offset:12800\n"
    "buffer_load_dwordx4 v225, s[44:47], s84 offen offset:3072 lds\n"
    "s_add_u32 m0, s65, 102400\n"
    "s_add_u32 s84, s57, 2953472\n"
    "v_mfma_f32_32x32x16_bf16 v[0:15], v[192:195], a[140:143], v[0:15]\n"
    "ds_read_b64_tr_b16 v[188:189], v219 offset:12288\n"
    "ds_read_b64_tr_b16 v[190:191], v219 offset:12800\n"
    "buffer_load_dwordx4 v242, s[44:47], s84 offen lds\n"
    "v_mfma_f32_32x32x16_bf16 v[16:31], v[196:199], a[140:143], v[16:31]\n"
    "ds_read_b64_tr_b16 v[192:193], v216 offset:18432\n"
    "ds_read_b64_tr_b16 v[194:195], v216 offset:18944\n"
    "buffer_load_dwordx4 v225, s[44:47], s84 offen offset:1024 lds\n"
    "s_waitcnt lgkmcnt(6)\n"
    "v_mfma_f32_32x32x16_bf16 v[32:47], v[168:171], a[0:3], 0\n"
    "ds_read_b64_tr_b16 v[196:197], v219 offset:18432\n"
    "ds_read_b64_tr_b16 v[198:199], v219 offset:18944\n"
    "s_add_u32 s86, s57, 4480\n"
    "buffer_load_dword v255, v251, s[44:47], s86 offen\n"
    "v_mfma_f32_32x32x16_bf16 v[48:63], v[172:175], a[0:3], 0\n"
    "ds_read_b64_tr_b16 v[168:169], v216 offset:2048\n"
    "ds_read_b64_tr_b16 v[170:171], v216 offset:2560\n"
    "v_mfma_f32_32x32x16_bf16 v[32:47], v[176:179], a[4:7], v[32:47]\n"
    "ds_read_b64_tr_b16 v[172:173], v219 offset:2048\n"
    "ds_read_b64_tr_b16 v[174:175], v219 offset:2560\n"
    "v_mul_f32 v200, v0, v0\n"
    "v_mul_f32 v201, v1, v1\n"
    "v_mul_f32 v202, v2, v2\n"
    "v_mul_f32 v203, v3, v3\n"
    "v_mfma_f32_32x32x16_bf16 v[48:63], v[180:183], a[4:7], v[48:63]\n"
    "ds_read_b64_tr_b16 v[176:177], v216 offset:8192\n"
    "ds_read_b64_tr_b16 v[178:179], v216 offset:8704\n"
    "v_fmaak_f32 v200, v200, v231, 0xc0135761\n"
    "v_fmaak_f32 v201, v201, v231, 0xc0135761\n"
    "v_fmaak_f32 v202, v202, v231, 0xc0135761\n"
    "s_waitcnt lgkmcnt(6)\n"
    "v_mfma_f32_32x32x16_bf16 v[32:47], v[184:187], a[8:11], v[32:47]\n"
    "ds_read_b64_tr_b16 v[180:181], v219 offset:8192\n"
    "ds_read_b64_tr_b16 v[182:183], v219 offset:8704\n"
    "v_fmaak_f32 v203, v203, v231, 0xc0135761\n"
    "v_mul_f32 v200, v200, v0\n"
    "v_mul_f32 v201, v201, v1\n"
    "v_mul_f32 v202, v202, v2\n"
    "v_mfma_f32_32x32x16_bf16 v[48:63], v[188:191], a[8:11], v[48:63]\n"
    "ds_read_b64_tr_b16 v[184:185], v216 offset:14336\n"
    "ds_read_b64_tr_b16 v[186:187], v216 offset:14848\n"
    "v_mul_f32 v203, v203, v3\n"
    "v_exp_f32 v200, v200\n"
    "v_mfma_f32_32x32x16_bf16 v[32:47], v[192:195], a[12:15], v[32:47]\n"
    "ds_read_b64_tr_b16 v[188:189], v219 offset:14336\n"
    "ds_read_b64_tr_b16 v[190:191], v219 offset:14848\n"
    "v_exp_f32 v201, v201\n"
    "v_exp_f32 v202, v202\n"
    "v_mfma_f32_32x32x16_bf16 v[48:63], v[196:199], a[12:15], v[48:63]\n"
    "ds_read_b64_tr_b16 v[192:193], v216 offset:20480\n"
    "ds_read_b64_tr_b16 v[194:195], v216 offset:20992\n"
    "v_exp_f32 v203, v203\n"
    "v_add_f32 v200, 1.0, v200\n"
    "s_waitcnt lgkmcnt(6)\n"
    "v_mfma_f32_32x32x16_bf16 v[32:47], v[168:171], a[16:19], v[32:47]\n"
    "ds_read_b64_tr_b16 v[196:197], v219 offset:20480\n"
    "ds_read_b64_tr_b16 v[198:199], v219 offset:20992\n"
    "v_add_f32 v201, 1.0, v201\n"
    "v_add_f32 v202, 1.0, v202\n"
    "v_add_f32 v203, 1.0, v203\n"
    "v_mfma_f32_32x32x16_bf16 v[48:63], v[172:175], a[16:19], v[48:63]\n"
    "ds_read_b64_tr_b16 v[168:169], v216 offset:4096\n"
    "ds_read_b64_tr_b16 v[170:171], v216 offset:4608\n"
    "v_rcp_f32 v200, v200\n"
    "v_rcp_f32 v201, v201\n"
    "v_mfma_f32_32x32x16_bf16 v[32:47], v[176:179], a[20:23], v[32:47]\n"
    "ds_read_b64_tr_b16 v[172:173], v219 offset:4096\n"
    "ds_read_b64_tr_b16 v[174:175], v219 offset:4608\n"
    "v_rcp_f32 v202, v202\n"
    "v_rcp_f32 v203, v203\n"
    "v_mfma_f32_32x32x16_bf16 v[48:63], v[180:183], a[20:23], v[48:63]\n"
    "ds_read_b64_tr_b16 v[176:177], v216 offset:10240\n"
    "ds_read_b64_tr_b16 v[178:179], v216 offset:10752\n"
    "v_mul_f32 v0, v0, v200\n"
    "v_mul_f32 v1, v1, v201\n"
    "v_mul_f32 v2, v2, v202\n"
    "s_waitcnt lgkmcnt(6)\n"
    "v_mfma_f32_32x32x16_bf16 v[32:47], v[184:187], a[24:27], v[32:47]\n"
    "ds_read_b64_tr_b16 v[180:181], v219 offset:10240\n"
    "ds_read_b64_tr_b16 v[182:183], v219 offset:10752\n"
    "v_mul_f32 v3, v3, v203\n"
    "v_mul_f32 v204, v4, v4\n"
    "v_mul_f32 v205, v5, v5\n"
    "v_mul_f32 v206, v6, v6\n"
    "v_mfma_f32_32x32x16_bf16 v[48:63], v[188:191], a[24:27], v[48:63]\n"
    "ds_read_b64_tr_b16 v[184:185], v216 offset:16384\n"
    "ds_read_b64_tr_b16 v[186:187], v216 offset:16896\n"
    "v_mul_f32 v207, v7, v7\n"
    "v_fmaak_f32 v204, v204, v231, 0xc0135761\n"
    "v_fmaak_f32 v205, v205, v231, 0xc0135761\n"
    "v_fmaak_f32 v206, v206, v231, 0xc0135761\n"
    "v_mfma_f32_32x32x16_bf16 v[32:47], v[192:195], a[28:31], v[32:47]\n"
    "ds_read_b64_tr_b16 v[188:189], v219 offset:16384\n"
    "ds_read_b64_tr_b16 v[190:191], v219 offset:16896\n"
    "v_fmaak_f32 v207, v207, v231, 0xc0135761\n"
    "v_mul_f32 v204, v204, v4\n"
    "v_mul_f32 v205, v205, v5\n"
    "v_mul_f32 v206, v206, v6\n"
    "v_mfma_f32_32x32x16_bf16 v[48:63], v[196:199], a[28:31], v[48:63]\n"
    "ds_read_b64_tr_b16 v[192:193], v216 offset:22528\n"
    "ds_read_b64_tr_b16 v[194:195], v216 offset:23040\n"
    "v_mul_f32 v207, v207, v7\n"
    "v_exp_f32 v204, v204\n"
    "s_waitcnt lgkmcnt(6)\n"
    "v_mfma_f32_32x32x16_bf16 v[32:47], v[168:171], a[32:35], v[32:47]\n"
    "s_waitcnt vmcnt(25)\n"
    "s_barrier\n"
    "v_mfma_f32_32x32x16_bf16 v[48:63], v[172:175], a[32:35], v[48:63]\n"
    "ds_read_b64_tr_b16 v[196:197], v219 offset:22528\n"
    "ds_read_b64_tr_b16 v[198:199], v219 offset:23040\n"
    "ds_read_b64_tr_b16 v[168:169], v218 offset:8192\n"
    "ds_read_b64_tr_b16 v[170:171], v218 offset:8704\n"
    "s_add_u32 m0, s65, 49152\n"
    "s_add_u32 s84, s57, 3543296\n"
    "v_mfma_f32_32x32x16_bf16 v[32:47], v[176:179], a[36:39], v[32:47]\n"
    "ds_read_b64_tr_b16 v[172:173], v221 offset:8192\n"
    "ds_read_b64_tr_b16 v[174:175], v221 offset:8704\n"
    "buffer_load_dwordx4 v242, s[44:47], s84 offen lds\n"
    "v_mfma_f32_32x32x16_bf16 v[48:63], v[180:183], a[36:39], v[48:63]\n"
    "ds_read_b64_tr_b16 v[176:177], v218 offset:14336\n"
    "ds_read_b64_tr_b16 v[178:179], v218 offset:14848\n"
    "buffer_load_dwordx4 v225, s[44:47], s84 offen offset:1024 lds\n"
    "s_add_u32 s84, s57, 4131072\n"
    "s_waitcnt lgkmcnt(6)\n"
    "v_mfma_f32_32x32x16_bf16 v[32:47], v[184:187], a[40:43], v[32:47]\n"
    "ds_read_b64_tr_b16 v[180:181], v221 offset:14336\n"
    "ds_read_b64_tr_b16 v[182:183], v221 offset:14848\n"
    "buffer_load_dwordx4 v242, s[44:47], s84 offen offset:2048 lds\n"
    "v_mfma_f32_32x32x16_bf16 v[48:63], v[188:191], a[40:43], v[48:63]\n"
    "ds_read_b64_tr_b16 v[184:185], v218 offset:20480\n"
    "ds_read_b64_tr_b16 v[186:187], v218 offset:20992\n"
    "buffer_load_dwordx4 v225, s[44:47], s84 offen offset:3072 lds\n"
    "s_add_u32 m0, s65, 53248\n"
    "s_add_u32 s84, s57, 4722944\n"
    "v_mfma_f32_32x32x16_bf16 v[32:47], v[192:195], a[44:47], v[32:47]\n"
    "ds_read_b64_tr_b16 v[188:189], v221 offset:20480\n"
    "ds_read_b64_tr_b16 v[190:191], v221 offset:20992\n"
    "buffer_load_dwordx4 v242, s[44:47], s84 offen lds\n"
    "v_mfma_f32_32x32x16_bf16 v[48:63], v[196:199], a[44:47], v[48:63]\n"
    "ds_read_b64_tr_b16 v[192:193], v218 offset:26624\n"
    "ds_read_b64_tr_b16 v[194:195], v218 offset:27136\n"
    "buffer_load_dwordx4 v225, s[44:47], s84 offen offset:1024 lds\n"
    "s_waitcnt lgkmcnt(6)\n"
    "v_mfma_f32_32x32x16_bf16 v[32:47], v[168:171], a[48:51], v[32:47]\n"
    "ds_read_b64_tr_b16 v[196:197], v221 offset:26624\n"
    "ds_read_b64_tr_b16 v[198:199], v221 offset:27136\n"
    "v_exp_f32 v205, v205\n"
    "v_exp_f32 v206, v206\n"
    "v_mfma_f32_32x32x16_bf16 v[48:63], v[172:175], a[48:51], v[48:63]\n"
    "ds_read_b64_tr_b16 v[168:169], v218 offset:10240\n"
    "ds_read_b64_tr_b16 v[170:171], v218 offset:10752\n"
    "v_exp_f32 v207, v207\n"
    "v_add_f32 v204, 1.0, v204\n"
    "v_add_f32 v205, 1.0, v205\n"
    "v_mfma_f32_32x32x16_bf16 v[32:47], v[176:179], a[52:55], v[32:47]\n"
    "ds_read_b64_tr_b16 v[172:173], v221 offset:10240\n"
    "ds_read_b64_tr_b16 v[174:175], v221 offset:10752\n"
    "v_add_f32 v206, 1.0, v206\n"
    "v_add_f32 v207, 1.0, v207\n"
    "v_rcp_f32 v204, v204\n"
    "v_mfma_f32_32x32x16_bf16 v[48:63], v[180:183], a[52:55], v[48:63]\n"
    "ds_read_b64_tr_b16 v[176:177], v218 offset:16384\n"
    "ds_read_b64_tr_b16 v[178:179], v218 offset:16896\n"
    "v_rcp_f32 v205, v205\n"
    "s_waitcnt lgkmcnt(6)\n"
    "v_mfma_f32_32x32x16_bf16 v[32:47], v[184:187], a[56:59], v[32:47]\n"
    "ds_read_b64_tr_b16 v[180:181], v221 offset:16384\n"
    "ds_read_b64_tr_b16 v[182:183], v221 offset:16896\n"
    "v_rcp_f32 v206, v206\n"
    "v_rcp_f32 v207, v207\n"
    "v_mfma_f32_32x32x16_bf16 v[48:63], v[188:191], a[56:59], v[48:63]\n"
    "ds_read_b64_tr_b16 v[184:185], v218 offset:22528\n"
    "ds_read_b64_tr_b16 v[186:187], v218 offset:23040\n"
    "v_mul_f32 v4, v4, v204\n"
    "v_mul_f32 v5, v5, v205\n"
    "v_mul_f32 v6, v6, v206\n"
    "v_mul_f32 v7, v7, v207\n"
    "v_mfma_f32_32x32x16_bf16 v[32:47], v[192:195], a[60:63], v[32:47]\n"
    "ds_read_b64_tr_b16 v[188:189], v221 offset:22528\n"
    "ds_read_b64_tr_b16 v[190:191], v221 offset:23040\n"
    "v_mul_f32 v200, v8, v8\n"
    "v_mul_f32 v201, v9, v9\n"
    "v_mul_f32 v202, v10, v10\n"
    "v_mul_f32 v203, v11, v11\n"
    "v_mfma_f32_32x32x16_bf16 v[48:63], v[196:199], a[60:63], v[48:63]\n"
    "ds_read_b64_tr_b16 v[192:193], v218 offset:28672\n"
    "ds_read_b64_tr_b16 v[194:195], v218 offset:29184\n"
    "v_fmaak_f32 v200, v200, v231, 0xc0135761\n"
    "v_fmaak_f32 v201, v201, v231, 0xc0135761\n"
    "v_fmaak_f32 v202, v202, v231, 0xc0135761\n"
    "s_waitcnt lgkmcnt(6)\n"
    "v_mfma_f32_32x32x16_bf16 v[32:47], v[168:171], a[64:67], v[32:47]\n"
    "ds_read_b64_tr_b16 v[196:197], v221 offset:28672\n"
    "ds_read_b64_tr_b16 v[198:199], v221 offset:29184\n"
    "v_fmaak_f32 v203, v203, v231, 0xc0135761\n"
    "v_mul_f32 v200, v200, v8\n"
    "v_mul_f32 v201, v201, v9\n"
    "v_mul_f32 v202, v202, v10\n"
    "v_mfma_f32_32x32x16_bf16 v[48:63], v[172:175], a[64:67], v[48:63]\n"
    "ds_read_b64_tr_b16 v[168:169], v218 offset:12288\n"
    "ds_read_b64_tr_b16 v[170:171], v218 offset:12800\n"
    "v_mul_f32 v203, v203, v11\n"
    "v_exp_f32 v200, v200\n"
    "v_mfma_f32_32x32x16_bf16 v[32:47], v[176:179], a[68:71], v[32:47]\n"
    "ds_read_b64_tr_b16 v[172:173], v221 offset:12288\n"
    "ds_read_b64_tr_b16 v[174:175], v221 offset:12800\n"
    "v_exp_f32 v201, v201\n"
    "v_exp_f32 v202, v202\n"
    "v_mfma_f32_32x32x16_bf16 v[48:63], v[180:183], a[68:71], v[48:63]\n"
    "ds_read_b64_tr_b16 v[176:177], v218 offset:18432\n"
    "ds_read_b64_tr_b16 v[178:179], v218 offset:18944\n"
    "v_exp_f32 v203, v203\n"
    "v_add_f32 v200, 1.0, v200\n"
    "s_waitcnt lgkmcnt(6)\n"
    "v_mfma_f32_32x32x16_bf16 v[32:47], v[184:187], a[72:75], v[32:47]\n"
    "ds_read_b64_tr_b16 v[180:181], v221 offset:18432\n"
    "ds_read_b64_tr_b16 v[182:183], v221 offset:18944\n"
    "v_add_f32 v201, 1.0, v201\n"
    "v_add_f32 v202, 1.0, v202\n"
    "v_add_f32 v203, 1.0, v203\n"
    "v_mfma_f32_32x32x16_bf16 v[48:63], v[188:191], a[72:75], v[48:63]\n"
    "ds_read_b64_tr_b16 v[184:185], v218 offset:24576\n"
    "ds_read_b64_tr_b16 v[186:187], v218 offset:25088\n"
    "v_rcp_f32 v200, v200\n"
    "v_rcp_f32 v201, v201\n"
    "v_mfma_f32_32x32x16_bf16 v[32:47], v[192:195], a[76:79], v[32:47]\n"
    "ds_read_b64_tr_b16 v[188:189], v221 offset:24576\n"
    "ds_read_b64_tr_b16 v[190:191], v221 offset:25088\n"
    "v_rcp_f32 v202, v202\n"
    "v_rcp_f32 v203, v203\n"
    "v_mfma_f32_32x32x16_bf16 v[48:63], v[196:199], a[76:79], v[48:63]\n"
    "ds_read_b64_tr_b16 v[192:193], v218 offset:30720\n"
    "ds_read_b64_tr_b16 v[194:195], v218 offset:31232\n"
    "v_mul_f32 v8, v8, v200\n"
    "v_mul_f32 v9, v9, v201\n"
    "v_mul_f32 v10, v10, v202\n"
    "s_waitcnt lgkmcnt(6)\n"
    "v_mfma_f32_32x32x16_bf16 v[32:47], v[168:171], a[80:83], v[32:47]\n"
    "s_waitcnt vmcnt(23)\n"
    "s_barrier\n"
    "v_mfma_f32_32x32x16_bf16 v[48:63], v[172:175], a[80:83], v[48:63]\n"
    "ds_read_b64_tr_b16 v[196:197], v221 offset:30720\n"
    "ds_read_b64_tr_b16 v[198:199], v221 offset:31232\n"
    "ds_read_b64_tr_b16 v[168:169], v216 offset:24576\n"
    "ds_read_b64_tr_b16 v[170:171], v216 offset:25088\n"
    "s_add_u32 m0, s65, 0\n"
    "s_add_u32 s84, s57, 4480\n"
    "v_mfma_f32_32x32x16_bf16 v[32:47], v[176:179], a[84:87], v[32:47]\n"
    "ds_read_b64_tr_b16 v[172:173], v219 offset:24576\n"
    "ds_read_b64_tr_b16 v[174:175], v219 offset:25088\n"
    "buffer_load_dwordx4 v242, s[40:43], s84 offen lds\n"
    "v_mul_f32 v11, v11, v203\n"
    "v_mfma_f32_32x32x16_bf16 v[48:63], v[180:183], a[84:87], v[48:63]\n"
    "ds_read_b64_tr_b16 v[176:177], v216 offset:30720\n"
    "ds_read_b64_tr_b16 v[178:179], v216 offset:31232\n"
    "buffer_load_dwordx4 v225, s[40:43], s84 offen offset:1024 lds\n"
    "s_add_u32 s84, s57, 592256\n"
    "s_waitcnt lgkmcnt(6)\n"
    "v_mfma_f32_32x32x16_bf16 v[32:47], v[184:187], a[88:91], v[32:47]\n"
    "ds_read_b64_tr_b16 v[180:181], v219 offset:30720\n"
    "ds_read_b64_tr_b16 v[182:183], v219 offset:31232\n"
    "buffer_load_dwordx4 v242, s[40:43], s84 offen offset:2048 lds\n"
    "v_mul_f32 v204, v12, v12\n"
    "v_mfma_f32_32x32x16_bf16 v[48:63], v[188:191], a[88:91], v[48:63]\n"
    "ds_read_b64_tr_b16 v[184:185], v216 offset:36864\n"
    "ds_read_b64_tr_b16 v[186:187], v216 offset:37376\n"
    "buffer_load_dwordx4 v225, s[40:43], s84 offen offset:3072 lds\n"
    "s_add_u32 m0, s65, 4096\n"
    "s_add_u32 s84, s57, 1184128\n"
    "v_mfma_f32_32x32x16_bf16 v[32:47], v[192:195], a[92:95], v[32:47]\n"
    "ds_read_b64_tr_b16 v[188:189], v219 offset:36864\n"
    "ds_read_b64_tr_b16 v[190:191], v219 offset:37376\n"
    "buffer_load_dwordx4 v242, s[40:43], s84 offen lds\n"
    "v_mul_f32 v205, v13, v13\n"
    "v_mfma_f32_32x32x16_bf16 v[48:63], v[196:199], a[92:95], v[48:63]\n"
    "ds_read_b64_tr_b16 v[192:193], v216 offset:43008\n"
    "ds_read_b64_tr_b16 v[194:195], v216 offset:43520\n"
    "buffer_load_dwordx4 v225, s[40:43], s84 offen offset:1024 lds\n"
    "s_waitcnt lgkmcnt(6)\n"
    "v_mfma_f32_32x32x16_bf16 v[32:47], v[168:171], a[96:99], v[32:47]\n"
    "ds_read_b64_tr_b16 v[196:197], v219 offset:43008\n"
    "ds_read_b64_tr_b16 v[198:199], v219 offset:43520\n"
    "v_mul_f32 v206, v14, v14\n"
    "v_mul_f32 v207, v15, v15\n"
    "v_fmaak_f32 v204, v204, v231, 0xc0135761\n"
    "v_fmaak_f32 v205, v205, v231, 0xc0135761\n"
    "v_mfma_f32_32x32x16_bf16 v[48:63], v[172:175], a[96:99], v[48:63]\n"
    "ds_read_b64_tr_b16 v[168:169], v216 offset:26624\n"
    "ds_read_b64_tr_b16 v[170:171], v216 offset:27136\n"
    "v_fmaak_f32 v206, v206, v231, 0xc0135761\n"
    "v_fmaak_f32 v207, v207, v231, 0xc0135761\n"
    "v_mul_f32 v204, v204, v12\n"
    "v_mul_f32 v205, v205, v13\n"
    "v_mfma_f32_32x32x16_bf16 v[32:47], v[176:179], a[100:103], v[32:47]\n"
    "ds_read_b64_tr_b16 v[172:173], v219 offset:26624\n"
    "ds_read_b64_tr_b16 v[174:175], v219 offset:27136\n"
    "v_mul_f32 v206, v206, v14\n"
    "v_mul_f32 v207, v207, v15\n"
    "v_exp_f32 v204, v204\n"
    "v_mfma_f32_32x32x16_bf16 v[48:63], v[180:183], a[100:103], v[48:63]\n"
    "ds_read_b64_tr_b16 v[176:177], v216 offset:32768\n"
    "ds_read_b64_tr_b16 v[178:179], v216 offset:33280\n"
    "v_exp_f32 v205, v205\n"
    "s_waitcnt lgkmcnt(6)\n"
    "v_mfma_f32_32x32x16_bf16 v[32:47], v[184:187], a[104:107], v[32:47]\n"
    "ds_read_b64_tr_b16 v[180:181], v219 offset:32768\n"
    "ds_read_b64_tr_b16 v[182:183], v219 offset:33280\n"
    "v_exp_f32 v206, v206\n"
    "v_exp_f32 v207, v207\n"
    "v_mfma_f32_32x32x16_bf16 v[48:63], v[188:191], a[104:107], v[48:63]\n"
    "ds_read_b64_tr_b16 v[184:185], v216 offset:38912\n"
    "ds_read_b64_tr_b16 v[186:187], v216 offset:39424\n"
    "v_add_f32 v204, 1.0, v204\n"
    "v_add_f32 v205, 1.0, v205\n"
    "v_add_f32 v206, 1.0, v206\n"
    "v_add_f32 v207, 1.0, v207\n"
    "v_mfma_f32_32x32x16_bf16 v[32:47], v[192:195], a[108:111], v[32:47]\n"
    "ds_read_b64_tr_b16 v[188:189], v219 offset:38912\n"
    "ds_read_b64_tr_b16 v[190:191], v219 offset:39424\n"
    "v_rcp_f32 v204, v204\n"
    "v_rcp_f32 v205, v205\n"
    "v_mfma_f32_32x32x16_bf16 v[48:63], v[196:199], a[108:111], v[48:63]\n"
    "ds_read_b64_tr_b16 v[192:193], v216 offset:45056\n"
    "ds_read_b64_tr_b16 v[194:195], v216 offset:45568\n"
    "v_rcp_f32 v206, v206\n"
    "s_waitcnt lgkmcnt(6)\n"
    "v_mfma_f32_32x32x16_bf16 v[32:47], v[168:171], a[112:115], v[32:47]\n"
    "ds_read_b64_tr_b16 v[196:197], v219 offset:45056\n"
    "ds_read_b64_tr_b16 v[198:199], v219 offset:45568\n"
    "v_rcp_f32 v207, v207\n"
    "v_mul_f32 v12, v12, v204\n"
    "v_mul_f32 v13, v13, v205\n"
    "v_mfma_f32_32x32x16_bf16 v[48:63], v[172:175], a[112:115], v[48:63]\n"
    "ds_read_b64_tr_b16 v[168:169], v216 offset:28672\n"
    "ds_read_b64_tr_b16 v[170:171], v216 offset:29184\n"
    "v_mul_f32 v14, v14, v206\n"
    "v_mul_f32 v15, v15, v207\n"
    "v_mul_f32 v0, v0, v16\n"
    "v_mul_f32 v1, v1, v17\n"
    "v_mfma_f32_32x32x16_bf16 v[32:47], v[176:179], a[116:119], v[32:47]\n"
    "ds_read_b64_tr_b16 v[172:173], v219 offset:28672\n"
    "ds_read_b64_tr_b16 v[174:175], v219 offset:29184\n"
    "v_mul_f32 v2, v2, v18\n"
    "v_mul_f32 v3, v3, v19\n"
    "v_mul_f32 v4, v4, v20\n"
    "v_mul_f32 v5, v5, v21\n"
    "v_mfma_f32_32x32x16_bf16 v[48:63], v[180:183], a[116:119], v[48:63]\n"
    "ds_read_b64_tr_b16 v[176:177], v216 offset:34816\n"
    "ds_read_b64_tr_b16 v[178:179], v216 offset:35328\n"
    "v_mul_f32 v6, v6, v22\n"
    "v_mul_f32 v7, v7, v23\n"
    "v_cvt_pk_bf16_f32 v160, v0, v1\n"
    "s_waitcnt lgkmcnt(6)\n"
    "v_mfma_f32_32x32x16_bf16 v[32:47], v[184:187], a[120:123], v[32:47]\n"
    "ds_read_b64_tr_b16 v[180:181], v219 offset:34816\n"
    "ds_read_b64_tr_b16 v[182:183], v219 offset:35328\n"
    "v_cvt_pk_bf16_f32 v161, v2, v3\n"
    "v_cvt_pk_bf16_f32 v162, v4, v5\n"
    "v_cvt_pk_bf16_f32 v163, v6, v7\n"
    "v_mfma_f32_32x32x16_bf16 v[48:63], v[188:191], a[120:123], v[48:63]\n"
    "ds_read_b64_tr_b16 v[184:185], v216 offset:40960\n"
    "ds_read_b64_tr_b16 v[186:187], v216 offset:41472\n"
    "s_add_u32 s85, s67, 16384\n"
    "buffer_store_dwordx4 v[160:163], v250, s[52:55], s85 offen sc1\n"
    "v_mfma_f32_32x32x16_bf16 v[32:47], v[192:195], a[124:127], v[32:47]\n"
    "ds_read_b64_tr_b16 v[188:189], v219 offset:40960\n"
    "ds_read_b64_tr_b16 v[190:191], v219 offset:41472\n"
    "v_mul_f32 v8, v8, v24\n"
    "v_mul_f32 v9, v9, v25\n"
    "v_mul_f32 v10, v10, v26\n"
    "v_mul_f32 v11, v11, v27\n"
    "v_mfma_f32_32x32x16_bf16 v[48:63], v[196:199], a[124:127], v[48:63]\n"
    "ds_read_b64_tr_b16 v[192:193], v216 offset:47104\n"
    "ds_read_b64_tr_b16 v[194:195], v216 offset:47616\n"
    "v_mul_f32 v12, v12, v28\n"
    "v_mul_f32 v13, v13, v29\n"
    "v_mul_f32 v14, v14, v30\n"
    "s_waitcnt lgkmcnt(6)\n"
    "v_mfma_f32_32x32x16_bf16 v[32:47], v[168:171], a[128:131], v[32:47]\n"
    "s_waitcnt vmcnt(23)\n"
    "s_barrier\n"
    "v_mfma_f32_32x32x16_bf16 v[48:63], v[172:175], a[128:131], v[48:63]\n"
    "ds_read_b64_tr_b16 v[196:197], v219 offset:47104\n"
    "ds_read_b64_tr_b16 v[198:199], v219 offset:47616\n"
    "ds_read_b64_tr_b16 v[168:169], v217 offset:8192\n"
    "ds_read_b64_tr_b16 v[170:171], v217 offset:8704\n"
    "s_add_u32 m0, s65, 139264\n"
    "s_add_u32 s84, s57, 1773952\n"
    "v_mfma_f32_32x32x16_bf16 v[32:47], v[176:179], a[132:135], v[32:47]\n"
    "ds_read_b64_tr_b16 v[172:173], v220 offset:8192\n"
    "ds_read_b64_tr_b16 v[174:175], v220 offset:8704\n"
    "buffer_load_dwordx4 v242, s[40:43], s84 offen lds\n"
    "v_mul_f32 v15, v15, v31\n"
    "v_mfma_f32_32x32x16_bf16 v[48:63], v[180:183], a[132:135], v[48:63]\n"
    "ds_read_b64_tr_b16 v[176:177], v217 offset:14336\n"
    "ds_read_b64_tr_b16 v[178:179], v217 offset:14848\n"
    "buffer_load_dwordx4 v225, s[40:43], s84 offen offset:1024 lds\n"
    "s_add_u32 s84, s57, 2361728\n"
    "s_waitcnt lgkmcnt(6)\n"
    "v_mfma_f32_32x32x16_bf16 v[32:47], v[184:187], a[136:139], v[32:47]\n"
    "ds_read_b64_tr_b16 v[180:181], v220 offset:14336\n"
    "ds_read_b64_tr_b16 v[182:183], v220 offset:14848\n"
    "buffer_load_dwordx4 v242, s[40:43], s84 offen offset:2048 lds\n"
    "v_cvt_pk_bf16_f32 v164, v8, v9\n"
    "v_mfma_f32_32x32x16_bf16 v[48:63], v[188:191], a[136:139], v[48:63]\n"
    "ds_read_b64_tr_b16 v[184:185], v217 offset:20480\n"
    "ds_read_b64_tr_b16 v[186:187], v217 offset:20992\n"
    "buffer_load_dwordx4 v225, s[40:43], s84 offen offset:3072 lds\n"
    "s_add_u32 m0, s65, 143360\n"
    "s_add_u32 s84, s57, 2953600\n"
    "v_mfma_f32_32x32x16_bf16 v[32:47], v[192:195], a[140:143], v[32:47]\n"
    "ds_read_b64_tr_b16 v[188:189], v220 offset:20480\n"
    "ds_read_b64_tr_b16 v[190:191], v220 offset:20992\n"
    "buffer_load_dwordx4 v242, s[40:43], s84 offen lds\n"
    "v_cvt_pk_bf16_f32 v165, v10, v11\n"
    "v_mfma_f32_32x32x16_bf16 v[48:63], v[196:199], a[140:143], v[48:63]\n"
    "ds_read_b64_tr_b16 v[192:193], v217 offset:26624\n"
    "ds_read_b64_tr_b16 v[194:195], v217 offset:27136\n"
    "buffer_load_dwordx4 v225, s[40:43], s84 offen offset:1024 lds\n"
    "s_waitcnt lgkmcnt(6)\n"
    "v_mfma_f32_32x32x16_bf16 v[64:79], v[168:171], a[0:3], 0\n"
    "ds_read_b64_tr_b16 v[196:197], v220 offset:26624\n"
    "ds_read_b64_tr_b16 v[198:199], v220 offset:27136\n"
    "s_add_u32 s86, s73, 4096\n"
    "buffer_load_dword v255, v252, s[48:51], s86 offen\n"
    "v_mfma_f32_32x32x16_bf16 v[80:95], v[172:175], a[0:3], 0\n"
    "ds_read_b64_tr_b16 v[168:169], v217 offset:10240\n"
    "ds_read_b64_tr_b16 v[170:171], v217 offset:10752\n"
    "v_cvt_pk_bf16_f32 v166, v12, v13\n"
    "v_cvt_pk_bf16_f32 v167, v14, v15\n"
    "v_mfma_f32_32x32x16_bf16 v[64:79], v[176:179], a[4:7], v[64:79]\n"
    "ds_read_b64_tr_b16 v[172:173], v220 offset:10240\n"
    "ds_read_b64_tr_b16 v[174:175], v220 offset:10752\n"
    "s_add_u32 s85, s67, 17408\n"
    "buffer_store_dwordx4 v[164:167], v250, s[52:55], s85 offen sc1\n"
    "v_mfma_f32_32x32x16_bf16 v[80:95], v[180:183], a[4:7], v[80:95]\n"
    "ds_read_b64_tr_b16 v[176:177], v217 offset:16384\n"
    "ds_read_b64_tr_b16 v[178:179], v217 offset:16896\n"
    "v_mul_f32 v200, v32, v32\n"
    "v_mul_f32 v201, v33, v33\n"
    "v_mul_f32 v202, v34, v34\n"
    "s_waitcnt lgkmcnt(6)\n"
    "v_mfma_f32_32x32x16_bf16 v[64:79], v[184:187], a[8:11], v[64:79]\n"
    "ds_read_b64_tr_b16 v[180:181], v220 offset:16384\n"
    "ds_read_b64_tr_b16 v[182:183], v220 offset:16896\n"
    "v_mul_f32 v203, v35, v35\n"
    "v_fmaak_f32 v200, v200, v231, 0xc0135761\n"
    "v_fmaak_f32 v201, v201, v231, 0xc0135761\n"
    "v_fmaak_f32 v202, v202, v231, 0xc0135761\n"
    "v_mfma_f32_32x32x16_bf16 v[80:95], v[188:191], a[8:11], v[80:95]\n"
    "ds_read_b64_tr_b16 v[184:185], v217 offset:22528\n"
    "ds_read_b64_tr_b16 v[186:187], v217 offset:23040\n"
    "v_fmaak_f32 v203, v203, v231, 0xc0135761\n"
    "v_mul_f32 v200, v200, v32\n"
    "v_mul_f32 v201, v201, v33\n"
    "v_mul_f32 v202, v202, v34\n"
    "v_mfma_f32_32x32x16_bf16 v[64:79], v[192:195], a[12:15], v[64:79]\n"
    "ds_read_b64_tr_b16 v[188:189], v220 offset:22528\n"
    "ds_read_b64_tr_b16 v[190:191], v220 offset:23040\n"
    "v_mul_f32 v203, v203, v35\n"
    "v_exp_f32 v200, v200\n"
    "v_mfma_f32_32x32x16_bf16 v[80:95], v[196:199], a[12:15], v[80:95]\n"
    "ds_read_b64_tr_b16 v[192:193], v217 offset:28672\n"
    "ds_read_b64_tr_b16 v[194:195], v217 offset:29184\n"
    "v_exp_f32 v201, v201\n"
    "s_waitcnt lgkmcnt(6)\n"
    "v_mfma_f32_32x32x16_bf16 v[64:79], v[168:171], a[16:19], v[64:79]\n"
    "ds_read_b64_tr_b16 v[196:197], v220 offset:28672\n"
    "ds_read_b64_tr_b16 v[198:199], v220 offset:29184\n"
    "v_exp_f32 v202, v202\n"
    "v_exp_f32 v203, v203\n"
    "v_mfma_f32_32x32x16_bf16 v[80:95], v[172:175], a[16:19], v[80:95]\n"
    "ds_read_b64_tr_b16 v[168:169], v217 offset:12288\n"
    "ds_read_b64_tr_b16 v[170:171], v217 offset:12800\n"
    "v_add_f32 v200, 1.0, v200\n"
    "v_add_f32 v201, 1.0, v201\n"
    "v_add_f32 v202, 1.0, v202\n"
    "v_add_f32 v203, 1.0, v203\n"
    "v_mfma_f32_32x32x16_bf16 v[64:79], v[176:179], a[20:23], v[64:79]\n"
    "ds_read_b64_tr_b16 v[172:173], v220 offset:12288\n"
    "ds_read_b64_tr_b16 v[174:175], v220 offset:12800\n"
    "v_rcp_f32 v200, v200\n"
    "v_rcp_f32 v201, v201\n"
    "v_mfma_f32_32x32x16_bf16 v[80:95], v[180:183], a[20:23], v[80:95]\n"
    "ds_read_b64_tr_b16 v[176:177], v217 offset:18432\n"
    "ds_read_b64_tr_b16 v[178:179], v217 offset:18944\n"
    "v_rcp_f32 v202, v202\n"
    "s_waitcnt lgkmcnt(6)\n"
    "v_mfma_f32_32x32x16_bf16 v[64:79], v[184:187], a[24:27], v[64:79]\n"
    "ds_read_b64_tr_b16 v[180:181], v220 offset:18432\n"
    "ds_read_b64_tr_b16 v[182:183], v220 offset:18944\n"
    "v_rcp_f32 v203, v203\n"
    "v_mul_f32 v32, v32, v200\n"
    "v_mul_f32 v33, v33, v201\n"
    "v_mfma_f32_32x32x16_bf16 v[80:95], v[188:191], a[24:27], v[80:95]\n"
    "ds_read_b64_tr_b16 v[184:185], v217 offset:24576\n"
    "ds_read_b64_tr_b16 v[186:187], v217 offset:25088\n"
    "v_mul_f32 v34, v34, v202\n"
    "v_mul_f32 v35, v35, v203\n"
    "v_mul_f32 v204, v36, v36\n"
    "v_mul_f32 v205, v37, v37\n"
    "v_mfma_f32_32x32x16_bf16 v[64:79], v[192:195], a[28:31], v[64:79]\n"
    "ds_read_b64_tr_b16 v[188:189], v220 offset:24576\n"
    "ds_read_b64_tr_b16 v[190:191], v220 offset:25088\n"
    "v_mul_f32 v206, v38, v38\n"
    "v_mul_f32 v207, v39, v39\n"
    "v_fmaak_f32 v204, v204, v231, 0xc0135761\n"
    "v_fmaak_f32 v205, v205, v231, 0xc0135761\n"
    "v_mfma_f32_32x32x16_bf16 v[80:95], v[196:199], a[28:31], v[80:95]\n"
    "ds_read_b64_tr_b16 v[192:193], v217 offset:30720\n"
    "ds_read_b64_tr_b16 v[194:195], v217 offset:31232\n"
    "v_fmaak_f32 v206, v206, v231, 0xc0135761\n"
    "v_fmaak_f32 v207, v207, v231, 0xc0135761\n"
    "v_mul_f32 v204, v204, v36\n"
    "s_waitcnt lgkmcnt(6)\n"
    "v_mfma_f32_32x32x16_bf16 v[64:79], v[168:171], a[32:35], v[64:79]\n"
    "s_waitcnt vmcnt(22)\n"
    "s_barrier\n"
    "v_mfma_f32_32x32x16_bf16 v[80:95], v[172:175], a[32:35], v[80:95]\n"
    "ds_read_b64_tr_b16 v[196:197], v220 offset:30720\n"
    "ds_read_b64_tr_b16 v[198:199], v220 offset:31232\n"
    "ds_read_b64_tr_b16 v[168:169], v217 offset:32768\n"
    "ds_read_b64_tr_b16 v[170:171], v217 offset:33280\n"
    "s_add_u32 m0, s65, 24576\n"
    "s_add_u32 s84, s57, 3543424\n"
    "v_mfma_f32_32x32x16_bf16 v[64:79], v[176:179], a[36:39], v[64:79]\n"
    "ds_read_b64_tr_b16 v[172:173], v220 offset:32768\n"
    "ds_read_b64_tr_b16 v[174:175], v220 offset:33280\n"
    "buffer_load_dwordx4 v242, s[40:43], s84 offen lds\n"
    "v_mul_f32 v205, v205, v37\n"
    "v_mfma_f32_32x32x16_bf16 v[80:95], v[180:183], a[36:39], v[80:95]\n"
    "ds_read_b64_tr_b16 v[176:177], v217 offset:38912\n"
    "ds_read_b64_tr_b16 v[178:179], v217 offset:39424\n"
    "buffer_load_dwordx4 v225, s[40:43], s84 offen offset:1024 lds\n"
    "s_add_u32 s84, s57, 4131200\n"
    "v_mul_f32 v206, v206, v38\n"
    "s_waitcnt lgkmcnt(6)\n"
    "v_mfma_f32_32x32x16_bf16 v[64:79], v[184:187], a[40:43], v[64:79]\n"
    "ds_read_b64_tr_b16 v[180:181], v220 offset:38912\n"
    "ds_read_b64_tr_b16 v[182:183], v220 offset:39424\n"
    "buffer_load_dwordx4 v242, s[40:43], s84 offen offset:2048 lds\n"
    "v_mul_f32 v207, v207, v39\n"
    "v_exp_f32 v204, v204\n"
    "v_mfma_f32_32x32x16_bf16 v[80:95], v[188:191], a[40:43], v[80:95]\n"
    "ds_read_b64_tr_b16 v[184:185], v217 offset:45056\n"
    "ds_read_b64_tr_b16 v[186:187], v217 offset:45568\n"
    "buffer_load_dwordx4 v225, s[40:43], s84 offen offset:3072 lds\n"
    "s_add_u32 m0, s65, 28672\n"
    "s_add_u32 s84, s57, 4723072\n"
    "v_exp_f32 v205, v205\n"
    "v_mfma_f32_32x32x16_bf16 v[64:79], v[192:195], a[44:47], v[64:79]\n"
    "ds_read_b64_tr_b16 v[188:189], v220 offset:45056\n"
    "ds_read_b64_tr_b16 v[190:191], v220 offset:45568\n"
    "buffer_load_dwordx4 v242, s[40:43], s84 offen lds\n"
    "v_exp_f32 v206, v206\n"
    "v_mfma_f32_32x32x16_bf16 v[80:95], v[196:199], a[44:47], v[80:95]\n"
    "ds_read_b64_tr_b16 v[192:193], v217 offset:51200\n"
    "ds_read_b64_tr_b16 v[194:195], v217 offset:51712\n"
    "buffer_load_dwordx4 v225, s[40:43], s84 offen offset:1024 lds\n"
    "v_exp_f32 v207, v207\n"
    "v_add_f32 v204, 1.0, v204\n"
    "v_add_f32 v205, 1.0, v205\n"
    "s_waitcnt lgkmcnt(6)\n"
    "v_mfma_f32_32x32x16_bf16 v[64:79], v[168:171], a[48:51], v[64:79]\n"
    "ds_read_b64_tr_b16 v[196:197], v220 offset:51200\n"
    "ds_read_b64_tr_b16 v[198:199], v220 offset:51712\n"
    "v_add_f32 v206, 1.0, v206\n"
    "v_add_f32 v207, 1.0, v207\n"
    "v_rcp_f32 v204, v204\n"
    "v_mfma_f32_32x32x16_bf16 v[80:95], v[172:175], a[48:51], v[80:95]\n"
    "ds_read_b64_tr_b16 v[168:169], v217 offset:34816\n"
    "ds_read_b64_tr_b16 v[170:171], v217 offset:35328\n"
    "v_rcp_f32 v205, v205\n"
    "v_rcp_f32 v206, v206\n"
    "v_mfma_f32_32x32x16_bf16 v[64:79], v[176:179], a[52:55], v[64:79]\n"
    "ds_read_b64_tr_b16 v[172:173], v220 offset:34816\n"
    "ds_read_b64_tr_b16 v[174:175], v220 offset:35328\n"
    "v_rcp_f32 v207, v207\n"
    "v_mul_f32 v36, v36, v204\n"
    "v_mul_f32 v37, v37, v205\n"
    "v_mfma_f32_32x32x16_bf16 v[80:95], v[180:183], a[52:55], v[80:95]\n"
    "ds_read_b64_tr_b16 v[176:177], v217 offset:40960\n"
    "ds_read_b64_tr_b16 v[178:179], v217 offset:41472\n"
    "v_mul_f32 v38, v38, v206\n"
    "v_mul_f32 v39, v39, v207\n"
    "v_mul_f32 v200, v40, v40\n"
    "s_waitcnt lgkmcnt(6)\n"
    "v_mfma_f32_32x32x16_bf16 v[64:79], v[184:187], a[56:59], v[64:79]\n"
    "ds_read_b64_tr_b16 v[180:181], v220 offset:40960\n"
    "ds_read_b64_tr_b16 v[182:183], v220 offset:41472\n"
    "v_mul_f32 v201, v41, v41\n"
    "v_mul_f32 v202, v42, v42\n"
    "v_mul_f32 v203, v43, v43\n"
    "v_fmaak_f32 v200, v200, v231, 0xc0135761\n"
    "v_mfma_f32_32x32x16_bf16 v[80:95], v[188:191], a[56:59], v[80:95]\n"
    "ds_read_b64_tr_b16 v[184:185], v217 offset:47104\n"
    "ds_read_b64_tr_b16 v[186:187], v217 offset:47616\n"
    "v_fmaak_f32 v201, v201, v231, 0xc0135761\n"
    "v_fmaak_f32 v202, v202, v231, 0xc0135761\n"
    "v_fmaak_f32 v203, v203, v231, 0xc0135761\n"
    "v_mul_f32 v200, v200, v40\n"
    "v_mfma_f32_32x32x16_bf16 v[64:79], v[192:195], a[60:63], v[64:79]\n"
    "ds_read_b64_tr_b16 v[188:189], v220 offset:47104\n"
    "ds_read_b64_tr_b16 v[190:191], v220 offset:47616\n"
    "v_mul_f32 v201, v201, v41\n"
    "v_mul_f32 v202, v202, v42\n"
    "v_mul_f32 v203, v203, v43\n"
    "v_mfma_f32_32x32x16_bf16 v[80:95], v[196:199], a[60:63], v[80:95]\n"
    "ds_read_b64_tr_b16 v[192:193], v217 offset:53248\n"
    "ds_read_b64_tr_b16 v[194:195], v217 offset:53760\n"
    "v_exp_f32 v200, v200\n"
    "s_waitcnt lgkmcnt(6)\n"
    "v_mfma_f32_32x32x16_bf16 v[64:79], v[168:171], a[64:67], v[64:79]\n"
    "ds_read_b64_tr_b16 v[196:197], v220 offset:53248\n"
    "ds_read_b64_tr_b16 v[198:199], v220 offset:53760\n"
    "v_exp_f32 v201, v201\n"
    "v_exp_f32 v202, v202\n"
    "v_mfma_f32_32x32x16_bf16 v[80:95], v[172:175], a[64:67], v[80:95]\n"
    "ds_read_b64_tr_b16 v[168:169], v217 offset:36864\n"
    "ds_read_b64_tr_b16 v[170:171], v217 offset:37376\n"
    "v_exp_f32 v203, v203\n"
    "v_add_f32 v200, 1.0, v200\n"
    "v_add_f32 v201, 1.0, v201\n"
    "v_mfma_f32_32x32x16_bf16 v[64:79], v[176:179], a[68:71], v[64:79]\n"
    "ds_read_b64_tr_b16 v[172:173], v220 offset:36864\n"
    "ds_read_b64_tr_b16 v[174:175], v220 offset:37376\n"
    "v_add_f32 v202, 1.0, v202\n"
    "v_add_f32 v203, 1.0, v203\n"
    "v_rcp_f32 v200, v200\n"
    "v_mfma_f32_32x32x16_bf16 v[80:95], v[180:183], a[68:71], v[80:95]\n"
    "ds_read_b64_tr_b16 v[176:177], v217 offset:43008\n"
    "ds_read_b64_tr_b16 v[178:179], v217 offset:43520\n"
    "v_rcp_f32 v201, v201\n"
    "s_waitcnt lgkmcnt(6)\n"
    "v_mfma_f32_32x32x16_bf16 v[64:79], v[184:187], a[72:75], v[64:79]\n"
    "ds_read_b64_tr_b16 v[180:181], v220 offset:43008\n"
    "ds_read_b64_tr_b16 v[182:183], v220 offset:43520\n"
    "v_rcp_f32 v202, v202\n"
    "v_rcp_f32 v203, v203\n"
    "v_mfma_f32_32x32x16_bf16 v[80:95], v[188:191], a[72:75], v[80:95]\n"
    "ds_read_b64_tr_b16 v[184:185], v217 offset:49152\n"
    "ds_read_b64_tr_b16 v[186:187], v217 offset:49664\n"
    "v_mul_f32 v40, v40, v200\n"
    "v_mul_f32 v41, v41, v201\n"
    "v_mul_f32 v42, v42, v202\n"
    "v_mul_f32 v43, v43, v203\n"
    "v_mfma_f32_32x32x16_bf16 v[64:79], v[192:195], a[76:79], v[64:79]\n"
    "ds_read_b64_tr_b16 v[188:189], v220 offset:49152\n"
    "ds_read_b64_tr_b16 v[190:191], v220 offset:49664\n"
    "v_mul_f32 v204, v44, v44\n"
    "v_mul_f32 v205, v45, v45\n"
    "v_mul_f32 v206, v46, v46\n"
    "v_mul_f32 v207, v47, v47\n"
    "v_mfma_f32_32x32x16_bf16 v[80:95], v[196:199], a[76:79], v[80:95]\n"
    "ds_read_b64_tr_b16 v[192:193], v217 offset:55296\n"
    "ds_read_b64_tr_b16 v[194:195], v217 offset:55808\n"
    "v_fmaak_f32 v204, v204, v231, 0xc0135761\n"
    "v_fmaak_f32 v205, v205, v231, 0xc0135761\n"
    "v_fmaak_f32 v206, v206, v231, 0xc0135761\n"
    "s_waitcnt lgkmcnt(6)\n"
    "v_mfma_f32_32x32x16_bf16 v[64:79], v[168:171], a[80:83], v[64:79]\n"
    "s_waitcnt vmcnt(21)\n"
    "s_barrier\n"
    "v_mfma_f32_32x32x16_bf16 v[80:95], v[172:175], a[80:83], v[80:95]\n"
    "ds_read_b64_tr_b16 v[196:197], v220 offset:55296\n"
    "ds_read_b64_tr_b16 v[198:199], v220 offset:55808\n"
    "ds_read_b64_tr_b16 v[168:169], v216 offset:49152\n"
    "ds_read_b64_tr_b16 v[170:171], v216 offset:49664\n"
    "s_add_u32 m0, s65, 73728\n"
    "s_add_u32 s84, s57, 4480\n"
    "v_mfma_f32_32x32x16_bf16 v[64:79], v[176:179], a[84:87], v[64:79]\n"
    "ds_read_b64_tr_b16 v[172:173], v219 offset:49152\n"
    "ds_read_b64_tr_b16 v[174:175], v219 offset:49664\n"
    "buffer_load_dwordx4 v242, s[44:47], s84 offen lds\n"
    "v_fmaak_f32 v207, v207, v231, 0xc0135761\n"
    "v_mfma_f32_32x32x16_bf16 v[80:95], v[180:183], a[84:87], v[80:95]\n"
    "ds_read_b64_tr_b16 v[176:177], v216 offset:55296\n"
    "ds_read_b64_tr_b16 v[178:179], v216 offset:55808\n"
    "buffer_load_dwordx4 v225, s[44:47], s84 offen offset:1024 lds\n"
    "s_add_u32 s84, s57, 592256\n"
    "v_mul_f32 v204, v204, v44\n"
    "s_waitcnt lgkmcnt(6)\n"
    "v_mfma_f32_32x32x16_bf16 v[64:79], v[184:187], a[88:91], v[64:79]\n"
    "ds_read_b64_tr_b16 v[180:181], v219 offset:55296\n"
    "ds_read_b64_tr_b16 v[182:183], v219 offset:55808\n"
    "buffer_load_dwordx4 v242, s[44:47], s84 offen offset:2048 lds\n"
    "v_mul_f32 v205, v205, v45\n"
    "v_mul_f32 v206, v206, v46\n"
    "v_mfma_f32_32x32x16_bf16 v[80:95], v[188:191], a[88:91], v[80:95]\n"
    "ds_read_b64_tr_b16 v[184:185], v216 offset:61440\n"
    "ds_read_b64_tr_b16 v[186:187], v216 offset:61952\n"
    "buffer_load_dwordx4 v225, s[44:47], s84 offen offset:3072 lds\n"
    "s_add_u32 m0, s65, 77824\n"
    "s_add_u32 s84, s57, 1184128\n"
    "v_mul_f32 v207, v207, v47\n"
    "v_exp_f32 v204, v204\n"
    "v_mfma_f32_32x32x16_bf16 v[64:79], v[192:195], a[92:95], v[64:79]\n"
    "ds_read_b64_tr_b16 v[188:189], v219 offset:61440\n"
    "ds_read_b64_tr_b16 v[190:191], v219 offset:61952\n"
    "buffer_load_dwordx4 v242, s[44:47], s84 offen lds\n"
    "v_exp_f32 v205, v205\n"
    "v_mfma_f32_32x32x16_bf16 v[80:95], v[196:199], a[92:95], v[80:95]\n"
    "ds_read_b64_tr_b16 v[192:193], v217 offset:2048\n"
    "ds_read_b64_tr_b16 v[194:195], v217 offset:2560\n"
    "buffer_load_dwordx4 v225, s[44:47], s84 offen offset:1024 lds\n"
    "v_exp_f32 v206, v206\n"
    "s_waitcnt lgkmcnt(6)\n"
    "v_mfma_f32_32x32x16_bf16 v[64:79], v[168:171], a[96:99], v[64:79]\n"
    "ds_read_b64_tr_b16 v[196:197], v220 offset:2048\n"
    "ds_read_b64_tr_b16 v[198:199], v220 offset:2560\n"
    "v_exp_f32 v207, v207\n"
    "v_add_f32 v204, 1.0, v204\n"
    "v_add_f32 v205, 1.0, v205\n"
    "v_mfma_f32_32x32x16_bf16 v[80:95], v[172:175], a[96:99], v[80:95]\n"
    "ds_read_b64_tr_b16 v[168:169], v216 offset:51200\n"
    "ds_read_b64_tr_b16 v[170:171], v216 offset:51712\n"
    "v_add_f32 v206, 1.0, v206\n"
    "v_add_f32 v207, 1.0, v207\n"
    "v_rcp_f32 v204, v204\n"
    "v_mfma_f32_32x32x16_bf16 v[64:79], v[176:179], a[100:103], v[64:79]\n"
    "ds_read_b64_tr_b16 v[172:173], v219 offset:51200\n"
    "ds_read_b64_tr_b16 v[174:175], v219 offset:51712\n"
    "v_rcp_f32 v205, v205\n"
    "v_rcp_f32 v206, v206\n"
    "v_mfma_f32_32x32x16_bf16 v[80:95], v[180:183], a[100:103], v[80:95]\n"
    "ds_read_b64_tr_b16 v[176:177], v216 offset:57344\n"
    "ds_read_b64_tr_b16 v[178:179], v216 offset:57856\n"
    "v_rcp_f32 v207, v207\n"
    "v_mul_f32 v44, v44, v204\n"
    "s_waitcnt lgkmcnt(6)\n"
    "v_mfma_f32_32x32x16_bf16 v[64:79], v[184:187], a[104:107], v[64:79]\n"
    "ds_read_b64_tr_b16 v[180:181], v219 offset:57344\n"
    "ds_read_b64_tr_b16 v[182:183], v219 offset:57856\n"
    "v_mul_f32 v45, v45, v205\n"
    "v_mul_f32 v46, v46, v206\n"
    "v_mul_f32 v47, v47, v207\n"
    "v_mul_f32 v200, v48, v48\n"
    "v_mfma_f32_32x32x16_bf16 v[80:95], v[188:191], a[104:107], v[80:95]\n"
    "ds_read_b64_tr_b16 v[184:185], v216 offset:63488\n"
    "ds_read_b64_tr_b16 v[186:187], v216 offset:64000\n"
    "v_mul_f32 v201, v49, v49\n"
    "v_mul_f32 v202, v50, v50\n"
    "v_mul_f32 v203, v51, v51\n"
    "v_fmaak_f32 v200, v200, v231, 0xc0135761\n"
    "v_mfma_f32_32x32x16_bf16 v[64:79], v[192:195], a[108:111], v[64:79]\n"
    "ds_read_b64_tr_b16 v[188:189], v219 offset:63488\n"
    "ds_read_b64_tr_b16 v[190:191], v219 offset:64000\n"
    "v_fmaak_f32 v201, v201, v231, 0xc0135761\n"
    "v_fmaak_f32 v202, v202, v231, 0xc0135761\n"
    "v_fmaak_f32 v203, v203, v231, 0xc0135761\n"
    "v_mul_f32 v200, v200, v48\n"
    "v_mfma_f32_32x32x16_bf16 v[80:95], v[196:199], a[108:111], v[80:95]\n"
    "ds_read_b64_tr_b16 v[192:193], v217 offset:4096\n"
    "ds_read_b64_tr_b16 v[194:195], v217 offset:4608\n"
    "v_mul_f32 v201, v201, v49\n"
    "v_mul_f32 v202, v202, v50\n"
    "v_mul_f32 v203, v203, v51\n"
    "s_waitcnt lgkmcnt(6)\n"
    "v_mfma_f32_32x32x16_bf16 v[64:79], v[168:171], a[112:115], v[64:79]\n"
    "ds_read_b64_tr_b16 v[196:197], v220 offset:4096\n"
    "ds_read_b64_tr_b16 v[198:199], v220 offset:4608\n"
    "v_exp_f32 v200, v200\n"
    "v_exp_f32 v201, v201\n"
    "v_mfma_f32_32x32x16_bf16 v[80:95], v[172:175], a[112:115], v[80:95]\n"
    "ds_read_b64_tr_b16 v[168:169], v216 offset:53248\n"
    "ds_read_b64_tr_b16 v[170:171], v216 offset:53760\n"
    "v_exp_f32 v202, v202\n"
    "v_exp_f32 v203, v203\n"
    "v_mfma_f32_32x32x16_bf16 v[64:79], v[176:179], a[116:119], v[64:79]\n"
    "ds_read_b64_tr_b16 v[172:173], v219 offset:53248\n"
    "ds_read_b64_tr_b16 v[174:175], v219 offset:53760\n"
    "v_add_f32 v200, 1.0, v200\n"
    "v_add_f32 v201, 1.0, v201\n"
    "v_add_f32 v202, 1.0, v202\n"
    "v_add_f32 v203, 1.0, v203\n"
    "v_mfma_f32_32x32x16_bf16 v[80:95], v[180:183], a[116:119], v[80:95]\n"
    "ds_read_b64_tr_b16 v[176:177], v216 offset:59392\n"
    "ds_read_b64_tr_b16 v[178:179], v216 offset:59904\n"
    "v_rcp_f32 v200, v200\n"
    "s_waitcnt lgkmcnt(6)\n"
    "v_mfma_f32_32x32x16_bf16 v[64:79], v[184:187], a[120:123], v[64:79]\n"
    "ds_read_b64_tr_b16 v[180:181], v219 offset:59392\n"
    "ds_read_b64_tr_b16 v[182:183], v219 offset:59904\n"
    "v_rcp_f32 v201, v201\n"
    "v_rcp_f32 v202, v202\n"
    "v_mfma_f32_32x32x16_bf16 v[80:95], v[188:191], a[120:123], v[80:95]\n"
    "ds_read_b64_tr_b16 v[184:185], v217 offset:0\n"
    "ds_read_b64_tr_b16 v[186:187], v217 offset:512\n"
    "v_rcp_f32 v203, v203\n"
    "v_mul_f32 v48, v48, v200\n"
    "v_mul_f32 v49, v49, v201\n"
    "v_mfma_f32_32x32x16_bf16 v[64:79], v[192:195], a[124:127], v[64:79]\n"
    "ds_read_b64_tr_b16 v[188:189], v220 offset:0\n"
    "ds_read_b64_tr_b16 v[190:191], v220 offset:512\n"
    "v_mul_f32 v50, v50, v202\n"
    "v_mul_f32 v51, v51, v203\n"
    "v_mul_f32 v204, v52, v52\n"
    "v_mul_f32 v205, v53, v53\n"
    "v_mfma_f32_32x32x16_bf16 v[80:95], v[196:199], a[124:127], v[80:95]\n"
    "ds_read_b64_tr_b16 v[192:193], v217 offset:6144\n"
    "ds_read_b64_tr_b16 v[194:195], v217 offset:6656\n"
    "v_mul_f32 v206, v54, v54\n"
    "v_mul_f32 v207, v55, v55\n"
    "v_fmaak_f32 v204, v204, v231, 0xc0135761\n"
    "s_waitcnt lgkmcnt(6)\n"
    "v_mfma_f32_32x32x16_bf16 v[64:79], v[168:171], a[128:131], v[64:79]\n"
    "s_waitcnt vmcnt(21)\n"
    "s_barrier\n"
    "v_mfma_f32_32x32x16_bf16 v[80:95], v[172:175], a[128:131], v[80:95]\n"
    "ds_read_b64_tr_b16 v[196:197], v220 offset:6144\n"
    "ds_read_b64_tr_b16 v[198:199], v220 offset:6656\n"
    "ds_read_b64_tr_b16 v[168:169], v216 offset:0\n"
    "ds_read_b64_tr_b16 v[170:171], v216 offset:512\n"
    "s_add_u32 m0, s65, 98304\n"
    "s_add_u32 s84, s57, 1773952\n"
    "v_mfma_f32_32x32x16_bf16 v[64:79], v[176:179], a[132:135], v[64:79]\n"
    "ds_read_b64_tr_b16 v[172:173], v219 offset:0\n"
    "ds_read_b64_tr_b16 v[174:175], v219 offset:512\n"
    "buffer_load_dwordx4 v242, s[44:47], s84 offen lds\n"
    "v_fmaak_f32 v205, v205, v231, 0xc0135761\n"
    "v_mfma_f32_32x32x16_bf16 v[80:95], v[180:183], a[132:135], v[80:95]\n"
    "ds_read_b64_tr_b16 v[176:177], v216 offset:6144\n"
    "ds_read_b64_tr_b16 v[178:179], v216 offset:6656\n"
    "buffer_load_dwordx4 v225, s[44:47], s84 offen offset:1024 lds\n"
    "s_add_u32 s84, s57, 2361728\n"
    "s_waitcnt lgkmcnt(6)\n"
    "v_mfma_f32_32x32x16_bf16 v[64:79], v[184:187], a[136:139], v[64:79]\n"
    "ds_read_b64_tr_b16 v[180:181], v219 offset:6144\n"
    "ds_read_b64_tr_b16 v[182:183], v219 offset:6656\n"
    "buffer_load_dwordx4 v242, s[44:47], s84 offen offset:2048 lds\n"
    "v_fmaak_f32 v206, v206, v231, 0xc0135761\n"
    "v_fmaak_f32 v207, v207, v231, 0xc0135761\n"
    "v_mfma_f32_32x32x16_bf16 v[80:95], v[188:191], a[136:139], v[80:95]\n"
    "ds_read_b64_tr_b16 v[184:185], v216 offset:12288\n"
    "ds_read_b64_tr_b16 v[186:187], v216 offset:12800\n"
    "buffer_load_dwordx4 v225, s[44:47], s84 offen offset:3072 lds\n"
    "s_add_u32 m0, s65, 102400\n"
    "s_add_u32 s84, s57, 2953600\n"
    "v_mul_f32 v204, v204, v52\n"
    "v_mul_f32 v205, v205, v53\n"
    "v_mul_f32 v206, v206, v54\n"
    "v_mfma_f32_32x32x16_bf16 v[64:79], v[192:195], a[140:143], v[64:79]\n"
    "ds_read_b64_tr_b16 v[188:189], v219 offset:12288\n"
    "ds_read_b64_tr_b16 v[190:191], v219 offset:12800\n"
    "buffer_load_dwordx4 v242, s[44:47], s84 offen lds\n"
    "v_mul_f32 v207, v207, v55\n"
    "v_mfma_f32_32x32x16_bf16 v[80:95], v[196:199], a[140:143], v[80:95]\n"
    "ds_read_b64_tr_b16 v[192:193], v216 offset:18432\n"
    "ds_read_b64_tr_b16 v[194:195], v216 offset:18944\n"
    "buffer_load_dwordx4 v225, s[44:47], s84 offen offset:1024 lds\n"
    "v_exp_f32 v204, v204\n"
    "s_waitcnt lgkmcnt(6)\n"
    "v_mfma_f32_32x32x16_bf16 v[0:15], v[168:171], a[0:3], 0\n"
    "ds_read_b64_tr_b16 v[196:197], v219 offset:18432\n"
    "ds_read_b64_tr_b16 v[198:199], v219 offset:18944\n"
    "s_add_u32 s86, s73, 4224\n"
    "buffer_load_dword v255, v252, s[48:51], s86 offen\n"
    "v_exp_f32 v205, v205\n"
    "v_exp_f32 v206, v206\n"
    "v_mfma_f32_32x32x16_bf16 v[16:31], v[172:175], a[0:3], 0\n"
    "ds_read_b64_tr_b16 v[168:169], v216 offset:2048\n"
    "ds_read_b64_tr_b16 v[170:171], v216 offset:2560\n"
    "v_exp_f32 v207, v207\n"
    "v_add_f32 v204, 1.0, v204\n"
    "v_add_f32 v205, 1.0, v205\n"
    "v_mfma_f32_32x32x16_bf16 v[0:15], v[176:179], a[4:7], v[0:15]\n"
    "ds_read_b64_tr_b16 v[172:173], v219 offset:2048\n"
    "ds_read_b64_tr_b16 v[174:175], v219 offset:2560\n"
    "v_add_f32 v206, 1.0, v206\n"
    "v_add_f32 v207, 1.0, v207\n"
    "v_rcp_f32 v204, v204\n"
    "v_mfma_f32_32x32x16_bf16 v[16:31], v[180:183], a[4:7], v[16:31]\n"
    "ds_read_b64_tr_b16 v[176:177], v216 offset:8192\n"
    "ds_read_b64_tr_b16 v[178:179], v216 offset:8704\n"
    "v_rcp_f32 v205, v205\n"
    "s_waitcnt lgkmcnt(6)\n"
    "v_mfma_f32_32x32x16_bf16 v[0:15], v[184:187], a[8:11], v[0:15]\n"
    "ds_read_b64_tr_b16 v[180:181], v219 offset:8192\n"
    "ds_read_b64_tr_b16 v[182:183], v219 offset:8704\n"
    "v_rcp_f32 v206, v206\n"
    "v_rcp_f32 v207, v207\n"
    "v_mfma_f32_32x32x16_bf16 v[16:31], v[188:191], a[8:11], v[16:31]\n"
    "ds_read_b64_tr_b16 v[184:185], v216 offset:14336\n"
    "ds_read_b64_tr_b16 v[186:187], v216 offset:14848\n"
    "v_mul_f32 v52, v52, v204\n"
    "v_mul_f32 v53, v53, v205\n"
    "v_mul_f32 v54, v54, v206\n"
    "v_mul_f32 v55, v55, v207\n"
    "v_mfma_f32_32x32x16_bf16 v[0:15], v[192:195], a[12:15], v[0:15]\n"
    "ds_read_b64_tr_b16 v[188:189], v219 offset:14336\n"
    "ds_read_b64_tr_b16 v[190:191], v219 offset:14848\n"
    "v_mul_f32 v200, v56, v56\n"
    "v_mul_f32 v201, v57, v57\n"
    "v_mul_f32 v202, v58, v58\n"
    "v_mul_f32 v203, v59, v59\n"
    "v_mfma_f32_32x32x16_bf16 v[16:31], v[196:199], a[12:15], v[16:31]\n"
    "ds_read_b64_tr_b16 v[192:193], v216 offset:20480\n"
    "ds_read_b64_tr_b16 v[194:195], v216 offset:20992\n"
    "v_fmaak_f32 v200, v200, v231, 0xc0135761\n"
    "v_fmaak_f32 v201, v201, v231, 0xc0135761\n"
    "v_fmaak_f32 v202, v202, v231, 0xc0135761\n"
    "s_waitcnt lgkmcnt(6)\n"
    "v_mfma_f32_32x32x16_bf16 v[0:15], v[168:171], a[16:19], v[0:15]\n"
    "ds_read_b64_tr_b16 v[196:197], v219 offset:20480\n"
    "ds_read_b64_tr_b16 v[198:199], v219 offset:20992\n"
    "v_fmaak_f32 v203, v203, v231, 0xc0135761\n"
    "v_mul_f32 v200, v200, v56\n"
    "v_mul_f32 v201, v201, v57\n"
    "v_mul_f32 v202, v202, v58\n"
    "v_mfma_f32_32x32x16_bf16 v[16:31], v[172:175], a[16:19], v[16:31]\n"
    "ds_read_b64_tr_b16 v[168:169], v216 offset:4096\n"
    "ds_read_b64_tr_b16 v[170:171], v216 offset:4608\n"
    "v_mul_f32 v203, v203, v59\n"
    "v_exp_f32 v200, v200\n"
    "v_mfma_f32_32x32x16_bf16 v[0:15], v[176:179], a[20:23], v[0:15]\n"
    "ds_read_b64_tr_b16 v[172:173], v219 offset:4096\n"
    "ds_read_b64_tr_b16 v[174:175], v219 offset:4608\n"
    "v_exp_f32 v201, v201\n"
    "v_exp_f32 v202, v202\n"
    "v_mfma_f32_32x32x16_bf16 v[16:31], v[180:183], a[20:23], v[16:31]\n"
    "ds_read_b64_tr_b16 v[176:177], v216 offset:10240\n"
    "ds_read_b64_tr_b16 v[178:179], v216 offset:10752\n"
    "v_exp_f32 v203, v203\n"
    "v_add_f32 v200, 1.0, v200\n"
    "s_waitcnt lgkmcnt(6)\n"
    "v_mfma_f32_32x32x16_bf16 v[0:15], v[184:187], a[24:27], v[0:15]\n"
    "ds_read_b64_tr_b16 v[180:181], v219 offset:10240\n"
    "ds_read_b64_tr_b16 v[182:183], v219 offset:10752\n"
    "v_add_f32 v201, 1.0, v201\n"
    "v_add_f32 v202, 1.0, v202\n"
    "v_add_f32 v203, 1.0, v203\n"
    "v_mfma_f32_32x32x16_bf16 v[16:31], v[188:191], a[24:27], v[16:31]\n"
    "ds_read_b64_tr_b16 v[184:185], v216 offset:16384\n"
    "ds_read_b64_tr_b16 v[186:187], v216 offset:16896\n"
    "v_rcp_f32 v200, v200\n"
    "v_rcp_f32 v201, v201\n"
    "v_mfma_f32_32x32x16_bf16 v[0:15], v[192:195], a[28:31], v[0:15]\n"
    "ds_read_b64_tr_b16 v[188:189], v219 offset:16384\n"
    "ds_read_b64_tr_b16 v[190:191], v219 offset:16896\n"
    "v_rcp_f32 v202, v202\n"
    "v_rcp_f32 v203, v203\n"
    "v_mfma_f32_32x32x16_bf16 v[16:31], v[196:199], a[28:31], v[16:31]\n"
    "ds_read_b64_tr_b16 v[192:193], v216 offset:22528\n"
    "ds_read_b64_tr_b16 v[194:195], v216 offset:23040\n"
    "v_mul_f32 v56, v56, v200\n"
    "v_mul_f32 v57, v57, v201\n"
    "v_mul_f32 v58, v58, v202\n"
    "s_waitcnt lgkmcnt(6)\n"
    "v_mfma_f32_32x32x16_bf16 v[0:15], v[168:171], a[32:35], v[0:15]\n"
    "s_waitcnt vmcnt(21)\n"
    "s_barrier\n"
    "v_mfma_f32_32x32x16_bf16 v[16:31], v[172:175], a[32:35], v[16:31]\n"
    "ds_read_b64_tr_b16 v[196:197], v219 offset:22528\n"
    "ds_read_b64_tr_b16 v[198:199], v219 offset:23040\n"
    "ds_read_b64_tr_b16 v[168:169], v218 offset:8192\n"
    "ds_read_b64_tr_b16 v[170:171], v218 offset:8704\n"
    "s_add_u32 m0, s65, 49152\n"
    "s_add_u32 s84, s57, 3543424\n"
    "v_mfma_f32_32x32x16_bf16 v[0:15], v[176:179], a[36:39], v[0:15]\n"
    "ds_read_b64_tr_b16 v[172:173], v221 offset:8192\n"
    "ds_read_b64_tr_b16 v[174:175], v221 offset:8704\n"
    "buffer_load_dwordx4 v242, s[44:47], s84 offen lds\n"
    "v_mul_f32 v59, v59, v203\n"
    "v_mfma_f32_32x32x16_bf16 v[16:31], v[180:183], a[36:39], v[16:31]\n"
    "ds_read_b64_tr_b16 v[176:177], v218 offset:14336\n"
    "ds_read_b64_tr_b16 v[178:179], v218 offset:14848\n"
    "buffer_load_dwordx4 v225, s[44:47], s84 offen offset:1024 lds\n"
    "s_add_u32 s84, s57, 4131200\n"
    "s_waitcnt lgkmcnt(6)\n"
    "v_mfma_f32_32x32x16_bf16 v[0:15], v[184:187], a[40:43], v[0:15]\n"
    "ds_read_b64_tr_b16 v[180:181], v221 offset:14336\n"
    "ds_read_b64_tr_b16 v[182:183], v221 offset:14848\n"
    "buffer_load_dwordx4 v242, s[44:47], s84 offen offset:2048 lds\n"
    "v_mul_f32 v204, v60, v60\n"
    "v_mfma_f32_32x32x16_bf16 v[16:31], v[188:191], a[40:43], v[16:31]\n"
    "ds_read_b64_tr_b16 v[184:185], v218 offset:20480\n"
    "ds_read_b64_tr_b16 v[186:187], v218 offset:20992\n"
    "buffer_load_dwordx4 v225, s[44:47], s84 offen offset:3072 lds\n"
    "s_add_u32 m0, s65, 53248\n"
    "s_add_u32 s84, s57, 4723072\n"
    "v_mul_f32 v205, v61, v61\n"
    "v_mul_f32 v206, v62, v62\n"
    "v_mfma_f32_32x32x16_bf16 v[0:15], v[192:195], a[44:47], v[0:15]\n"
    "ds_read_b64_tr_b16 v[188:189], v221 offset:20480\n"
    "ds_read_b64_tr_b16 v[190:191], v221 offset:20992\n"
    "buffer_load_dwordx4 v242, s[44:47], s84 offen lds\n"
    "v_mul_f32 v207, v63, v63\n"
    "v_fmaak_f32 v204, v204, v231, 0xc0135761\n"
    "v_mfma_f32_32x32x16_bf16 v[16:31], v[196:199], a[44:47], v[16:31]\n"
    "ds_read_b64_tr_b16 v[192:193], v218 offset:26624\n"
    "ds_read_b64_tr_b16 v[194:195], v218 offset:27136\n"
    "buffer_load_dwordx4 v225, s[44:47], s84 offen offset:1024 lds\n"
    "v_fmaak_f32 v205, v205, v231, 0xc0135761\n"
    "v_fmaak_f32 v206, v206, v231, 0xc0135761\n"
    "v_fmaak_f32 v207, v207, v231, 0xc0135761\n"
    "s_waitcnt lgkmcnt(6)\n"
    "v_mfma_f32_32x32x16_bf16 v[0:15], v[168:171], a[48:51], v[0:15]\n"
    "ds_read_b64_tr_b16 v[196:197], v221 offset:26624\n"
    "ds_read_b64_tr_b16 v[198:199], v221 offset:27136\n"
    "v_mul_f32 v204, v204, v60\n"
    "v_mul_f32 v205, v205, v61\n"
    "v_mul_f32 v206, v206, v62\n"
    "v_mul_f32 v207, v207, v63\n"
    "v_mfma_f32_32x32x16_bf16 v[16:31], v[172:175], a[48:51], v[16:31]\n"
    "ds_read_b64_tr_b16 v[168:169], v218 offset:10240\n"
    "ds_read_b64_tr_b16 v[170:171], v218 offset:10752\n"
    "v_exp_f32 v204, v204\n"
    "v_exp_f32 v205, v205\n"
    "v_mfma_f32_32x32x16_bf16 v[0:15], v[176:179], a[52:55], v[0:15]\n"
    "ds_read_b64_tr_b16 v[172:173], v221 offset:10240\n"
    "ds_read_b64_tr_b16 v[174:175], v221 offset:10752\n"
    "v_exp_f32 v206, v206\n"
    "v_exp_f32 v207, v207\n"
    "v_mfma_f32_32x32x16_bf16 v[16:31], v[180:183], a[52:55], v[16:31]\n"
    "ds_read_b64_tr_b16 v[176:177], v218 offset:16384\n"
    "ds_read_b64_tr_b16 v[178:179], v218 offset:16896\n"
    "v_add_f32 v204, 1.0, v204\n"
    "v_add_f32 v205, 1.0, v205\n"
    "v_add_f32 v206, 1.0, v206\n"
    "s_waitcnt lgkmcnt(6)\n"
    "v_mfma_f32_32x32x16_bf16 v[0:15], v[184:187], a[56:59], v[0:15]\n"
    "ds_read_b64_tr_b16 v[180:181], v221 offset:16384\n"
    "ds_read_b64_tr_b16 v[182:183], v221 offset:16896\n"
    "v_add_f32 v207, 1.0, v207\n"
    "v_rcp_f32 v204, v204\n"
    "v_mfma_f32_32x32x16_bf16 v[16:31], v[188:191], a[56:59], v[16:31]\n"
    "ds_read_b64_tr_b16 v[184:185], v218 offset:22528\n"
    "ds_read_b64_tr_b16 v[186:187], v218 offset:23040\n"
    "v_rcp_f32 v205, v205\n"
    "v_rcp_f32 v206, v206\n"
    "v_mfma_f32_32x32x16_bf16 v[0:15], v[192:195], a[60:63], v[0:15]\n"
    "ds_read_b64_tr_b16 v[188:189], v221 offset:22528\n"
    "ds_read_b64_tr_b16 v[190:191], v221 offset:23040\n"
    "v_rcp_f32 v207, v207\n"
    "v_mul_f32 v60, v60, v204\n"
    "v_mul_f32 v61, v61, v205\n"
    "v_mfma_f32_32x32x16_bf16 v[16:31], v[196:199], a[60:63], v[16:31]\n"
    "ds_read_b64_tr_b16 v[192:193], v218 offset:28672\n"
    "ds_read_b64_tr_b16 v[194:195], v218 offset:29184\n"
    "v_mul_f32 v62, v62, v206\n"
    "v_mul_f32 v63, v63, v207\n"
    "v_mul_f32 v32, v32, v64\n"
    "s_waitcnt lgkmcnt(6)\n"
    "v_mfma_f32_32x32x16_bf16 v[0:15], v[168:171], a[64:67], v[0:15]\n"
    "ds_read_b64_tr_b16 v[196:197], v221 offset:28672\n"
    "ds_read_b64_tr_b16 v[198:199], v221 offset:29184\n"
    "v_mul_f32 v33, v33, v65\n"
    "v_mul_f32 v34, v34, v66\n"
    "v_mul_f32 v35, v35, v67\n"
    "v_mul_f32 v36, v36, v68\n"
    "v_mfma_f32_32x32x16_bf16 v[16:31], v[172:175], a[64:67], v[16:31]\n"
    "ds_read_b64_tr_b16 v[168:169], v218 offset:12288\n"
    "ds_read_b64_tr_b16 v[170:171], v218 offset:12800\n"
    "v_mul_f32 v37, v37, v69\n"
    "v_mul_f32 v38, v38, v70\n"
    "v_mul_f32 v39, v39, v71\n"
    "v_cvt_pk_bf16_f32 v128, v32, v33\n"
    "v_mfma_f32_32x32x16_bf16 v[0:15], v[176:179], a[68:71], v[0:15]\n"
    "ds_read_b64_tr_b16 v[172:173], v221 offset:12288\n"
    "ds_read_b64_tr_b16 v[174:175], v221 offset:12800\n"
    "v_cvt_pk_bf16_f32 v129, v34, v35\n"
    "v_cvt_pk_bf16_f32 v130, v36, v37\n"
    "v_cvt_pk_bf16_f32 v131, v38, v39\n"
    "v_mfma_f32_32x32x16_bf16 v[16:31], v[180:183], a[68:71], v[16:31]\n"
    "ds_read_b64_tr_b16 v[176:177], v218 offset:18432\n"
    "ds_read_b64_tr_b16 v[178:179], v218 offset:18944\n"
    "s_waitcnt lgkmcnt(6)\n"
    "v_mfma_f32_32x32x16_bf16 v[0:15], v[184:187], a[72:75], v[0:15]\n"
    "ds_read_b64_tr_b16 v[180:181], v221 offset:18432\n"
    "ds_read_b64_tr_b16 v[182:183], v221 offset:18944\n"
    "s_add_u32 s85, s67, 8192\n"
    "buffer_store_dwordx4 v[128:131], v250, s[52:55], s85 offen sc1\n"
    "v_mfma_f32_32x32x16_bf16 v[16:31], v[188:191], a[72:75], v[16:31]\n"
    "ds_read_b64_tr_b16 v[184:185], v218 offset:24576\n"
    "ds_read_b64_tr_b16 v[186:187], v218 offset:25088\n"
    "v_mul_f32 v40, v40, v72\n"
    "v_mul_f32 v41, v41, v73\n"
    "v_mul_f32 v42, v42, v74\n"
    "v_mul_f32 v43, v43, v75\n"
    "v_mfma_f32_32x32x16_bf16 v[0:15], v[192:195], a[76:79], v[0:15]\n"
    "ds_read_b64_tr_b16 v[188:189], v221 offset:24576\n"
    "ds_read_b64_tr_b16 v[190:191], v221 offset:25088\n"
    "v_mul_f32 v44, v44, v76\n"
    "v_mul_f32 v45, v45, v77\n"
    "v_mul_f32 v46, v46, v78\n"
    "v_mul_f32 v47, v47, v79\n"
    "v_mfma_f32_32x32x16_bf16 v[16:31], v[196:199], a[76:79], v[16:31]\n"
    "ds_read_b64_tr_b16 v[192:193], v218 offset:30720\n"
    "ds_read_b64_tr_b16 v[194:195], v218 offset:31232\n"
    "v_cvt_pk_bf16_f32 v132, v40, v41\n"
    "v_cvt_pk_bf16_f32 v133, v42, v43\n"
    "v_cvt_pk_bf16_f32 v134, v44, v45\n"
    "s_waitcnt lgkmcnt(6)\n"
    "v_mfma_f32_32x32x16_bf16 v[0:15], v[168:171], a[80:83], v[0:15]\n"
    "s_waitcnt vmcnt(20)\n"
    "s_barrier\n"
    "v_mfma_f32_32x32x16_bf16 v[16:31], v[172:175], a[80:83], v[16:31]\n"
    "ds_read_b64_tr_b16 v[196:197], v221 offset:30720\n"
    "ds_read_b64_tr_b16 v[198:199], v221 offset:31232\n"
    "ds_read_b64_tr_b16 v[168:169], v216 offset:24576\n"
    "ds_read_b64_tr_b16 v[170:171], v216 offset:25088\n"
    "s_add_u32 m0, s65, 0\n"
    "s_add_u32 s84, s59, 4096\n"
    "v_mfma_f32_32x32x16_bf16 v[0:15], v[176:179], a[84:87], v[0:15]\n"
    "ds_read_b64_tr_b16 v[172:173], v219 offset:24576\n"
    "ds_read_b64_tr_b16 v[174:175], v219 offset:25088\n"
    "buffer_load_dwordx4 v243, s[48:51], s84 offen lds\n"
    "v_cvt_pk_bf16_f32 v135, v46, v47\n"
    "v_mfma_f32_32x32x16_bf16 v[16:31], v[180:183], a[84:87], v[16:31]\n"
    "ds_read_b64_tr_b16 v[176:177], v216 offset:30720\n"
    "ds_read_b64_tr_b16 v[178:179], v216 offset:31232\n"
    "buffer_load_dwordx4 v226, s[48:51], s84 offen offset:1024 lds\n"
    "s_add_u32 s84, s59, 75776\n"
    "s_waitcnt lgkmcnt(6)\n"
    "v_mfma_f32_32x32x16_bf16 v[0:15], v[184:187], a[88:91], v[0:15]\n"
    "ds_read_b64_tr_b16 v[180:181], v219 offset:30720\n"
    "ds_read_b64_tr_b16 v[182:183], v219 offset:31232\n"
    "buffer_load_dwordx4 v243, s[48:51], s84 offen offset:2048 lds\n"
    "v_mfma_f32_32x32x16_bf16 v[16:31], v[188:191], a[88:91], v[16:31]\n"
    "ds_read_b64_tr_b16 v[184:185], v216 offset:36864\n"
    "ds_read_b64_tr_b16 v[186:187], v216 offset:37376\n"
    "buffer_load_dwordx4 v226, s[48:51], s84 offen offset:3072 lds\n"
    "s_add_u32 m0, s65, 4096\n"
    "s_add_u32 s84, s59, 151552\n"
    "v_mfma_f32_32x32x16_bf16 v[0:15], v[192:195], a[92:95], v[0:15]\n"
    "ds_read_b64_tr_b16 v[188:189], v219 offset:36864\n"
    "ds_read_b64_tr_b16 v[190:191], v219 offset:37376\n"
    "buffer_load_dwordx4 v243, s[48:51], s84 offen lds\n"
    "v_mfma_f32_32x32x16_bf16 v[16:31], v[196:199], a[92:95], v[16:31]\n"
    "ds_read_b64_tr_b16 v[192:193], v216 offset:43008\n"
    "ds_read_b64_tr_b16 v[194:195], v216 offset:43520\n"
    "buffer_load_dwordx4 v226, s[48:51], s84 offen offset:1024 lds\n"
    "s_waitcnt lgkmcnt(6)\n"
    "v_mfma_f32_32x32x16_bf16 v[0:15], v[168:171], a[96:99], v[0:15]\n"
    "ds_read_b64_tr_b16 v[196:197], v219 offset:43008\n"
    "ds_read_b64_tr_b16 v[198:199], v219 offset:43520\n"
    "s_add_u32 s85, s67, 9216\n"
    "buffer_store_dwordx4 v[132:135], v250, s[52:55], s85 offen sc1\n"
    "v_mul_f32 v48, v48, v80\n"
    "v_mfma_f32_32x32x16_bf16 v[16:31], v[172:175], a[96:99], v[16:31]\n"
    "ds_read_b64_tr_b16 v[168:169], v216 offset:26624\n"
    "ds_read_b64_tr_b16 v[170:171], v216 offset:27136\n"
    "v_mul_f32 v49, v49, v81\n"
    "v_mul_f32 v50, v50, v82\n"
    "v_mul_f32 v51, v51, v83\n"
    "v_mul_f32 v52, v52, v84\n"
    "v_mfma_f32_32x32x16_bf16 v[0:15], v[176:179], a[100:103], v[0:15]\n"
    "ds_read_b64_tr_b16 v[172:173], v219 offset:26624\n"
    "ds_read_b64_tr_b16 v[174:175], v219 offset:27136\n"
    "v_mul_f32 v53, v53, v85\n"
    "v_mul_f32 v54, v54, v86\n"
    "v_mul_f32 v55, v55, v87\n"
    "v_cvt_pk_bf16_f32 v136, v48, v49\n"
    "v_mfma_f32_32x32x16_bf16 v[16:31], v[180:183], a[100:103], v[16:31]\n"
    "ds_read_b64_tr_b16 v[176:177], v216 offset:32768\n"
    "ds_read_b64_tr_b16 v[178:179], v216 offset:33280\n"
    "v_cvt_pk_bf16_f32 v137, v50, v51\n"
    "v_cvt_pk_bf16_f32 v138, v52, v53\n"
    "v_cvt_pk_bf16_f32 v139, v54, v55\n"
    "s_waitcnt lgkmcnt(6)\n"
    "v_mfma_f32_32x32x16_bf16 v[0:15], v[184:187], a[104:107], v[0:15]\n"
    "ds_read_b64_tr_b16 v[180:181], v219 offset:32768\n"
    "ds_read_b64_tr_b16 v[182:183], v219 offset:33280\n"
    "s_add_u32 s85, s67, 10240\n"
    "buffer_store_dwordx4 v[136:139], v250, s[52:55], s85 offen sc1\n"
    "v_mfma_f32_32x32x16_bf16 v[16:31], v[188:191], a[104:107], v[16:31]\n"
    "ds_read_b64_tr_b16 v[184:185], v216 offset:38912\n"
    "ds_read_b64_tr_b16 v[186:187], v216 offset:39424\n"
    "v_mul_f32 v56, v56, v88\n"
    "v_mul_f32 v57, v57, v89\n"
    "v_mul_f32 v58, v58, v90\n"
    "v_mul_f32 v59, v59, v91\n"
    "v_mfma_f32_32x32x16_bf16 v[0:15], v[192:195], a[108:111], v[0:15]\n"
    "ds_read_b64_tr_b16 v[188:189], v219 offset:38912\n"
    "ds_read_b64_tr_b16 v[190:191], v219 offset:39424\n"
    "v_mul_f32 v60, v60, v92\n"
    "v_mul_f32 v61, v61, v93\n"
    "v_mul_f32 v62, v62, v94\n"
    "v_mul_f32 v63, v63, v95\n"
    "v_mfma_f32_32x32x16_bf16 v[16:31], v[196:199], a[108:111], v[16:31]\n"
    "ds_read_b64_tr_b16 v[192:193], v216 offset:45056\n"
    "ds_read_b64_tr_b16 v[194:195], v216 offset:45568\n"
    "v_cvt_pk_bf16_f32 v140, v56, v57\n"
    "v_cvt_pk_bf16_f32 v141, v58, v59\n"
    "v_cvt_pk_bf16_f32 v142, v60, v61\n"
    "s_waitcnt lgkmcnt(6)\n"
    "v_mfma_f32_32x32x16_bf16 v[0:15], v[168:171], a[112:115], v[0:15]\n"
    "ds_read_b64_tr_b16 v[196:197], v219 offset:45056\n"
    "ds_read_b64_tr_b16 v[198:199], v219 offset:45568\n"
    "v_cvt_pk_bf16_f32 v143, v62, v63\n"
    "v_mfma_f32_32x32x16_bf16 v[16:31], v[172:175], a[112:115], v[16:31]\n"
    "ds_read_b64_tr_b16 v[168:169], v216 offset:28672\n"
    "ds_read_b64_tr_b16 v[170:171], v216 offset:29184\n"
    "s_add_u32 s85, s67, 11264\n"
    "buffer_store_dwordx4 v[140:143], v250, s[52:55], s85 offen sc1\n"
    "v_mfma_f32_32x32x16_bf16 v[0:15], v[176:179], a[116:119], v[0:15]\n"
    "ds_read_b64_tr_b16 v[172:173], v219 offset:28672\n"
    "ds_read_b64_tr_b16 v[174:175], v219 offset:29184\n"
    "v_mfma_f32_32x32x16_bf16 v[16:31], v[180:183], a[116:119], v[16:31]\n"
    "ds_read_b64_tr_b16 v[176:177], v216 offset:34816\n"
    "ds_read_b64_tr_b16 v[178:179], v216 offset:35328\n"
    "s_waitcnt lgkmcnt(6)\n"
    "v_mfma_f32_32x32x16_bf16 v[0:15], v[184:187], a[120:123], v[0:15]\n"
    "ds_read_b64_tr_b16 v[180:181], v219 offset:34816\n"
    "ds_read_b64_tr_b16 v[182:183], v219 offset:35328\n"
    "v_mfma_f32_32x32x16_bf16 v[16:31], v[188:191], a[120:123], v[16:31]\n"
    "ds_read_b64_tr_b16 v[184:185], v216 offset:40960\n"
    "ds_read_b64_tr_b16 v[186:187], v216 offset:41472\n"
    "v_mfma_f32_32x32x16_bf16 v[0:15], v[192:195], a[124:127], v[0:15]\n"
    "ds_read_b64_tr_b16 v[188:189], v219 offset:40960\n"
    "ds_read_b64_tr_b16 v[190:191], v219 offset:41472\n"
    "v_mfma_f32_32x32x16_bf16 v[16:31], v[196:199], a[124:127], v[16:31]\n"
    "ds_read_b64_tr_b16 v[192:193], v216 offset:47104\n"
    "ds_read_b64_tr_b16 v[194:195], v216 offset:47616\n"
    "s_waitcnt lgkmcnt(6)\n"
    "v_mfma_f32_32x32x16_bf16 v[0:15], v[168:171], a[128:131], v[0:15]\n"
    "s_waitcnt vmcnt(23)\n"
    "s_barrier\n"
    "v_mfma_f32_32x32x16_bf16 v[16:31], v[172:175], a[128:131], v[16:31]\n"
    "ds_read_b64_tr_b16 v[196:197], v219 offset:47104\n"
    "ds_read_b64_tr_b16 v[198:199], v219 offset:47616\n"
    "ds_read_b64_tr_b16 v[168:169], v217 offset:8192\n"
    "ds_read_b64_tr_b16 v[170:171], v217 offset:8704\n"
    "s_add_u32 m0, s65, 139264\n"
    "s_add_u32 s84, s60, 4096\n"
    "v_mfma_f32_32x32x16_bf16 v[0:15], v[176:179], a[132:135], v[0:15]\n"
    "ds_read_b64_tr_b16 v[172:173], v220 offset:8192\n"
    "ds_read_b64_tr_b16 v[174:175], v220 offset:8704\n"
    "buffer_load_dwordx4 v243, s[48:51], s84 offen lds\n"
    "v_mfma_f32_32x32x16_bf16 v[16:31], v[180:183], a[132:135], v[16:31]\n"
    "ds_read_b64_tr_b16 v[176:177], v217 offset:14336\n"
    "ds_read_b64_tr_b16 v[178:179], v217 offset:14848\n"
    "buffer_load_dwordx4 v226, s[48:51], s84 offen offset:1024 lds\n"
    "s_add_u32 s84, s60, 75776\n"
    "s_waitcnt lgkmcnt(6)\n"
    "v_mfma_f32_32x32x16_bf16 v[0:15], v[184:187], a[136:139], v[0:15]\n"
    "ds_read_b64_tr_b16 v[180:181], v220 offset:14336\n"
    "ds_read_b64_tr_b16 v[182:183], v220 offset:14848\n"
    "buffer_load_dwordx4 v243, s[48:51], s84 offen offset:2048 lds\n"
    "v_mfma_f32_32x32x16_bf16 v[16:31], v[188:191], a[136:139], v[16:31]\n"
    "ds_read_b64_tr_b16 v[184:185], v217 offset:20480\n"
    "ds_read_b64_tr_b16 v[186:187], v217 offset:20992\n"
    "buffer_load_dwordx4 v226, s[48:51], s84 offen offset:3072 lds\n"
    "s_add_u32 m0, s65, 143360\n"
    "s_add_u32 s84, s60, 151552\n"
    "v_mfma_f32_32x32x16_bf16 v[0:15], v[192:195], a[140:143], v[0:15]\n"
    "ds_read_b64_tr_b16 v[188:189], v220 offset:20480\n"
    "ds_read_b64_tr_b16 v[190:191], v220 offset:20992\n"
    "buffer_load_dwordx4 v243, s[48:51], s84 offen lds\n"
    "v_mfma_f32_32x32x16_bf16 v[16:31], v[196:199], a[140:143], v[16:31]\n"
    "ds_read_b64_tr_b16 v[192:193], v217 offset:26624\n"
    "ds_read_b64_tr_b16 v[194:195], v217 offset:27136\n"
    "buffer_load_dwordx4 v226, s[48:51], s84 offen offset:1024 lds\n"
    "s_waitcnt lgkmcnt(6)\n"
    "v_mfma_f32_32x32x16_bf16 v[64:79], v[168:171], a[0:3], 0\n"
    "ds_read_b64_tr_b16 v[196:197], v220 offset:26624\n"
    "ds_read_b64_tr_b16 v[198:199], v220 offset:27136\n"
    "s_add_u32 s86, s73, 4352\n"
    "buffer_load_dword v255, v252, s[48:51], s86 offen\n"
    "v_mfma_f32_32x32x16_bf16 v[80:95], v[172:175], a[0:3], 0\n"
    "ds_read_b64_tr_b16 v[168:169], v217 offset:10240\n"
    "ds_read_b64_tr_b16 v[170:171], v217 offset:10752\n"
    "v_mfma_f32_32x32x16_bf16 v[64:79], v[176:179], a[4:7], v[64:79]\n"
    "ds_read_b64_tr_b16 v[172:173], v220 offset:10240\n"
    "ds_read_b64_tr_b16 v[174:175], v220 offset:10752\n"
    "v_mul_f32 v200, v0, v0\n"
    "v_mul_f32 v201, v1, v1\n"
    "v_mul_f32 v202, v2, v2\n"
    "v_mul_f32 v203, v3, v3\n"
    "v_mfma_f32_32x32x16_bf16 v[80:95], v[180:183], a[4:7], v[80:95]\n"
    "ds_read_b64_tr_b16 v[176:177], v217 offset:16384\n"
    "ds_read_b64_tr_b16 v[178:179], v217 offset:16896\n"
    "v_fmaak_f32 v200, v200, v231, 0xc0135761\n"
    "v_fmaak_f32 v201, v201, v231, 0xc0135761\n"
    "v_fmaak_f32 v202, v202, v231, 0xc0135761\n"
    "s_waitcnt lgkmcnt(6)\n"
    "v_mfma_f32_32x32x16_bf16 v[64:79], v[184:187], a[8:11], v[64:79]\n"
    "ds_read_b64_tr_b16 v[180:181], v220 offset:16384\n"
    "ds_read_b64_tr_b16 v[182:183], v220 offset:16896\n"
    "v_fmaak_f32 v203, v203, v231, 0xc0135761\n"
    "v_mul_f32 v200, v200, v0\n"
    "v_mul_f32 v201, v201, v1\n"
    "v_mul_f32 v202, v202, v2\n"
    "v_mfma_f32_32x32x16_bf16 v[80:95], v[188:191], a[8:11], v[80:95]\n"
    "ds_read_b64_tr_b16 v[184:185], v217 offset:22528\n"
    "ds_read_b64_tr_b16 v[186:187], v217 offset:23040\n"
    "v_mul_f32 v203, v203, v3\n"
    "v_exp_f32 v200, v200\n"
    "v_mfma_f32_32x32x16_bf16 v[64:79], v[192:195], a[12:15], v[64:79]\n"
    "ds_read_b64_tr_b16 v[188:189], v220 offset:22528\n"
    "ds_read_b64_tr_b16 v[190:191], v220 offset:23040\n"
    "v_exp_f32 v201, v201\n"
    "v_exp_f32 v202, v202\n"
    "v_mfma_f32_32x32x16_bf16 v[80:95], v[196:199], a[12:15], v[80:95]\n"
    "ds_read_b64_tr_b16 v[192:193], v217 offset:28672\n"
    "ds_read_b64_tr_b16 v[194:195], v217 offset:29184\n"
    "v_exp_f32 v203, v203\n"
    "v_add_f32 v200, 1.0, v200\n"
    "s_waitcnt lgkmcnt(6)\n"
    "v_mfma_f32_32x32x16_bf16 v[64:79], v[168:171], a[16:19], v[64:79]\n"
    "ds_read_b64_tr_b16 v[196:197], v220 offset:28672\n"
    "ds_read_b64_tr_b16 v[198:199], v220 offset:29184\n"
    "v_add_f32 v201, 1.0, v201\n"
    "v_add_f32 v202, 1.0, v202\n"
    "v_add_f32 v203, 1.0, v203\n"
    "v_mfma_f32_32x32x16_bf16 v[80:95], v[172:175], a[16:19], v[80:95]\n"
    "ds_read_b64_tr_b16 v[168:169], v217 offset:12288\n"
    "ds_read_b64_tr_b16 v[170:171], v217 offset:12800\n"
    "s_waitcnt vmcnt(7)\n"
    "v_add_u32 v238, 1, v230\n"
    "buffer_store_dword v238, off, s[52:55], s69 sc1\n"
    "v_mfma_f32_32x32x16_bf16 v[64:79], v[176:179], a[20:23], v[64:79]\n"
    "ds_read_b64_tr_b16 v[172:173], v220 offset:12288\n"
    "ds_read_b64_tr_b16 v[174:175], v220 offset:12800\n"
    "v_rcp_f32 v200, v200\n"
    "v_rcp_f32 v201, v201\n"
    "v_mfma_f32_32x32x16_bf16 v[80:95], v[180:183], a[20:23], v[80:95]\n"
    "ds_read_b64_tr_b16 v[176:177], v217 offset:18432\n"
    "ds_read_b64_tr_b16 v[178:179], v217 offset:18944\n"
    "v_rcp_f32 v202, v202\n"
    "s_waitcnt lgkmcnt(6)\n"
    "v_mfma_f32_32x32x16_bf16 v[64:79], v[184:187], a[24:27], v[64:79]\n"
    "ds_read_b64_tr_b16 v[180:181], v220 offset:18432\n"
    "ds_read_b64_tr_b16 v[182:183], v220 offset:18944\n"
    "v_rcp_f32 v203, v203\n"
    "v_mul_f32 v0, v0, v200\n"
    "v_mul_f32 v1, v1, v201\n"
    "v_mfma_f32_32x32x16_bf16 v[80:95], v[188:191], a[24:27], v[80:95]\n"
    "ds_read_b64_tr_b16 v[184:185], v217 offset:24576\n"
    "ds_read_b64_tr_b16 v[186:187], v217 offset:25088\n"
    "v_mul_f32 v2, v2, v202\n"
    "v_mul_f32 v3, v3, v203\n"
    "v_mul_f32 v204, v4, v4\n"
    "v_mul_f32 v205, v5, v5\n"
    "v_mfma_f32_32x32x16_bf16 v[64:79], v[192:195], a[28:31], v[64:79]\n"
    "ds_read_b64_tr_b16 v[188:189], v220 offset:24576\n"
    "ds_read_b64_tr_b16 v[190:191], v220 offset:25088\n"
    "v_mul_f32 v206, v6, v6\n"
    "v_mul_f32 v207, v7, v7\n"
    "v_fmaak_f32 v204, v204, v231, 0xc0135761\n"
    "v_fmaak_f32 v205, v205, v231, 0xc0135761\n"
    "v_mfma_f32_32x32x16_bf16 v[80:95], v[196:199], a[28:31], v[80:95]\n"
    "ds_read_b64_tr_b16 v[192:193], v217 offset:30720\n"
    "ds_read_b64_tr_b16 v[194:195], v217 offset:31232\n"
    "v_fmaak_f32 v206, v206, v231, 0xc0135761\n"
    "v_fmaak_f32 v207, v207, v231, 0xc0135761\n"
    "v_mul_f32 v204, v204, v4\n"
    "s_waitcnt lgkmcnt(6)\n"
    "v_mfma_f32_32x32x16_bf16 v[64:79], v[168:171], a[32:35], v[64:79]\n"
    "s_barrier\n"
    "v_mfma_f32_32x32x16_bf16 v[80:95], v[172:175], a[32:35], v[80:95]\n"
    "ds_read_b64_tr_b16 v[196:197], v220 offset:30720\n"
    "ds_read_b64_tr_b16 v[198:199], v220 offset:31232\n"
    "ds_read_b64_tr_b16 v[168:169], v217 offset:32768\n"
    "ds_read_b64_tr_b16 v[170:171], v217 offset:33280\n"
    "s_add_u32 m0, s65, 24576\n"
    "s_add_u32 s84, s61, 4096\n"
    "v_mul_f32 v205, v205, v5\n"
    "v_mul_f32 v206, v206, v6\n"
    "v_mul_f32 v207, v207, v7\n"
    "v_mfma_f32_32x32x16_bf16 v[64:79], v[176:179], a[36:39], v[64:79]\n"
    "ds_read_b64_tr_b16 v[172:173], v220 offset:32768\n"
    "ds_read_b64_tr_b16 v[174:175], v220 offset:33280\n"
    "buffer_load_dwordx4 v243, s[48:51], s84 offen lds\n"
    "v_exp_f32 v204, v204\n"
    "v_exp_f32 v205, v205\n"
    "v_mfma_f32_32x32x16_bf16 v[80:95], v[180:183], a[36:39], v[80:95]\n"
    "ds_read_b64_tr_b16 v[176:177], v217 offset:38912\n"
    "ds_read_b64_tr_b16 v[178:179], v217 offset:39424\n"
    "buffer_load_dwordx4 v226, s[48:51], s84 offen offset:1024 lds\n"
    "s_add_u32 s84, s59, 2176\n"
    "v_exp_f32 v206, v206\n"
    "s_waitcnt lgkmcnt(6)\n"
    "v_mfma_f32_32x32x16_bf16 v[64:79], v[184:187], a[40:43], v[64:79]\n"
    "ds_read_b64_tr_b16 v[180:181], v220 offset:38912\n"
    "ds_read_b64_tr_b16 v[182:183], v220 offset:39424\n"
    "buffer_load_dwordx4 v243, s[48:51], s84 offen offset:2048 lds\n"
    "v_exp_f32 v207, v207\n"
    "v_add_f32 v204, 1.0, v204\n"
    "v_add_f32 v205, 1.0, v205\n"
    "v_mfma_f32_32x32x16_bf16 v[80:95], v[188:191], a[40:43], v[80:95]\n"
    "ds_read_b64_tr_b16 v[184:185], v217 offset:45056\n"
    "ds_read_b64_tr_b16 v[186:187], v217 offset:45568\n"
    "buffer_load_dwordx4 v226, s[48:51], s84 offen offset:3072 lds\n"
    "s_add_u32 m0, s65, 28672\n"
    "s_add_u32 s84, s59, 77952\n"
    "v_add_f32 v206, 1.0, v206\n"
    "v_add_f32 v207, 1.0, v207\n"
    "v_mfma_f32_32x32x16_bf16 v[64:79], v[192:195], a[44:47], v[64:79]\n"
    "ds_read_b64_tr_b16 v[188:189], v220 offset:45056\n"
    "ds_read_b64_tr_b16 v[190:191], v220 offset:45568\n"
    "buffer_load_dwordx4 v243, s[48:51], s84 offen lds\n"
    "v_rcp_f32 v204, v204\n"
    "v_rcp_f32 v205, v205\n"
    "v_mfma_f32_32x32x16_bf16 v[80:95], v[196:199], a[44:47], v[80:95]\n"
    "ds_read_b64_tr_b16 v[192:193], v217 offset:51200\n"
    "ds_read_b64_tr_b16 v[194:195], v217 offset:51712\n"
    "buffer_load_dwordx4 v226, s[48:51], s84 offen offset:1024 lds\n"
    "v_rcp_f32 v206, v206\n"
    "v_rcp_f32 v207, v207\n"
    "s_waitcnt lgkmcnt(6)\n"
    "v_mfma_f32_32x32x16_bf16 v[64:79], v[168:171], a[48:51], v[64:79]\n"
    "ds_read_b64_tr_b16 v[196:197], v220 offset:51200\n"
    "ds_read_b64_tr_b16 v[198:199], v220 offset:51712\n"
    "v_mul_f32 v4, v4, v204\n"
    "v_mul_f32 v5, v5, v205\n"
    "v_mul_f32 v6, v6, v206\n"
    "v_mul_f32 v7, v7, v207\n"
    "v_mfma_f32_32x32x16_bf16 v[80:95], v[172:175], a[48:51], v[80:95]\n"
    "ds_read_b64_tr_b16 v[168:169], v217 offset:34816\n"
    "ds_read_b64_tr_b16 v[170:171], v217 offset:35328\n"
    "v_mul_f32 v200, v8, v8\n"
    "v_mul_f32 v201, v9, v9\n"
    "v_mul_f32 v202, v10, v10\n"
    "v_mul_f32 v203, v11, v11\n"
    "v_mfma_f32_32x32x16_bf16 v[64:79], v[176:179], a[52:55], v[64:79]\n"
    "ds_read_b64_tr_b16 v[172:173], v220 offset:34816\n"
    "ds_read_b64_tr_b16 v[174:175], v220 offset:35328\n"
    "v_fmaak_f32 v200, v200, v231, 0xc0135761\n"
    "v_fmaak_f32 v201, v201, v231, 0xc0135761\n"
    "v_fmaak_f32 v202, v202, v231, 0xc0135761\n"
    "v_fmaak_f32 v203, v203, v231, 0xc0135761\n"
    "v_mfma_f32_32x32x16_bf16 v[80:95], v[180:183], a[52:55], v[80:95]\n"
    "ds_read_b64_tr_b16 v[176:177], v217 offset:40960\n"
    "ds_read_b64_tr_b16 v[178:179], v217 offset:41472\n"
    "v_mul_f32 v200, v200, v8\n"
    "v_mul_f32 v201, v201, v9\n"
    "v_mul_f32 v202, v202, v10\n"
    "s_waitcnt lgkmcnt(6)\n"
    "v_mfma_f32_32x32x16_bf16 v[64:79], v[184:187], a[56:59], v[64:79]\n"
    "ds_read_b64_tr_b16 v[180:181], v220 offset:40960\n"
    "ds_read_b64_tr_b16 v[182:183], v220 offset:41472\n"
    "v_mul_f32 v203, v203, v11\n"
    "v_exp_f32 v200, v200\n"
    "v_mfma_f32_32x32x16_bf16 v[80:95], v[188:191], a[56:59], v[80:95]\n"
    "ds_read_b64_tr_b16 v[184:185], v217 offset:47104\n"
    "ds_read_b64_tr_b16 v[186:187], v217 offset:47616\n"
    "v_exp_f32 v201, v201\n"
    "v_exp_f32 v202, v202\n"
    "v_mfma_f32_32x32x16_bf16 v[64:79], v[192:195], a[60:63], v[64:79]\n"
    "ds_read_b64_tr_b16 v[188:189], v220 offset:47104\n"
    "ds_read_b64_tr_b16 v[190:191], v220 offset:47616\n"
    "buffer_load_dword v229, off, s[52:55], s70 sc1\n"
    "v_mfma_f32_32x32x16_bf16 v[80:95], v[196:199], a[60:63], v[80:95]\n"
    "ds_read_b64_tr_b16 v[192:193], v217 offset:53248\n"
    "ds_read_b64_tr_b16 v[194:195], v217 offset:53760\n"
    "v_exp_f32 v203, v203\n"
    "v_add_f32 v200, 1.0, v200\n"
    "s_waitcnt lgkmcnt(6)\n"
    "v_mfma_f32_32x32x16_bf16 v[64:79], v[168:171], a[64:67], v[64:79]\n"
    "ds_read_b64_tr_b16 v[196:197], v220 offset:53248\n"
    "ds_read_b64_tr_b16 v[198:199], v220 offset:53760\n"
    "v_add_f32 v201, 1.0, v201\n"
    "v_add_f32 v202, 1.0, v202\n"
    "v_add_f32 v203, 1.0, v203\n"
    "v_mfma_f32_32x32x16_bf16 v[80:95], v[172:175], a[64:67], v[80:95]\n"
    "ds_read_b64_tr_b16 v[168:169], v217 offset:36864\n"
    "ds_read_b64_tr_b16 v[170:171], v217 offset:37376\n"
    "v_rcp_f32 v200, v200\n"
    "v_rcp_f32 v201, v201\n"
    "v_mfma_f32_32x32x16_bf16 v[64:79], v[176:179], a[68:71], v[64:79]\n"
    "ds_read_b64_tr_b16 v[172:173], v220 offset:36864\n"
    "ds_read_b64_tr_b16 v[174:175], v220 offset:37376\n"
    "v_rcp_f32 v202, v202\n"
    "v_rcp_f32 v203, v203\n"
    "v_mfma_f32_32x32x16_bf16 v[80:95], v[180:183], a[68:71], v[80:95]\n"
    "ds_read_b64_tr_b16 v[176:177], v217 offset:43008\n"
    "ds_read_b64_tr_b16 v[178:179], v217 offset:43520\n"
    "v_mul_f32 v8, v8, v200\n"
    "v_mul_f32 v9, v9, v201\n"
    "v_mul_f32 v10, v10, v202\n"
    "s_waitcnt lgkmcnt(6)\n"
    "v_mfma_f32_32x32x16_bf16 v[64:79], v[184:187], a[72:75], v[64:79]\n"
    "ds_read_b64_tr_b16 v[180:181], v220 offset:43008\n"
    "ds_read_b64_tr_b16 v[182:183], v220 offset:43520\n"
    "v_mul_f32 v11, v11, v203\n"
    "v_mul_f32 v204, v12, v12\n"
    "v_mul_f32 v205, v13, v13\n"
    "v_mul_f32 v206, v14, v14\n"
    "v_mfma_f32_32x32x16_bf16 v[80:95], v[188:191], a[72:75], v[80:95]\n"
    "ds_read_b64_tr_b16 v[184:185], v217 offset:49152\n"
    "ds_read_b64_tr_b16 v[186:187], v217 offset:49664\n"
    "v_mul_f32 v207, v15, v15\n"
    "v_fmaak_f32 v204, v204, v231, 0xc0135761\n"
    "v_fmaak_f32 v205, v205, v231, 0xc0135761\n"
    "v_fmaak_f32 v206, v206, v231, 0xc0135761\n"
    "v_mfma_f32_32x32x16_bf16 v[64:79], v[192:195], a[76:79], v[64:79]\n"
    "ds_read_b64_tr_b16 v[188:189], v220 offset:49152\n"
    "ds_read_b64_tr_b16 v[190:191], v220 offset:49664\n"
    "v_fmaak_f32 v207, v207, v231, 0xc0135761\n"
    "v_mul_f32 v204, v204, v12\n"
    "v_mul_f32 v205, v205, v13\n"
    "v_mul_f32 v206, v206, v14\n"
    "v_mfma_f32_32x32x16_bf16 v[80:95], v[196:199], a[76:79], v[80:95]\n"
    "ds_read_b64_tr_b16 v[192:193], v217 offset:55296\n"
    "ds_read_b64_tr_b16 v[194:195], v217 offset:55808\n"
    "v_mul_f32 v207, v207, v15\n"
    "v_exp_f32 v204, v204\n"
    "s_waitcnt lgkmcnt(6)\n"
    "v_mfma_f32_32x32x16_bf16 v[64:79], v[168:171], a[80:83], v[64:79]\n"
    "s_barrier\n"
    "v_mfma_f32_32x32x16_bf16 v[80:95], v[172:175], a[80:83], v[80:95]\n"
    "ds_read_b64_tr_b16 v[196:197], v220 offset:55296\n"
    "ds_read_b64_tr_b16 v[198:199], v220 offset:55808\n"
    "ds_read_b64_tr_b16 v[168:169], v216 offset:49152\n"
    "ds_read_b64_tr_b16 v[170:171], v216 offset:49664\n"
    "s_add_u32 m0, s65, 73728\n"
    "s_add_u32 s84, s59, 151680\n"
    "v_mfma_f32_32x32x16_bf16 v[64:79], v[176:179], a[84:87], v[64:79]\n"
    "ds_read_b64_tr_b16 v[172:173], v219 offset:49152\n"
    "ds_read_b64_tr_b16 v[174:175], v219 offset:49664\n"
    "buffer_load_dwordx4 v243, s[48:51], s84 offen lds\n"
    "v_exp_f32 v205, v205\n"
    "v_exp_f32 v206, v206\n"
    "v_mfma_f32_32x32x16_bf16 v[80:95], v[180:183], a[84:87], v[80:95]\n"
    "ds_read_b64_tr_b16 v[176:177], v216 offset:55296\n"
    "ds_read_b64_tr_b16 v[178:179], v216 offset:55808\n"
    "buffer_load_dwordx4 v226, s[48:51], s84 offen offset:1024 lds\n"
    "s_add_u32 s84, s60, 2176\n"
    "v_exp_f32 v207, v207\n"
    "v_add_f32 v204, 1.0, v204\n"
    "s_waitcnt lgkmcnt(6)\n"
    "v_mfma_f32_32x32x16_bf16 v[64:79], v[184:187], a[88:91], v[64:79]\n"
    "ds_read_b64_tr_b16 v[180:181], v219 offset:55296\n"
    "ds_read_b64_tr_b16 v[182:183], v219 offset:55808\n"
    "buffer_load_dwordx4 v243, s[48:51], s84 offen offset:2048 lds\n"
    "v_add_f32 v205, 1.0, v205\n"
    "v_add_f32 v206, 1.0, v206\n"
    "v_add_f32 v207, 1.0, v207\n"
    "v_mfma_f32_32x32x16_bf16 v[80:95], v[188:191], a[88:91], v[80:95]\n"
    "ds_read_b64_tr_b16 v[184:185], v216 offset:61440\n"
    "ds_read_b64_tr_b16 v[186:187], v216 offset:61952\n"
    "buffer_load_dwordx4 v226, s[48:51], s84 offen offset:3072 lds\n"
    "s_add_u32 m0, s65, 77824\n"
    "s_add_u32 s84, s60, 77952\n"
    "v_rcp_f32 v204, v204\n"
    "v_mfma_f32_32x32x16_bf16 v[64:79], v[192:195], a[92:95], v[64:79]\n"
    "ds_read_b64_tr_b16 v[188:189], v219 offset:61440\n"
    "ds_read_b64_tr_b16 v[190:191], v219 offset:61952\n"
    "buffer_load_dwordx4 v243, s[48:51], s84 offen lds\n"
    "v_rcp_f32 v205, v205\n"
    "v_rcp_f32 v206, v206\n"
    "v_mfma_f32_32x32x16_bf16 v[80:95], v[196:199], a[92:95], v[80:95]\n"
    "ds_read_b64_tr_b16 v[192:193], v217 offset:2048\n"
    "ds_read_b64_tr_b16 v[194:195], v217 offset:2560\n"
    "buffer_load_dwordx4 v226, s[48:51], s84 offen offset:1024 lds\n"
    "v_rcp_f32 v207, v207\n"
    "v_mul_f32 v12, v12, v204\n"
    "s_waitcnt lgkmcnt(6)\n"
    "v_mfma_f32_32x32x16_bf16 v[64:79], v[168:171], a[96:99], v[64:79]\n"
    "ds_read_b64_tr_b16 v[196:197], v220 offset:2048\n"
    "ds_read_b64_tr_b16 v[198:199], v220 offset:2560\n"
    "v_mul_f32 v13, v13, v205\n"
    "v_mul_f32 v14, v14, v206\n"
    "v_mul_f32 v15, v15, v207\n"
    "v_mul_f32 v200, v16, v16\n"
    "v_mfma_f32_32x32x16_bf16 v[80:95], v[172:175], a[96:99], v[80:95]\n"
    "ds_read_b64_tr_b16 v[168:169], v216 offset:51200\n"
    "ds_read_b64_tr_b16 v[170:171], v216 offset:51712\n"
    "v_mul_f32 v201, v17, v17\n"
    "v_mul_f32 v202, v18, v18\n"
    "v_mul_f32 v203, v19, v19\n"
    "v_fmaak_f32 v200, v200, v231, 0xc0135761\n"
    "v_mfma_f32_32x32x16_bf16 v[64:79], v[176:179], a[100:103], v[64:79]\n"
    "ds_read_b64_tr_b16 v[172:173], v219 offset:51200\n"
    "ds_read_b64_tr_b16 v[174:175], v219 offset:51712\n"
    "v_fmaak_f32 v201, v201, v231, 0xc0135761\n"
    "v_fmaak_f32 v202, v202, v231, 0xc0135761\n"
    "v_fmaak_f32 v203, v203, v231, 0xc0135761\n"
    "v_mul_f32 v200, v200, v16\n"
    "v_mfma_f32_32x32x16_bf16 v[80:95], v[180:183], a[100:103], v[80:95]\n"
    "ds_read_b64_tr_b16 v[176:177], v216 offset:57344\n"
    "ds_read_b64_tr_b16 v[178:179], v216 offset:57856\n"
    "v_mul_f32 v201, v201, v17\n"
    "v_mul_f32 v202, v202, v18\n"
    "v_mul_f32 v203, v203, v19\n"
    "s_waitcnt lgkmcnt(6)\n"
    "v_mfma_f32_32x32x16_bf16 v[64:79], v[184:187], a[104:107], v[64:79]\n"
    "ds_read_b64_tr_b16 v[180:181], v219 offset:57344\n"
    "ds_read_b64_tr_b16 v[182:183], v219 offset:57856\n"
    "s_waitcnt vmcnt(6)\n"
    "v_sub_u32 v229, v229, v230\n"
    "s_nop 0\n"
    "v_readfirstlane_b32 s86, v229\n"
    "s_nop 1\n"
    "s_sub_u32 s86, s86, 1\n"
    "s_cmp_le_u32 s86, 1\n"
    "s_cbranch_scc0 Lslow0_%=\n"
    "Lback0_%=:\n"
    "buffer_store_dword v230, off, s[52:55], s71 sc1\n"
    "s_add_u32 s85, s68, 0\n"
    "buffer_load_dwordx4 a[144:147], v250, s[52:55], s85 offen sc1\n"
    "v_mfma_f32_32x32x16_bf16 v[80:95], v[188:191], a[104:107], v[80:95]\n"
    "ds_read_b64_tr_b16 v[184:185], v216 offset:63488\n"
    "ds_read_b64_tr_b16 v[186:187], v216 offset:64000\n"
    "s_add_u32 s85, s68, 1024\n"
    "buffer_load_dwordx4 a[148:151], v250, s[52:55], s85 offen sc1\n"
    "v_exp_f32 v200, v200\n"
    "v_mfma_f32_32x32x16_bf16 v[64:79], v[192:195], a[108:111], v[64:79]\n"
    "ds_read_b64_tr_b16 v[188:189], v219 offset:63488\n"
    "ds_read_b64_tr_b16 v[190:191], v219 offset:64000\n"
    "s_add_u32 s85, s68, 2048\n"
    "buffer_load_dwordx4 a[152:155], v250, s[52:55], s85 offen sc1\n"
    "v_exp_f32 v201, v201\n"
    "v_exp_f32 v202, v202\n"
    "v_mfma_f32_32x32x16_bf16 v[80:95], v[196:199], a[108:111], v[80:95]\n"
    "ds_read_b64_tr_b16 v[192:193], v217 offset:4096\n"
    "ds_read_b64_tr_b16 v[194:195], v217 offset:4608\n"
    "s_add_u32 s85, s68, 3072\n"
    "buffer_load_dwordx4 a[156:159], v250, s[52:55], s85 offen sc1\n"
    "v_exp_f32 v203, v203\n"
    "v_add_f32 v200, 1.0, v200\n"
    "s_waitcnt lgkmcnt(6)\n"
    "v_mfma_f32_32x32x16_bf16 v[64:79], v[168:171], a[112:115], v[64:79]\n"
    "ds_read_b64_tr_b16 v[196:197], v220 offset:4096\n"
    "ds_read_b64_tr_b16 v[198:199], v220 offset:4608\n"
    "s_add_u32 s85, s68, 4096\n"
    "buffer_load_dwordx4 a[160:163], v250, s[52:55], s85 offen sc1\n"
    "v_add_f32 v201, 1.0, v201\n"
    "v_add_f32 v202, 1.0, v202\n"
    "v_add_f32 v203, 1.0, v203\n"
    "v_mfma_f32_32x32x16_bf16 v[80:95], v[172:175], a[112:115], v[80:95]\n"
    "ds_read_b64_tr_b16 v[168:169], v216 offset:53248\n"
    "ds_read_b64_tr_b16 v[170:171], v216 offset:53760\n"
    "s_add_u32 s85, s68, 5120\n"
    "buffer_load_dwordx4 a[164:167], v250, s[52:55], s85 offen sc1\n"
    "v_rcp_f32 v200, v200\n"
    "v_mfma_f32_32x32x16_bf16 v[64:79], v[176:179], a[116:119], v[64:79]\n"
    "ds_read_b64_tr_b16 v[172:173], v219 offset:53248\n"
    "ds_read_b64_tr_b16 v[174:175], v219 offset:53760\n"
    "s_add_u32 s85, s68, 6144\n"
    "buffer_load_dwordx4 a[168:171], v250, s[52:55], s85 offen sc1\n"
    "v_rcp_f32 v201, v201\n"
    "v_rcp_f32 v202, v202\n"
    "v_mfma_f32_32x32x16_bf16 v[80:95], v[180:183], a[116:119], v[80:95]\n"
    "ds_read_b64_tr_b16 v[176:177], v216 offset:59392\n"
    "ds_read_b64_tr_b16 v[178:179], v216 offset:59904\n"
    "s_add_u32 s85, s68, 7168\n"
    "buffer_load_dwordx4 a[172:175], v250, s[52:55], s85 offen sc1\n"
    "v_rcp_f32 v203, v203\n"
    "v_mul_f32 v16, v16, v200\n"
    "s_waitcnt lgkmcnt(6)\n"
    "v_mfma_f32_32x32x16_bf16 v[64:79], v[184:187], a[120:123], v[64:79]\n"
    "ds_read_b64_tr_b16 v[180:181], v219 offset:59392\n"
    "ds_read_b64_tr_b16 v[182:183], v219 offset:59904\n"
    "s_add_u32 s85, s68, 8192\n"
    "buffer_load_dwordx4 a[176:179], v250, s[52:55], s85 offen sc1\n"
    "v_mul_f32 v17, v17, v201\n"
    "v_mul_f32 v18, v18, v202\n"
    "v_mul_f32 v19, v19, v203\n"
    "v_mfma_f32_32x32x16_bf16 v[80:95], v[188:191], a[120:123], v[80:95]\n"
    "ds_read_b64_tr_b16 v[184:185], v217 offset:0\n"
    "ds_read_b64_tr_b16 v[186:187], v217 offset:512\n"
    "s_add_u32 s85, s68, 9216\n"
    "buffer_load_dwordx4 a[180:183], v250, s[52:55], s85 offen sc1\n"
    "v_mul_f32 v204, v20, v20\n"
    "v_mul_f32 v205, v21, v21\n"
    "v_mul_f32 v206, v22, v22\n"
    "v_mul_f32 v207, v23, v23\n"
    "v_mfma_f32_32x32x16_bf16 v[64:79], v[192:195], a[124:127], v[64:79]\n"
    "ds_read_b64_tr_b16 v[188:189], v220 offset:0\n"
    "ds_read_b64_tr_b16 v[190:191], v220 offset:512\n"
    "s_add_u32 s85, s68, 10240\n"
    "buffer_load_dwordx4 a[184:187], v250, s[52:55], s85 offen sc1\n"
    "v_fmaak_f32 v204, v204, v231, 0xc0135761\n"
    "v_fmaak_f32 v205, v205, v231, 0xc0135761\n"
    "v_fmaak_f32 v206, v206, v231, 0xc0135761\n"
    "v_mfma_f32_32x32x16_bf16 v[80:95], v[196:199], a[124:127], v[80:95]\n"
    "ds_read_b64_tr_b16 v[192:193], v217 offset:6144\n"
    "ds_read_b64_tr_b16 v[194:195], v217 offset:6656\n"
    "s_add_u32 s85, s68, 11264\n"
    "buffer_load_dwordx4 a[188:191], v250, s[52:55], s85 offen sc1\n"
    "v_fmaak_f32 v207, v207, v231, 0xc0135761\n"
    "v_mul_f32 v204, v204, v20\n"
    "v_mul_f32 v205, v205, v21\n"
    "s_waitcnt lgkmcnt(6)\n"
    "v_mfma_f32_32x32x16_bf16 v[64:79], v[168:171], a[128:131], v[64:79]\n"
    "s_barrier\n"
    "v_mfma_f32_32x32x16_bf16 v[80:95], v[172:175], a[128:131], v[80:95]\n"
    "ds_read_b64_tr_b16 v[196:197], v220 offset:6144\n"
    "ds_read_b64_tr_b16 v[198:199], v220 offset:6656\n"
    "v_lshlrev_b32 v237, 9, v253\n"
    "v_sub_u32 v216, v216, v237\n"
    "v_sub_u32 v217, v217, v237\n"
    "v_sub_u32 v218, v218, v237\n"
    "v_sub_u32 v219, v219, v237\n"
    "v_sub_u32 v220, v220, v237\n"
    "v_sub_u32 v221, v221, v237\n"
    "v_lshlrev_b32 v237, 8, v253\n"
    "v_sub_u32 v222, v222, v237\n"
    "v_sub_u32 v223, v223, v237\n"
    "v_sub_u32 v224, v224, v237\n"
    "ds_read_b64_tr_b16 v[168:169], v216 offset:0\n"
    "ds_read_b64_tr_b16 v[170:171], v216 offset:1024\n"
    "s_add_u32 s85, s68, 16384\n"
    "buffer_load_dwordx4 a[208:211], v250, s[52:55], s85 offen sc1\n"
    "v_mul_f32 v206, v206, v22\n"
    "v_mul_f32 v207, v207, v23\n"
    "v_exp_f32 v204, v204\n"
    "v_exp_f32 v205, v205\n"
    "v_mfma_f32_32x32x16_bf16 v[64:79], v[176:179], a[132:135], v[64:79]\n"
    "ds_read_b64_tr_b16 v[172:173], v219 offset:0\n"
    "ds_read_b64_tr_b16 v[174:175], v219 offset:1024\n"
    "s_add_u32 m0, s65, 98304\n"
    "s_add_u32 s84, s60, 151680\n"
    "buffer_load_dwordx4 v243, s[48:51], s84 offen lds\n"
    "v_exp_f32 v206, v206\n"
    "v_mfma_f32_32x32x16_bf16 v[80:95], v[180:183], a[132:135], v[80:95]\n"
    "ds_read_b64_tr_b16 v[176:177], v216 offset:6144\n"
    "ds_read_b64_tr_b16 v[178:179], v216 offset:7168\n"
    "buffer_load_dwordx4 v226, s[48:51], s84 offen offset:1024 lds\n"
    "s_add_u32 s84, s61, 2176\n"
    "v_exp_f32 v207, v207\n"
    "v_add_f32 v204, 1.0, v204\n"
    "v_add_f32 v205, 1.0, v205\n"
    "s_waitcnt lgkmcnt(6)\n"
    "v_mfma_f32_32x32x16_bf16 v[64:79], v[184:187], a[136:139], v[64:79]\n"
    "ds_read_b64_tr_b16 v[180:181], v219 offset:6144\n"
    "ds_read_b64_tr_b16 v[182:183], v219 offset:7168\n"
    "buffer_load_dwordx4 v243, s[48:51], s84 offen offset:2048 lds\n"
    "v_add_f32 v206, 1.0, v206\n"
    "v_add_f32 v207, 1.0, v207\n"
    "v_mfma_f32_32x32x16_bf16 v[80:95], v[188:191], a[136:139], v[80:95]\n"
    "ds_read_b64_tr_b16 v[184:185], v216 offset:12288\n"
    "ds_read_b64_tr_b16 v[186:187], v216 offset:13312\n"
    "buffer_load_dwordx4 v226, s[48:51], s84 offen offset:3072 lds\n"
    "s_add_u32 m0, s65, 102400\n"
    "s_add_u32 s84, s59, 225280\n"
    "v_rcp_f32 v204, v204\n"
    "v_rcp_f32 v205, v205\n"
    "v_mfma_f32_32x32x16_bf16 v[64:79], v[192:195], a[140:143], v[64:79]\n"
    "ds_read_b64_tr_b16 v[188:189], v219 offset:12288\n"
    "ds_read_b64_tr_b16 v[190:191], v219 offset:13312\n"
    "buffer_load_dwordx4 v243, s[48:51], s84 offen lds\n"
    "v_rcp_f32 v206, v206\n"
    "v_mfma_f32_32x32x16_bf16 v[80:95], v[196:199], a[140:143], v[80:95]\n"
    "ds_read_b64_tr_b16 v[192:193], v216 offset:18432\n"
    "ds_read_b64_tr_b16 v[194:195], v216 offset:19456\n"
    "buffer_load_dwordx4 v226, s[48:51], s84 offen offset:1024 lds\n"
    "v_rcp_f32 v207, v207\n"
    "v_mul_f32 v20, v20, v204\n"
    "v_mul_f32 v21, v21, v205\n"
    "s_waitcnt lgkmcnt(6)\n"
    "v_mfma_f32_32x32x16_bf16 a[0:15], v[168:171], v[96:99], 0\n"
    "ds_read_b64_tr_b16 v[196:197], v219 offset:18432\n"
    "ds_read_b64_tr_b16 v[198:199], v219 offset:19456\n"
    "s_add_u32 s86, s73, 4480\n"
    "buffer_load_dword v255, v252, s[48:51], s86 offen\n"
    "s_add_u32 s85, s68, 17408\n"
    "buffer_load_dwordx4 a[212:215], v250, s[52:55], s85 offen sc1\n"
    "v_mul_f32 v22, v22, v206\n"
    "v_mul_f32 v23, v23, v207\n"
    "v_mul_f32 v200, v24, v24\n"
    "v_mfma_f32_32x32x16_bf16 a[16:31], v[172:175], v[96:99], 0\n"
    "ds_read_b64_tr_b16 v[168:169], v216 offset:2048\n"
    "ds_read_b64_tr_b16 v[170:171], v216 offset:3072\n"
    "v_mul_f32 v201, v25, v25\n"
    "v_mul_f32 v202, v26, v26\n"
    "v_mul_f32 v203, v27, v27\n"
    "v_fmaak_f32 v200, v200, v231, 0xc0135761\n"
    "v_mfma_f32_32x32x16_bf16 a[0:15], v[176:179], v[100:103], a[0:15]\n"
    "ds_read_b64_tr_b16 v[172:173], v219 offset:2048\n"
    "ds_read_b64_tr_b16 v[174:175], v219 offset:3072\n"
    "v_fmaak_f32 v201, v201, v231, 0xc0135761\n"
    "v_fmaak_f32 v202, v202, v231, 0xc0135761\n"
    "v_fmaak_f32 v203, v203, v231, 0xc0135761\n"
    "v_mul_f32 v200, v200, v24\n"
    "v_mfma_f32_32x32x16_bf16 a[16:31], v[180:183], v[100:103], a[16:31]\n"
    "ds_read_b64_tr_b16 v[176:177], v216 offset:8192\n"
    "ds_read_b64_tr_b16 v[178:179], v216 offset:9216\n"
    "v_mul_f32 v201, v201, v25\n"
    "v_mul_f32 v202, v202, v26\n"
    "v_mul_f32 v203, v203, v27\n"
    "s_waitcnt lgkmcnt(6)\n"
    "v_mfma_f32_32x32x16_bf16 a[0:15], v[184:187], v[104:107], a[0:15]\n"
    "ds_read_b64_tr_b16 v[180:181], v219 offset:8192\n"
    "ds_read_b64_tr_b16 v[182:183], v219 offset:9216\n"
    "v_exp_f32 v200, v200\n"
    "v_exp_f32 v201, v201\n"
    "v_mfma_f32_32x32x16_bf16 a[16:31], v[188:191], v[104:107], a[16:31]\n"
    "ds_read_b64_tr_b16 v[184:185], v216 offset:14336\n"
    "ds_read_b64_tr_b16 v[186:187], v216 offset:15360\n"
    "v_exp_f32 v202, v202\n"
    "v_exp_f32 v203, v203\n"
    "v_mfma_f32_32x32x16_bf16 a[0:15], v[192:195], v[108:111], a[0:15]\n"
    "ds_read_b64_tr_b16 v[188:189], v219 offset:14336\n"
    "ds_read_b64_tr_b16 v[190:191], v219 offset:15360\n"
    "v_add_f32 v200, 1.0, v200\n"
    "v_add_f32 v201, 1.0, v201\n"
    "v_add_f32 v202, 1.0, v202\n"
    "v_add_f32 v203, 1.0, v203\n"
    "v_mfma_f32_32x32x16_bf16 a[16:31], v[196:199], v[108:111], a[16:31]\n"
    "ds_read_b64_tr_b16 v[192:193], v216 offset:20480\n"
    "ds_read_b64_tr_b16 v[194:195], v216 offset:21504\n"
    "v_rcp_f32 v200, v200\n"
    "s_waitcnt lgkmcnt(6)\n"
    "v_mfma_f32_32x32x16_bf16 a[0:15], v[168:171], v[112:115], a[0:15]\n"
    "ds_read_b64_tr_b16 v[196:197], v219 offset:20480\n"
    "ds_read_b64_tr_b16 v[198:199], v219 offset:21504\n"
    "v_rcp_f32 v201, v201\n"
    "v_rcp_f32 v202, v202\n"
    "v_mfma_f32_32x32x16_bf16 a[16:31], v[172:175], v[112:115], a[16:31]\n"
    "ds_read_b64_tr_b16 v[168:169], v216 offset:4096\n"
    "ds_read_b64_tr_b16 v[170:171], v216 offset:5120\n"
    "v_rcp_f32 v203, v203\n"
    "v_mul_f32 v24, v24, v200\n"
    "v_mul_f32 v25, v25, v201\n"
    "v_mfma_f32_32x32x16_bf16 a[0:15], v[176:179], v[116:119], a[0:15]\n"
    "ds_read_b64_tr_b16 v[172:173], v219 offset:4096\n"
    "ds_read_b64_tr_b16 v[174:175], v219 offset:5120\n"
    "v_mul_f32 v26, v26, v202\n"
    "v_mul_f32 v27, v27, v203\n"
    "v_mul_f32 v204, v28, v28\n"
    "v_mul_f32 v205, v29, v29\n"
    "v_mfma_f32_32x32x16_bf16 a[16:31], v[180:183], v[116:119], a[16:31]\n"
    "ds_read_b64_tr_b16 v[176:177], v216 offset:10240\n"
    "ds_read_b64_tr_b16 v[178:179], v216 offset:11264\n"
    "v_mul_f32 v206, v30, v30\n"
    "v_mul_f32 v207, v31, v31\n"
    "v_fmaak_f32 v204, v204, v231, 0xc0135761\n"
    "s_waitcnt lgkmcnt(6)\n"
    "v_mfma_f32_32x32x16_bf16 a[0:15], v[184:187], v[120:123], a[0:15]\n"
    "ds_read_b64_tr_b16 v[180:181], v219 offset:10240\n"
    "ds_read_b64_tr_b16 v[182:183], v219 offset:11264\n"
    "v_fmaak_f32 v205, v205, v231, 0xc0135761\n"
    "v_fmaak_f32 v206, v206, v231, 0xc0135761\n"
    "v_fmaak_f32 v207, v207, v231, 0xc0135761\n"
    "v_mul_f32 v204, v204, v28\n"
    "v_mfma_f32_32x32x16_bf16 a[16:31], v[188:191], v[120:123], a[16:31]\n"
    "ds_read_b64_tr_b16 v[184:185], v216 offset:16384\n"
    "ds_read_b64_tr_b16 v[186:187], v216 offset:17408\n"
    "v_mul_f32 v205, v205, v29\n"
    "v_mul_f32 v206, v206, v30\n"
    "v_mul_f32 v207, v207, v31\n"
    "v_mfma_f32_32x32x16_bf16 a[0:15], v[192:195], v[124:127], a[0:15]\n"
    "ds_read_b64_tr_b16 v[188:189], v219 offset:16384\n"
    "ds_read_b64_tr_b16 v[190:191], v219 offset:17408\n"
    "v_exp_f32 v204, v204\n"
    "v_exp_f32 v205, v205\n"
    "v_mfma_f32_32x32x16_bf16 a[16:31], v[196:199], v[124:127], a[16:31]\n"
    "ds_read_b64_tr_b16 v[192:193], v216 offset:22528\n"
    "ds_read_b64_tr_b16 v[194:195], v216 offset:23552\n"
    "v_exp_f32 v206, v206\n"
    "s_waitcnt lgkmcnt(6)\n"
    "v_mfma_f32_32x32x16_bf16 a[0:15], v[168:171], v[128:131], a[0:15]\n"
    "s_barrier\n"
    "v_mfma_f32_32x32x16_bf16 a[16:31], v[172:175], v[128:131], a[16:31]\n"
    "ds_read_b64_tr_b16 v[196:197], v219 offset:22528\n"
    "ds_read_b64_tr_b16 v[198:199], v219 offset:23552\n"
    "ds_read_b64_tr_b16 v[168:169], v218 offset:8192\n"
    "ds_read_b64_tr_b16 v[170:171], v218 offset:9216\n"
    "s_add_u32 m0, s65, 49152\n"
    "s_add_u32 s84, s59, 225408\n"
    "v_mfma_f32_32x32x16_bf16 a[0:15], v[176:179], v[132:135], a[0:15]\n"
    "ds_read_b64_tr_b16 v[172:173], v221 offset:8192\n"
    "ds_read_b64_tr_b16 v[174:175], v221 offset:9216\n"
    "buffer_load_dwordx4 v243, s[48:51], s84 offen lds\n"
    "v_mfma_f32_32x32x16_bf16 a[16:31], v[180:183], v[132:135], a[16:31]\n"
    "ds_read_b64_tr_b16 v[176:177], v218 offset:14336\n"
    "ds_read_b64_tr_b16 v[178:179], v218 offset:15360\n"
    "buffer_load_dwordx4 v226, s[48:51], s84 offen offset:1024 lds\n"
    "s_add_u32 s84, s59, 2304\n"
    "v_exp_f32 v207, v207\n"
    "v_add_f32 v204, 1.0, v204\n"
    "s_waitcnt lgkmcnt(6)\n"
    "v_mfma_f32_32x32x16_bf16 a[0:15], v[184:187], v[136:139], a[0:15]\n"
    "ds_read_b64_tr_b16 v[180:181], v221 offset:14336\n"
    "ds_read_b64_tr_b16 v[182:183], v221 offset:15360\n"
    "buffer_load_dwordx4 v243, s[48:51], s84 offen offset:2048 lds\n"
    "v_add_f32 v205, 1.0, v205\n"
    "v_add_f32 v206, 1.0, v206\n"
    "v_add_f32 v207, 1.0, v207\n"
    "v_mfma_f32_32x32x16_bf16 a[16:31], v[188:191], v[136:139], a[16:31]\n"
    "ds_read_b64_tr_b16 v[184:185], v218 offset:20480\n"
    "ds_read_b64_tr_b16 v[186:187], v218 offset:21504\n"
    "buffer_load_dwordx4 v226, s[48:51], s84 offen offset:3072 lds\n"
    "s_add_u32 m0, s65, 53248\n"
    "s_add_u32 s84, s59, 78080\n"
    "v_rcp_f32 v204, v204\n"
    "v_mfma_f32_32x32x16_bf16 a[0:15], v[192:195], v[140:143], a[0:15]\n"
    "ds_read_b64_tr_b16 v[188:189], v221 offset:20480\n"
    "ds_read_b64_tr_b16 v[190:191], v221 offset:21504\n"
    "buffer_load_dwordx4 v243, s[48:51], s84 offen lds\n"
    "v_rcp_f32 v205, v205\n"
    "v_rcp_f32 v206, v206\n"
    "v_mfma_f32_32x32x16_bf16 a[16:31], v[196:199], v[140:143], a[16:31]\n"
    "ds_read_b64_tr_b16 v[192:193], v218 offset:26624\n"
    "ds_read_b64_tr_b16 v[194:195], v218 offset:27648\n"
    "buffer_load_dwordx4 v226, s[48:51], s84 offen offset:1024 lds\n"
    "v_rcp_f32 v207, v207\n"
    "v_mul_f32 v28, v28, v204\n"
    "s_waitcnt vmcnt(26)\n"
    "s_waitcnt lgkmcnt(6)\n"
    "v_mfma_f32_32x32x16_bf16 a[0:15], v[168:171], a[144:147], a[0:15]\n"
    "ds_read_b64_tr_b16 v[196:197], v221 offset:26624\n"
    "ds_read_b64_tr_b16 v[198:199], v221 offset:27648\n"
    "v_mul_f32 v29, v29, v205\n"
    "v_mul_f32 v30, v30, v206\n"
    "v_mul_f32 v31, v31, v207\n"
    "v_mul_f32 v0, v0, v64\n"
    "v_mfma_f32_32x32x16_bf16 a[16:31], v[172:175], a[144:147], a[16:31]\n"
    "ds_read_b64_tr_b16 v[168:169], v218 offset:10240\n"
    "ds_read_b64_tr_b16 v[170:171], v218 offset:11264\n"
    "v_mul_f32 v1, v1, v65\n"
    "v_mul_f32 v2, v2, v66\n"
    "v_mul_f32 v3, v3, v67\n"
    "v_mul_f32 v4, v4, v68\n"
    "s_waitcnt vmcnt(25)\n"
    "v_mfma_f32_32x32x16_bf16 a[0:15], v[176:179], a[148:151], a[0:15]\n"
    "ds_read_b64_tr_b16 v[172:173], v221 offset:10240\n"
    "ds_read_b64_tr_b16 v[174:175], v221 offset:11264\n"
    "v_mul_f32 v5, v5, v69\n"
    "v_mul_f32 v6, v6, v70\n"
    "v_mul_f32 v7, v7, v71\n"
    "v_cvt_pk_bf16_f32 v144, v0, v1\n"
    "v_mfma_f32_32x32x16_bf16 a[16:31], v[180:183], a[148:151], a[16:31]\n"
    "ds_read_b64_tr_b16 v[176:177], v218 offset:16384\n"
    "ds_read_b64_tr_b16 v[178:179], v218 offset:17408\n"
    "v_cvt_pk_bf16_f32 v145, v2, v3\n"
    "v_cvt_pk_bf16_f32 v146, v4, v5\n"
    "v_cvt_pk_bf16_f32 v147, v6, v7\n"
    "s_waitcnt vmcnt(24)\n"
    "s_waitcnt lgkmcnt(6)\n"
    "v_mfma_f32_32x32x16_bf16 a[0:15], v[184:187], a[152:155], a[0:15]\n"
    "ds_read_b64_tr_b16 v[180:181], v221 offset:16384\n"
    "ds_read_b64_tr_b16 v[182:183], v221 offset:17408\n"
    "s_add_u32 s85, s67, 12288\n"
    "buffer_store_dwordx4 v[144:147], v250, s[52:55], s85 offen sc1\n"
    "v_mfma_f32_32x32x16_bf16 a[16:31], v[188:191], a[152:155], a[16:31]\n"
    "ds_read_b64_tr_b16 v[184:185], v218 offset:22528\n"
    "ds_read_b64_tr_b16 v[186:187], v218 offset:23552\n"
    "v_mul_f32 v8, v8, v72\n"
    "v_mul_f32 v9, v9, v73\n"
    "v_mul_f32 v10, v10, v74\n"
    "v_mul_f32 v11, v11, v75\n"
    "s_waitcnt vmcnt(24)\n"
    "v_mfma_f32_32x32x16_bf16 a[0:15], v[192:195], a[156:159], a[0:15]\n"
    "ds_read_b64_tr_b16 v[188:189], v221 offset:22528\n"
    "ds_read_b64_tr_b16 v[190:191], v221 offset:23552\n"
    "v_mul_f32 v12, v12, v76\n"
    "v_mul_f32 v13, v13, v77\n"
    "v_mul_f32 v14, v14, v78\n"
    "v_mul_f32 v15, v15, v79\n"
    "v_mfma_f32_32x32x16_bf16 a[16:31], v[196:199], a[156:159], a[16:31]\n"
    "ds_read_b64_tr_b16 v[192:193], v218 offset:28672\n"
    "ds_read_b64_tr_b16 v[194:195], v218 offset:29696\n"
    "v_cvt_pk_bf16_f32 v148, v8, v9\n"
    "v_cvt_pk_bf16_f32 v149, v10, v11\n"
    "v_cvt_pk_bf16_f32 v150, v12, v13\n"
    "s_waitcnt vmcnt(23)\n"
    "s_waitcnt lgkmcnt(6)\n"
    "v_mfma_f32_32x32x16_bf16 a[0:15], v[168:171], a[160:163], a[0:15]\n"
    "ds_read_b64_tr_b16 v[196:197], v221 offset:28672\n"
    "ds_read_b64_tr_b16 v[198:199], v221 offset:29696\n"
    "v_cvt_pk_bf16_f32 v151, v14, v15\n"
    "v_mfma_f32_32x32x16_bf16 a[16:31], v[172:175], a[160:163], a[16:31]\n"
    "ds_read_b64_tr_b16 v[168:169], v218 offset:12288\n"
    "ds_read_b64_tr_b16 v[170:171], v218 offset:13312\n"
    "s_add_u32 s85, s67, 13312\n"
    "buffer_store_dwordx4 v[148:151], v250, s[52:55], s85 offen sc1\n"
    "s_waitcnt vmcnt(23)\n"
    "v_mfma_f32_32x32x16_bf16 a[0:15], v[176:179], a[164:167], a[0:15]\n"
    "ds_read_b64_tr_b16 v[172:173], v221 offset:12288\n"
    "ds_read_b64_tr_b16 v[174:175], v221 offset:13312\n"
    "v_mul_f32 v16, v16, v80\n"
    "v_mul_f32 v17, v17, v81\n"
    "v_mul_f32 v18, v18, v82\n"
    "v_mul_f32 v19, v19, v83\n"
    "v_mfma_f32_32x32x16_bf16 a[16:31], v[180:183], a[164:167], a[16:31]\n"
    "ds_read_b64_tr_b16 v[176:177], v218 offset:18432\n"
    "ds_read_b64_tr_b16 v[178:179], v218 offset:19456\n"
    "v_mul_f32 v20, v20, v84\n"
    "v_mul_f32 v21, v21, v85\n"
    "v_mul_f32 v22, v22, v86\n"
    "s_waitcnt vmcnt(22)\n"
    "s_waitcnt lgkmcnt(6)\n"
    "v_mfma_f32_32x32x16_bf16 a[0:15], v[184:187], a[168:171], a[0:15]\n"
    "ds_read_b64_tr_b16 v[180:181], v221 offset:18432\n"
    "ds_read_b64_tr_b16 v[182:183], v221 offset:19456\n"
    "v_mul_f32 v23, v23, v87\n"
    "v_cvt_pk_bf16_f32 v152, v16, v17\n"
    "v_cvt_pk_bf16_f32 v153, v18, v19\n"
    "v_cvt_pk_bf16_f32 v154, v20, v21\n"
    "v_mfma_f32_32x32x16_bf16 a[16:31], v[188:191], a[168:171], a[16:31]\n"
    "ds_read_b64_tr_b16 v[184:185], v218 offset:24576\n"
    "ds_read_b64_tr_b16 v[186:187], v218 offset:25600\n"
    "v_cvt_pk_bf16_f32 v155, v22, v23\n"
    "s_waitcnt vmcnt(21)\n"
    "v_mfma_f32_32x32x16_bf16 a[0:15], v[192:195], a[172:175], a[0:15]\n"
    "ds_read_b64_tr_b16 v[188:189], v221 offset:24576\n"
    "ds_read_b64_tr_b16 v[190:191], v221 offset:25600\n"
    "s_add_u32 s85, s67, 14336\n"
    "buffer_store_dwordx4 v[152:155], v250, s[52:55], s85 offen sc1\n"
    "v_mfma_f32_32x32x16_bf16 a[16:31], v[196:199], a[172:175], a[16:31]\n"
    "ds_read_b64_tr_b16 v[192:193], v218 offset:30720\n"
    "ds_read_b64_tr_b16 v[194:195], v218 offset:31744\n"
    "v_mul_f32 v24, v24, v88\n"
    "v_mul_f32 v25, v25, v89\n"
    "v_mul_f32 v26, v26, v90\n"
    "s_waitcnt vmcnt(21)\n"
    "s_waitcnt lgkmcnt(6)\n"
    "v_mfma_f32_32x32x16_bf16 a[0:15], v[168:171], a[176:179], a[0:15]\n"
    "s_barrier\n"
    "v_mfma_f32_32x32x16_bf16 a[16:31], v[172:175], a[176:179], a[16:31]\n"
    "ds_read_b64_tr_b16 v[196:197], v221 offset:30720\n"
    "ds_read_b64_tr_b16 v[198:199], v221 offset:31744\n"
    "ds_read_b64_tr_b16 v[168:169], v216 offset:24576\n"
    "ds_read_b64_tr_b16 v[170:171], v216 offset:25600\n"
    "s_add_u32 m0, s65, 0\n"
    "s_add_u32 s84, s60, 225280\n"
    "s_waitcnt vmcnt(20)\n"
    "v_mfma_f32_32x32x16_bf16 a[0:15], v[176:179], a[180:183], a[0:15]\n"
    "ds_read_b64_tr_b16 v[172:173], v219 offset:24576\n"
    "ds_read_b64_tr_b16 v[174:175], v219 offset:25600\n"
    "buffer_load_dwordx4 v243, s[48:51], s84 offen lds\n"
    "v_mul_f32 v27, v27, v91\n"
    "v_mfma_f32_32x32x16_bf16 a[16:31], v[180:183], a[180:183], a[16:31]\n"
    "ds_read_b64_tr_b16 v[176:177], v216 offset:30720\n"
    "ds_read_b64_tr_b16 v[178:179], v216 offset:31744\n"
    "buffer_load_dwordx4 v226, s[48:51], s84 offen offset:1024 lds\n"
    "s_add_u32 s84, s60, 223360\n"
    "s_waitcnt vmcnt(21)\n"
    "s_waitcnt lgkmcnt(6)\n"
    "v_mfma_f32_32x32x16_bf16 a[0:15], v[184:187], a[184:187], a[0:15]\n"
    "ds_read_b64_tr_b16 v[180:181], v219 offset:30720\n"
    "ds_read_b64_tr_b16 v[182:183], v219 offset:31744\n"
    "buffer_load_dwordx4 v243, s[48:51], s84 offen offset:2048 lds\n"
    "v_mul_f32 v28, v28, v92\n"
    "v_mul_f32 v29, v29, v93\n"
    "v_mfma_f32_32x32x16_bf16 a[16:31], v[188:191], a[184:187], a[16:31]\n"
    "ds_read_b64_tr_b16 v[184:185], v216 offset:36864\n"
    "ds_read_b64_tr_b16 v[186:187], v216 offset:37888\n"
    "buffer_load_dwordx4 v226, s[48:51], s84 offen offset:3072 lds\n"
    "s_add_u32 m0, s65, 4096\n"
    "s_add_u32 s84, s59, 151808\n"
    "v_mul_f32 v30, v30, v94\n"
    "v_mul_f32 v31, v31, v95\n"
    "v_cvt_pk_bf16_f32 v156, v24, v25\n"
    "v_cvt_pk_bf16_f32 v157, v26, v27\n"
    "s_waitcnt vmcnt(22)\n"
    "v_mfma_f32_32x32x16_bf16 a[0:15], v[192:195], a[188:191], a[0:15]\n"
    "ds_read_b64_tr_b16 v[188:189], v219 offset:36864\n"
    "ds_read_b64_tr_b16 v[190:191], v219 offset:37888\n"
    "buffer_load_dwordx4 v243, s[48:51], s84 offen lds\n"
    "v_cvt_pk_bf16_f32 v158, v28, v29\n"
    "v_cvt_pk_bf16_f32 v159, v30, v31\n"
    "v_mfma_f32_32x32x16_bf16 a[16:31], v[196:199], a[188:191], a[16:31]\n"
    "ds_read_b64_tr_b16 v[192:193], v216 offset:43008\n"
    "ds_read_b64_tr_b16 v[194:195], v216 offset:44032\n"
    "buffer_load_dwordx4 v226, s[48:51], s84 offen offset:1024 lds\n"
    "s_waitcnt lgkmcnt(6)\n"
    "v_mfma_f32_32x32x16_bf16 a[0:15], v[168:171], v[160:163], a[0:15]\n"
    "ds_read_b64_tr_b16 v[196:197], v219 offset:43008\n"
    "ds_read_b64_tr_b16 v[198:199], v219 offset:44032\n"
    "s_add_u32 s85, s67, 15360\n"
    "buffer_store_dwordx4 v[156:159], v250, s[52:55], s85 offen sc1\n"
    "v_mfma_f32_32x32x16_bf16 a[16:31], v[172:175], v[160:163], a[16:31]\n"
    "ds_read_b64_tr_b16 v[168:169], v216 offset:26624\n"
    "ds_read_b64_tr_b16 v[170:171], v216 offset:27648\n"
    "v_mfma_f32_32x32x16_bf16 a[0:15], v[176:179], v[164:167], a[0:15]\n"
    "ds_read_b64_tr_b16 v[172:173], v219 offset:26624\n"
    "ds_read_b64_tr_b16 v[174:175], v219 offset:27648\n"
    "v_mfma_f32_32x32x16_bf16 a[16:31], v[180:183], v[164:167], a[16:31]\n"
    "ds_read_b64_tr_b16 v[176:177], v216 offset:32768\n"
    "ds_read_b64_tr_b16 v[178:179], v216 offset:33792\n"
    "s_waitcnt vmcnt(24)\n"
    "s_waitcnt lgkmcnt(6)\n"
    "v_mfma_f32_32x32x16_bf16 a[0:15], v[184:187], a[208:211], a[0:15]\n"
    "ds_read_b64_tr_b16 v[180:181], v219 offset:32768\n"
    "ds_read_b64_tr_b16 v[182:183], v219 offset:33792\n"
    "v_mfma_f32_32x32x16_bf16 a[16:31], v[188:191], a[208:211], a[16:31]\n"
    "ds_read_b64_tr_b16 v[184:185], v216 offset:38912\n"
    "ds_read_b64_tr_b16 v[186:187], v216 offset:39936\n"
    "s_waitcnt vmcnt(16)\n"
    "v_mfma_f32_32x32x16_bf16 a[0:15], v[192:195], a[212:215], a[0:15]\n"
    "ds_read_b64_tr_b16 v[188:189], v219 offset:38912\n"
    "ds_read_b64_tr_b16 v[190:191], v219 offset:39936\n"
    "v_mfma_f32_32x32x16_bf16 a[16:31], v[196:199], a[212:215], a[16:31]\n"
    "ds_read_b64_tr_b16 v[192:193], v216 offset:45056\n"
    "ds_read_b64_tr_b16 v[194:195], v216 offset:46080\n"
    "s_waitcnt lgkmcnt(6)\n"
    "v_mfma_f32_32x32x16_bf16 a[32:47], v[168:171], v[96:99], 0\n"
    "ds_read_b64_tr_b16 v[196:197], v219 offset:45056\n"
    "ds_read_b64_tr_b16 v[198:199], v219 offset:46080\n"
    "s_sub_u32 s86, s62, s59\n"
    "s_add_u32 s86, s86, s73\n"
    "s_add_u32 s86, s86, 4096\n"
    "buffer_load_dword v255, v252, s[48:51], s86 offen\n"
    "v_mfma_f32_32x32x16_bf16 a[48:63], v[172:175], v[96:99], 0\n"
    "ds_read_b64_tr_b16 v[168:169], v216 offset:28672\n"
    "ds_read_b64_tr_b16 v[170:171], v216 offset:29696\n"
    "v_mfma_f32_32x32x16_bf16 a[32:47], v[176:179], v[100:103], a[32:47]\n"
    "ds_read_b64_tr_b16 v[172:173], v219 offset:28672\n"
    "ds_read_b64_tr_b16 v[174:175], v219 offset:29696\n"
    "v_mfma_f32_32x32x16_bf16 a[48:63], v[180:183], v[100:103], a[48:63]\n"
    "ds_read_b64_tr_b16 v[176:177], v216 offset:34816\n"
    "ds_read_b64_tr_b16 v[178:179], v216 offset:35840\n"
    "s_waitcnt lgkmcnt(6)\n"
    "v_mfma_f32_32x32x16_bf16 a[32:47], v[184:187], v[104:107], a[32:47]\n"
    "ds_read_b64_tr_b16 v[180:181], v219 offset:34816\n"
    "ds_read_b64_tr_b16 v[182:183], v219 offset:35840\n"
    "v_mfma_f32_32x32x16_bf16 a[48:63], v[188:191], v[104:107], a[48:63]\n"
    "ds_read_b64_tr_b16 v[184:185], v216 offset:40960\n"
    "ds_read_b64_tr_b16 v[186:187], v216 offset:41984\n"
    "v_mfma_f32_32x32x16_bf16 a[32:47], v[192:195], v[108:111], a[32:47]\n"
    "ds_read_b64_tr_b16 v[188:189], v219 offset:40960\n"
    "ds_read_b64_tr_b16 v[190:191], v219 offset:41984\n"
    "v_mfma_f32_32x32x16_bf16 a[48:63], v[196:199], v[108:111], a[48:63]\n"
    "ds_read_b64_tr_b16 v[192:193], v216 offset:47104\n"
    "ds_read_b64_tr_b16 v[194:195], v216 offset:48128\n"
    "s_waitcnt lgkmcnt(6)\n"
    "v_mfma_f32_32x32x16_bf16 a[32:47], v[168:171], v[112:115], a[32:47]\n"
    "s_barrier\n"
    "v_mfma_f32_32x32x16_bf16 a[48:63], v[172:175], v[112:115], a[48:63]\n"
    "ds_read_b64_tr_b16 v[196:197], v219 offset:47104\n"
    "ds_read_b64_tr_b16 v[198:199], v219 offset:48128\n"
    "ds_read_b64_tr_b16 v[168:169], v217 offset:8192\n"
    "ds_read_b64_tr_b16 v[170:171], v217 offset:9216\n"
    "s_add_u32 m0, s65, 139264\n"
    "s_add_u32 s84, s60, 4352\n"
    "v_mfma_f32_32x32x16_bf16 a[32:47], v[176:179], v[116:119], a[32:47]\n"
    "ds_read_b64_tr_b16 v[172:173], v220 offset:8192\n"
    "ds_read_b64_tr_b16 v[174:175], v220 offset:9216\n"
    "buffer_load_dwordx4 v243, s[48:51], s84 offen lds\n"
    "v_mfma_f32_32x32x16_bf16 a[48:63], v[180:183], v[116:119], a[48:63]\n"
    "ds_read_b64_tr_b16 v[176:177], v217 offset:14336\n"
    "ds_read_b64_tr_b16 v[178:179], v217 offset:15360\n"
    "buffer_load_dwordx4 v226, s[48:51], s84 offen offset:1024 lds\n"
    "s_add_u32 s84, s60, 76032\n"
    "s_waitcnt lgkmcnt(6)\n"
    "v_mfma_f32_32x32x16_bf16 a[32:47], v[184:187], v[120:123], a[32:47]\n"
    "ds_read_b64_tr_b16 v[180:181], v220 offset:14336\n"
    "ds_read_b64_tr_b16 v[182:183], v220 offset:15360\n"
    "buffer_load_dwordx4 v243, s[48:51], s84 offen offset:2048 lds\n"
    "v_mfma_f32_32x32x16_bf16 a[48:63], v[188:191], v[120:123], a[48:63]\n"
    "ds_read_b64_tr_b16 v[184:185], v217 offset:20480\n"
    "ds_read_b64_tr_b16 v[186:187], v217 offset:21504\n"
    "buffer_load_dwordx4 v226, s[48:51], s84 offen offset:3072 lds\n"
    "s_add_u32 m0, s65, 143360\n"
    "s_add_u32 s84, s60, 151808\n"
    "v_mfma_f32_32x32x16_bf16 a[32:47], v[192:195], v[124:127], a[32:47]\n"
    "ds_read_b64_tr_b16 v[188:189], v220 offset:20480\n"
    "ds_read_b64_tr_b16 v[190:191], v220 offset:21504\n"
    "buffer_load_dwordx4 v243, s[48:51], s84 offen lds\n"
    "v_mfma_f32_32x32x16_bf16 a[48:63], v[196:199], v[124:127], a[48:63]\n"
    "ds_read_b64_tr_b16 v[192:193], v217 offset:26624\n"
    "ds_read_b64_tr_b16 v[194:195], v217 offset:27648\n"
    "buffer_load_dwordx4 v226, s[48:51], s84 offen offset:1024 lds\n"
    "s_waitcnt lgkmcnt(6)\n"
    "v_mfma_f32_32x32x16_bf16 a[32:47], v[168:171], v[128:131], a[32:47]\n"
    "ds_read_b64_tr_b16 v[196:197], v220 offset:26624\n"
    "ds_read_b64_tr_b16 v[198:199], v220 offset:27648\n"
    "s_waitcnt vmcnt(7)\n"
    "v_add_u32 v238, 2, v230\n"
    "buffer_store_dword v238, off, s[52:55], s69 sc1\n"
    "v_mfma_f32_32x32x16_bf16 a[48:63], v[172:175], v[128:131], a[48:63]\n"
    "ds_read_b64_tr_b16 v[168:169], v217 offset:10240\n"
    "ds_read_b64_tr_b16 v[170:171], v217 offset:11264\n"
    "v_mfma_f32_32x32x16_bf16 a[32:47], v[176:179], v[132:135], a[32:47]\n"
    "ds_read_b64_tr_b16 v[172:173], v220 offset:10240\n"
    "ds_read_b64_tr_b16 v[174:175], v220 offset:11264\n"
    "v_mfma_f32_32x32x16_bf16 a[48:63], v[180:183], v[132:135], a[48:63]\n"
    "ds_read_b64_tr_b16 v[176:177], v217 offset:16384\n"
    "ds_read_b64_tr_b16 v[178:179], v217 offset:17408\n"
    "s_waitcnt lgkmcnt(6)\n"
    "v_mfma_f32_32x32x16_bf16 a[32:47], v[184:187], v[136:139], a[32:47]\n"
    "ds_read_b64_tr_b16 v[180:181], v220 offset:16384\n"
    "ds_read_b64_tr_b16 v[182:183], v220 offset:17408\n"
    "v_mfma_f32_32x32x16_bf16 a[48:63], v[188:191], v[136:139], a[48:63]\n"
    "ds_read_b64_tr_b16 v[184:185], v217 offset:22528\n"
    "ds_read_b64_tr_b16 v[186:187], v217 offset:23552\n"
    "v_mfma_f32_32x32x16_bf16 a[32:47], v[192:195], v[140:143], a[32:47]\n"
    "ds_read_b64_tr_b16 v[188:189], v220 offset:22528\n"
    "ds_read_b64_tr_b16 v[190:191], v220 offset:23552\n"
    "v_mfma_f32_32x32x16_bf16 a[48:63], v[196:199], v[140:143], a[48:63]\n"
    "ds_read_b64_tr_b16 v[192:193], v217 offset:28672\n"
    "ds_read_b64_tr_b16 v[194:195], v217 offset:29696\n"
    "s_waitcnt lgkmcnt(6)\n"
    "v_mfma_f32_32x32x16_bf16 a[32:47], v[168:171], a[144:147], a[32:47]\n"
    "ds_read_b64_tr_b16 v[196:197], v220 offset:28672\n"
    "ds_read_b64_tr_b16 v[198:199], v220 offset:29696\n"
    "v_mfma_f32_32x32x16_bf16 a[48:63], v[172:175], a[144:147], a[48:63]\n"
    "ds_read_b64_tr_b16 v[168:169], v217 offset:12288\n"
    "ds_read_b64_tr_b16 v[170:171], v217 offset:13312\n"
    "v_mfma_f32_32x32x16_bf16 a[32:47], v[176:179], a[148:151], a[32:47]\n"
    "ds_read_b64_tr_b16 v[172:173], v220 offset:12288\n"
    "ds_read_b64_tr_b16 v[174:175], v220 offset:13312\n"
    "v_mfma_f32_32x32x16_bf16 a[48:63], v[180:183], a[148:151], a[48:63]\n"
    "ds_read_b64_tr_b16 v[176:177], v217 offset:18432\n"
    "ds_read_b64_tr_b16 v[178:179], v217 offset:19456\n"
    "s_waitcnt lgkmcnt(6)\n"
    "v_mfma_f32_32x32x16_bf16 a[32:47], v[184:187], a[152:155], a[32:47]\n"
    "ds_read_b64_tr_b16 v[180:181], v220 offset:18432\n"
    "ds_read_b64_tr_b16 v[182:183], v220 offset:19456\n"
    "v_mfma_f32_32x32x16_bf16 a[48:63], v[188:191], a[152:155], a[48:63]\n"
    "ds_read_b64_tr_b16 v[184:185], v217 offset:24576\n"
    "ds_read_b64_tr_b16 v[186:187], v217 offset:25600\n"
    "v_mfma_f32_32x32x16_bf16 a[32:47], v[192:195], a[156:159], a[32:47]\n"
    "ds_read_b64_tr_b16 v[188:189], v220 offset:24576\n"
    "ds_read_b64_tr_b16 v[190:191], v220 offset:25600\n"
    "v_mfma_f32_32x32x16_bf16 a[48:63], v[196:199], a[156:159], a[48:63]\n"
    "ds_read_b64_tr_b16 v[192:193], v217 offset:30720\n"
    "ds_read_b64_tr_b16 v[194:195], v217 offset:31744\n"
    "s_waitcnt lgkmcnt(6)\n"
    "v_mfma_f32_32x32x16_bf16 a[32:47], v[168:171], a[160:163], a[32:47]\n"
    "s_barrier\n"
    "buffer_load_dword v229, off, s[52:55], s70 sc1\n"
    "v_mfma_f32_32x32x16_bf16 a[48:63], v[172:175], a[160:163], a[48:63]\n"
    "ds_read_b64_tr_b16 v[196:197], v220 offset:30720\n"
    "ds_read_b64_tr_b16 v[198:199], v220 offset:31744\n"
    "ds_read_b64_tr_b16 v[168:169], v217 offset:32768\n"
    "ds_read_b64_tr_b16 v[170:171], v217 offset:33792\n"
    "s_add_u32 m0, s65, 24576\n"
    "s_add_u32 s84, s61, 4352\n"
    "v_mfma_f32_32x32x16_bf16 a[32:47], v[176:179], a[164:167], a[32:47]\n"
    "ds_read_b64_tr_b16 v[172:173], v220 offset:32768\n"
    "ds_read_b64_tr_b16 v[174:175], v220 offset:33792\n"
    "buffer_load_dwordx4 v243, s[48:51], s84 offen lds\n"
    "v_mfma_f32_32x32x16_bf16 a[48:63], v[180:183], a[164:167], a[48:63]\n"
    "ds_read_b64_tr_b16 v[176:177], v217 offset:38912\n"
    "ds_read_b64_tr_b16 v[178:179], v217 offset:39936\n"
    "buffer_load_dwordx4 v226, s[48:51], s84 offen offset:1024 lds\n"
    "s_add_u32 s84, s59, 223488\n"
    "s_waitcnt lgkmcnt(6)\n"
    "v_mfma_f32_32x32x16_bf16 a[32:47], v[184:187], a[168:171], a[32:47]\n"
    "ds_read_b64_tr_b16 v[180:181], v220 offset:38912\n"
    "ds_read_b64_tr_b16 v[182:183], v220 offset:39936\n"
    "buffer_load_dwordx4 v243, s[48:51], s84 offen offset:2048 lds\n"
    "v_mfma_f32_32x32x16_bf16 a[48:63], v[188:191], a[168:171], a[48:63]\n"
    "ds_read_b64_tr_b16 v[184:185], v217 offset:45056\n"
    "ds_read_b64_tr_b16 v[186:187], v217 offset:46080\n"
    "buffer_load_dwordx4 v226, s[48:51], s84 offen offset:3072 lds\n"
    "s_add_u32 m0, s65, 28672\n"
    "s_add_u32 s84, s60, 225536\n"
    "v_mfma_f32_32x32x16_bf16 a[32:47], v[192:195], a[172:175], a[32:47]\n"
    "ds_read_b64_tr_b16 v[188:189], v220 offset:45056\n"
    "ds_read_b64_tr_b16 v[190:191], v220 offset:46080\n"
    "buffer_load_dwordx4 v243, s[48:51], s84 offen lds\n"
    "v_mfma_f32_32x32x16_bf16 a[48:63], v[196:199], a[172:175], a[48:63]\n"
    "ds_read_b64_tr_b16 v[192:193], v217 offset:51200\n"
    "ds_read_b64_tr_b16 v[194:195], v217 offset:52224\n"
    "buffer_load_dwordx4 v226, s[48:51], s84 offen offset:1024 lds\n"
    "s_waitcnt lgkmcnt(6)\n"
    "v_mfma_f32_32x32x16_bf16 a[32:47], v[168:171], a[176:179], a[32:47]\n"
    "ds_read_b64_tr_b16 v[196:197], v220 offset:51200\n"
    "ds_read_b64_tr_b16 v[198:199], v220 offset:52224\n"
    "v_mfma_f32_32x32x16_bf16 a[48:63], v[172:175], a[176:179], a[48:63]\n"
    "ds_read_b64_tr_b16 v[168:169], v217 offset:34816\n"
    "ds_read_b64_tr_b16 v[170:171], v217 offset:35840\n"
    "v_mfma_f32_32x32x16_bf16 a[32:47], v[176:179], a[180:183], a[32:47]\n"
    "ds_read_b64_tr_b16 v[172:173], v220 offset:34816\n"
    "ds_read_b64_tr_b16 v[174:175], v220 offset:35840\n"
    "v_mfma_f32_32x32x16_bf16 a[48:63], v[180:183], a[180:183], a[48:63]\n"
    "ds_read_b64_tr_b16 v[176:177], v217 offset:40960\n"
    "ds_read_b64_tr_b16 v[178:179], v217 offset:41984\n"
    "s_waitcnt lgkmcnt(6)\n"
    "v_mfma_f32_32x32x16_bf16 a[32:47], v[184:187], a[184:187], a[32:47]\n"
    "ds_read_b64_tr_b16 v[180:181], v220 offset:40960\n"
    "ds_read_b64_tr_b16 v[182:183], v220 offset:41984\n"
    "v_mfma_f32_32x32x16_bf16 a[48:63], v[188:191], a[184:187], a[48:63]\n"
    "ds_read_b64_tr_b16 v[184:185], v217 offset:47104\n"
    "ds_read_b64_tr_b16 v[186:187], v217 offset:48128\n"
    "v_mfma_f32_32x32x16_bf16 a[32:47], v[192:195], a[188:191], a[32:47]\n"
    "ds_read_b64_tr_b16 v[188:189], v220 offset:47104\n"
    "ds_read_b64_tr_b16 v[190:191], v220 offset:48128\n"
    "v_mfma_f32_32x32x16_bf16 a[48:63], v[196:199], a[188:191], a[48:63]\n"
    "ds_read_b64_tr_b16 v[192:193], v217 offset:53248\n"
    "ds_read_b64_tr_b16 v[194:195], v217 offset:54272\n"
    "s_waitcnt lgkmcnt(6)\n"
    "v_mfma_f32_32x32x16_bf16 a[32:47], v[168:171], v[160:163], a[32:47]\n"
    "ds_read_b64_tr_b16 v[196:197], v220 offset:53248\n"
    "ds_read_b64_tr_b16 v[198:199], v220 offset:54272\n"
    "v_mfma_f32_32x32x16_bf16 a[48:63], v[172:175], v[160:163], a[48:63]\n"
    "ds_read_b64_tr_b16 v[168:169], v217 offset:36864\n"
    "ds_read_b64_tr_b16 v[170:171], v217 offset:37888\n"
    "v_mfma_f32_32x32x16_bf16 a[32:47], v[176:179], v[164:167], a[32:47]\n"
    "ds_read_b64_tr_b16 v[172:173], v220 offset:36864\n"
    "ds_read_b64_tr_b16 v[174:175], v220 offset:37888\n"
    "v_mfma_f32_32x32x16_bf16 a[48:63], v[180:183], v[164:167], a[48:63]\n"
    "ds_read_b64_tr_b16 v[176:177], v217 offset:43008\n"
    "ds_read_b64_tr_b16 v[178:179], v217 offset:44032\n"
    "s_waitcnt lgkmcnt(6)\n"
    "v_mfma_f32_32x32x16_bf16 a[32:47], v[184:187], a[208:211], a[32:47]\n"
    "ds_read_b64_tr_b16 v[180:181], v220 offset:43008\n"
    "ds_read_b64_tr_b16 v[182:183], v220 offset:44032\n"
    "v_mfma_f32_32x32x16_bf16 a[48:63], v[188:191], a[208:211], a[48:63]\n"
    "ds_read_b64_tr_b16 v[184:185], v217 offset:49152\n"
    "ds_read_b64_tr_b16 v[186:187], v217 offset:50176\n"
    "v_mfma_f32_32x32x16_bf16 a[32:47], v[192:195], a[212:215], a[32:47]\n"
    "ds_read_b64_tr_b16 v[188:189], v220 offset:49152\n"
    "ds_read_b64_tr_b16 v[190:191], v220 offset:50176\n"
    "s_waitcnt vmcnt(6)\n"
    "v_sub_u32 v229, v229, v230\n"
    "s_nop 0\n"
    "v_readfirstlane_b32 s86, v229\n"
    "s_nop 1\n"
    "s_sub_u32 s86, s86, 2\n"
    "s_cmp_le_u32 s86, 0\n"
    "s_cbranch_scc0 Lslow1_%=\n"
    "Lback1_%=:\n"
    "s_add_u32 s85, s68, 12288\n"
    "buffer_load_dwordx4 a[192:195], v250, s[52:55], s85 offen sc1\n"
    "v_mfma_f32_32x32x16_bf16 a[48:63], v[196:199], a[212:215], a[48:63]\n"
    "ds_read_b64_tr_b16 v[192:193], v217 offset:55296\n"
    "ds_read_b64_tr_b16 v[194:195], v217 offset:56320\n"
    "s_add_u32 s85, s68, 13312\n"
    "buffer_load_dwordx4 a[196:199], v250, s[52:55], s85 offen sc1\n"
    "s_waitcnt lgkmcnt(6)\n"
    "v_mfma_f32_32x32x16_bf16 a[0:15], v[168:171], v[144:147], a[0:15]\n"
    "s_barrier\n"
    "v_mfma_f32_32x32x16_bf16 a[16:31], v[172:175], v[144:147], a[16:31]\n"
    "ds_read_b64_tr_b16 v[196:197], v220 offset:55296\n"
    "ds_read_b64_tr_b16 v[198:199], v220 offset:56320\n"
    "ds_read_b64_tr_b16 v[168:169], v216 offset:49152\n"
    "ds_read_b64_tr_b16 v[170:171], v216 offset:50176\n"
    "s_add_u32 m0, s65, 73728\n"
    "s_add_u32 s84, s59, 4480\n"
    "s_add_u32 s85, s68, 14336\n"
    "buffer_load_dwordx4 a[200:203], v250, s[52:55], s85 offen sc1\n"
    "v_mfma_f32_32x32x16_bf16 a[0:15], v[176:179], v[148:151], a[0:15]\n"
    "ds_read_b64_tr_b16 v[172:173], v219 offset:49152\n"
    "ds_read_b64_tr_b16 v[174:175], v219 offset:50176\n"
    "buffer_load_dwordx4 v243, s[48:51], s84 offen lds\n"
    "v_mfma_f32_32x32x16_bf16 a[16:31], v[180:183], v[148:151], a[16:31]\n"
    "ds_read_b64_tr_b16 v[176:177], v216 offset:55296\n"
    "ds_read_b64_tr_b16 v[178:179], v216 offset:56320\n"
    "buffer_load_dwordx4 v226, s[48:51], s84 offen offset:1024 lds\n"
    "s_add_u32 s84, s59, 76160\n"
    "s_waitcnt lgkmcnt(6)\n"
    "v_mfma_f32_32x32x16_bf16 a[0:15], v[184:187], v[152:155], a[0:15]\n"
    "ds_read_b64_tr_b16 v[180:181], v219 offset:55296\n"
    "ds_read_b64_tr_b16 v[182:183], v219 offset:56320\n"
    "buffer_load_dwordx4 v243, s[48:51], s84 offen offset:2048 lds\n"
    "v_mfma_f32_32x32x16_bf16 a[16:31], v[188:191], v[152:155], a[16:31]\n"
    "ds_read_b64_tr_b16 v[184:185], v216 offset:61440\n"
    "ds_read_b64_tr_b16 v[186:187], v216 offset:62464\n"
    "buffer_load_dwordx4 v226, s[48:51], s84 offen offset:3072 lds\n"
    "s_add_u32 m0, s65, 77824\n"
    "s_add_u32 s84, s59, 151936\n"
    "v_mfma_f32_32x32x16_bf16 a[0:15], v[192:195], v[156:159], a[0:15]\n"
    "ds_read_b64_tr_b16 v[188:189], v219 offset:61440\n"
    "ds_read_b64_tr_b16 v[190:191], v219 offset:62464\n"
    "buffer_load_dwordx4 v243, s[48:51], s84 offen lds\n"
    "v_mfma_f32_32x32x16_bf16 a[16:31], v[196:199], v[156:159], a[16:31]\n"
    "ds_read_b64_tr_b16 v[192:193], v217 offset:2048\n"
    "ds_read_b64_tr_b16 v[194:195], v217 offset:3072\n"
    "buffer_load_dwordx4 v226, s[48:51], s84 offen offset:1024 lds\n"
    "s_waitcnt lgkmcnt(6)\n"
    "v_mfma_f32_32x32x16_bf16 a[32:47], v[168:171], v[144:147], a[32:47]\n"
    "ds_read_b64_tr_b16 v[196:197], v220 offset:2048\n"
    "ds_read_b64_tr_b16 v[198:199], v220 offset:3072\n"
    "s_add_u32 s85, s68, 15360\n"
    "buffer_load_dwordx4 a[204:207], v250, s[52:55], s85 offen sc1\n"
    "v_mfma_f32_32x32x16_bf16 a[48:63], v[172:175], v[144:147], a[48:63]\n"
    "ds_read_b64_tr_b16 v[168:169], v216 offset:51200\n"
    "ds_read_b64_tr_b16 v[170:171], v216 offset:52224\n"
    "v_mfma_f32_32x32x16_bf16 a[32:47], v[176:179], v[148:151], a[32:47]\n"
    "ds_read_b64_tr_b16 v[172:173], v219 offset:51200\n"
    "ds_read_b64_tr_b16 v[174:175], v219 offset:52224\n"
    "v_mfma_f32_32x32x16_bf16 a[48:63], v[180:183], v[148:151], a[48:63]\n"
    "ds_read_b64_tr_b16 v[176:177], v216 offset:57344\n"
    "ds_read_b64_tr_b16 v[178:179], v216 offset:58368\n"
    "s_waitcnt lgkmcnt(6)\n"
    "v_mfma_f32_32x32x16_bf16 a[32:47], v[184:187], v[152:155], a[32:47]\n"
    "ds_read_b64_tr_b16 v[180:181], v219 offset:57344\n"
    "ds_read_b64_tr_b16 v[182:183], v219 offset:58368\n"
    "v_mfma_f32_32x32x16_bf16 a[48:63], v[188:191], v[152:155], a[48:63]\n"
    "ds_read_b64_tr_b16 v[184:185], v216 offset:63488\n"
    "ds_read_b64_tr_b16 v[186:187], v216 offset:64512\n"
    "v_mfma_f32_32x32x16_bf16 a[32:47], v[192:195], v[156:159], a[32:47]\n"
    "ds_read_b64_tr_b16 v[188:189], v219 offset:63488\n"
    "ds_read_b64_tr_b16 v[190:191], v219 offset:64512\n"
    "v_mfma_f32_32x32x16_bf16 a[48:63], v[196:199], v[156:159], a[48:63]\n"
    "ds_read_b64_tr_b16 v[192:193], v217 offset:4096\n"
    "ds_read_b64_tr_b16 v[194:195], v217 offset:5120\n"
    "s_waitcnt lgkmcnt(6)\n"
    "v_mfma_f32_32x32x16_bf16 a[64:79], v[168:171], v[96:99], 0\n"
    "ds_read_b64_tr_b16 v[196:197], v220 offset:4096\n"
    "ds_read_b64_tr_b16 v[198:199], v220 offset:5120\n"
    "v_mfma_f32_32x32x16_bf16 a[80:95], v[172:175], v[96:99], 0\n"
    "ds_read_b64_tr_b16 v[168:169], v216 offset:53248\n"
    "ds_read_b64_tr_b16 v[170:171], v216 offset:54272\n"
    "v_mfma_f32_32x32x16_bf16 a[64:79], v[176:179], v[100:103], a[64:79]\n"
    "ds_read_b64_tr_b16 v[172:173], v219 offset:53248\n"
    "ds_read_b64_tr_b16 v[174:175], v219 offset:54272\n"
    "v_mfma_f32_32x32x16_bf16 a[80:95], v[180:183], v[100:103], a[80:95]\n"
    "ds_read_b64_tr_b16 v[176:177], v216 offset:59392\n"
    "ds_read_b64_tr_b16 v[178:179], v216 offset:60416\n"
    "s_waitcnt lgkmcnt(6)\n"
    "v_mfma_f32_32x32x16_bf16 a[64:79], v[184:187], v[104:107], a[64:79]\n"
    "ds_read_b64_tr_b16 v[180:181], v219 offset:59392\n"
    "ds_read_b64_tr_b16 v[182:183], v219 offset:60416\n"
    "v_mfma_f32_32x32x16_bf16 a[80:95], v[188:191], v[104:107], a[80:95]\n"
    "ds_read_b64_tr_b16 v[184:185], v217 offset:0\n"
    "ds_read_b64_tr_b16 v[186:187], v217 offset:1024\n"
    "v_mfma_f32_32x32x16_bf16 a[64:79], v[192:195], v[108:111], a[64:79]\n"
    "ds_read_b64_tr_b16 v[188:189], v220 offset:0\n"
    "ds_read_b64_tr_b16 v[190:191], v220 offset:1024\n"
    "v_mfma_f32_32x32x16_bf16 a[80:95], v[196:199], v[108:111], a[80:95]\n"
    "ds_read_b64_tr_b16 v[192:193], v217 offset:6144\n"
    "ds_read_b64_tr_b16 v[194:195], v217 offset:7168\n"
    "s_waitcnt lgkmcnt(6)\n"
    "v_mfma_f32_32x32x16_bf16 a[64:79], v[168:171], v[112:115], a[64:79]\n"
    "s_barrier\n"
    "v_mfma_f32_32x32x16_bf16 a[80:95], v[172:175], v[112:115], a[80:95]\n"
    "ds_read_b64_tr_b16 v[196:197], v220 offset:6144\n"
    "ds_read_b64_tr_b16 v[198:199], v220 offset:7168\n"
    "ds_read_b64_tr_b16 v[168:169], v216 offset:0\n"
    "ds_read_b64_tr_b16 v[170:171], v216 offset:1024\n"
    "s_add_u32 m0, s65, 98304\n"
    "s_add_u32 s84, s60, 4480\n"
    "v_mfma_f32_32x32x16_bf16 a[64:79], v[176:179], v[116:119], a[64:79]\n"
    "ds_read_b64_tr_b16 v[172:173], v219 offset:0\n"
    "ds_read_b64_tr_b16 v[174:175], v219 offset:1024\n"
    "buffer_load_dwordx4 v243, s[48:51], s84 offen lds\n"
    "v_mfma_f32_32x32x16_bf16 a[80:95], v[180:183], v[116:119], a[80:95]\n"
    "ds_read_b64_tr_b16 v[176:177], v216 offset:6144\n"
    "ds_read_b64_tr_b16 v[178:179], v216 offset:7168\n"
    "buffer_load_dwordx4 v226, s[48:51], s84 offen offset:1024 lds\n"
    "s_add_u32 s84, s60, 76160\n"
    "s_waitcnt lgkmcnt(6)\n"
    "v_mfma_f32_32x32x16_bf16 a[64:79], v[184:187], v[120:123], a[64:79]\n"
    "ds_read_b64_tr_b16 v[180:181], v219 offset:6144\n"
    "ds_read_b64_tr_b16 v[182:183], v219 offset:7168\n"
    "buffer_load_dwordx4 v243, s[48:51], s84 offen offset:2048 lds\n"
    "v_mfma_f32_32x32x16_bf16 a[80:95], v[188:191], v[120:123], a[80:95]\n"
    "ds_read_b64_tr_b16 v[184:185], v216 offset:12288\n"
    "ds_read_b64_tr_b16 v[186:187], v216 offset:13312\n"
    "buffer_load_dwordx4 v226, s[48:51], s84 offen offset:3072 lds\n"
    "s_add_u32 m0, s65, 102400\n"
    "s_add_u32 s84, s60, 151936\n"
    "v_mfma_f32_32x32x16_bf16 a[64:79], v[192:195], v[124:127], a[64:79]\n"
    "ds_read_b64_tr_b16 v[188:189], v219 offset:12288\n"
    "ds_read_b64_tr_b16 v[190:191], v219 offset:13312\n"
    "buffer_load_dwordx4 v243, s[48:51], s84 offen lds\n"
    "v_mfma_f32_32x32x16_bf16 a[80:95], v[196:199], v[124:127], a[80:95]\n"
    "ds_read_b64_tr_b16 v[192:193], v216 offset:18432\n"
    "ds_read_b64_tr_b16 v[194:195], v216 offset:19456\n"
    "buffer_load_dwordx4 v226, s[48:51], s84 offen offset:1024 lds\n"
    "s_waitcnt vmcnt(15)\n"
    "s_waitcnt lgkmcnt(6)\n"
    "v_mfma_f32_32x32x16_bf16 a[0:15], v[168:171], a[192:195], a[0:15]\n"
    "ds_read_b64_tr_b16 v[196:197], v219 offset:18432\n"
    "ds_read_b64_tr_b16 v[198:199], v219 offset:19456\n"
    "v_mfma_f32_32x32x16_bf16 a[16:31], v[172:175], a[192:195], a[16:31]\n"
    "ds_read_b64_tr_b16 v[168:169], v216 offset:2048\n"
    "ds_read_b64_tr_b16 v[170:171], v216 offset:3072\n"
    "s_waitcnt vmcnt(14)\n"
    "v_mfma_f32_32x32x16_bf16 a[0:15], v[176:179], a[196:199], a[0:15]\n"
    "ds_read_b64_tr_b16 v[172:173], v219 offset:2048\n"
    "ds_read_b64_tr_b16 v[174:175], v219 offset:3072\n"
    "v_mfma_f32_32x32x16_bf16 a[16:31], v[180:183], a[196:199], a[16:31]\n"
    "ds_read_b64_tr_b16 v[176:177], v216 offset:8192\n"
    "ds_read_b64_tr_b16 v[178:179], v216 offset:9216\n"
    "s_waitcnt vmcnt(13)\n"
    "s_waitcnt lgkmcnt(6)\n"
    "v_mfma_f32_32x32x16_bf16 a[0:15], v[184:187], a[200:203], a[0:15]\n"
    "ds_read_b64_tr_b16 v[180:181], v219 offset:8192\n"
    "ds_read_b64_tr_b16 v[182:183], v219 offset:9216\n"
    "v_mfma_f32_32x32x16_bf16 a[16:31], v[188:191], a[200:203], a[16:31]\n"
    "ds_read_b64_tr_b16 v[184:185], v216 offset:14336\n"
    "ds_read_b64_tr_b16 v[186:187], v216 offset:15360\n"
    "s_waitcnt vmcnt(6)\n"
    "v_mfma_f32_32x32x16_bf16 a[0:15], v[192:195], a[204:207], a[0:15]\n"
    "ds_read_b64_tr_b16 v[188:189], v219 offset:14336\n"
    "ds_read_b64_tr_b16 v[190:191], v219 offset:15360\n"
    "v_mfma_f32_32x32x16_bf16 a[16:31], v[196:199], a[204:207], a[16:31]\n"
    "ds_read_b64_tr_b16 v[192:193], v216 offset:20480\n"
    "ds_read_b64_tr_b16 v[194:195], v216 offset:21504\n"
    "s_waitcnt lgkmcnt(6)\n"
    "v_mfma_f32_32x32x16_bf16 a[32:47], v[168:171], a[192:195], a[32:47]\n"
    "ds_read_b64_tr_b16 v[196:197], v219 offset:20480\n"
    "ds_read_b64_tr_b16 v[198:199], v219 offset:21504\n"
    "v_mfma_f32_32x32x16_bf16 a[48:63], v[172:175], a[192:195], a[48:63]\n"
    "ds_read_b64_tr_b16 v[168:169], v216 offset:4096\n"
    "ds_read_b64_tr_b16 v[170:171], v216 offset:5120\n"
    "v_mfma_f32_32x32x16_bf16 a[32:47], v[176:179], a[196:199], a[32:47]\n"
    "ds_read_b64_tr_b16 v[172:173], v219 offset:4096\n"
    "ds_read_b64_tr_b16 v[174:175], v219 offset:5120\n"
    "v_accvgpr_read_b32 v0, a0\n"
    "v_accvgpr_read_b32 v1, a1\n"
    "v_accvgpr_read_b32 v2, a2\n"
    "v_accvgpr_read_b32 v3, a3\n"
    "v_mfma_f32_32x32x16_bf16 a[48:63], v[180:183], a[196:199], a[48:63]\n"
    "ds_read_b64_tr_b16 v[176:177], v216 offset:10240\n"
    "ds_read_b64_tr_b16 v[178:179], v216 offset:11264\n"
    "v_accvgpr_read_b32 v4, a4\n"
    "v_accvgpr_read_b32 v5, a5\n"
    "v_accvgpr_read_b32 v6, a6\n"
    "s_waitcnt lgkmcnt(6)\n"
    "v_mfma_f32_32x32x16_bf16 a[32:47], v[184:187], a[200:203], a[32:47]\n"
    "ds_read_b64_tr_b16 v[180:181], v219 offset:10240\n"
    "ds_read_b64_tr_b16 v[182:183], v219 offset:11264\n"
    "v_accvgpr_read_b32 v7, a7\n"
    "v_cvt_pk_bf16_f32 v208, v0, v1\n"
    "v_cvt_pk_bf16_f32 v209, v2, v3\n"
    "v_cvt_pk_bf16_f32 v210, v4, v5\n"
    "v_mfma_f32_32x32x16_bf16 a[48:63], v[188:191], a[200:203], a[48:63]\n"
    "ds_read_b64_tr_b16 v[184:185], v216 offset:16384\n"
    "ds_read_b64_tr_b16 v[186:187], v216 offset:17408\n"
    "v_cvt_pk_bf16_f32 v211, v6, v7\n"
    "v_mfma_f32_32x32x16_bf16 a[32:47], v[192:195], a[204:207], a[32:47]\n"
    "ds_read_b64_tr_b16 v[188:189], v219 offset:16384\n"
    "ds_read_b64_tr_b16 v[190:191], v219 offset:17408\n"
    "s_add_u32 s85, s72, 0\n"
    "buffer_store_dwordx4 v[208:211], v250, s[52:55], s85 offen sc1\n"
    "v_mfma_f32_32x32x16_bf16 a[48:63], v[196:199], a[204:207], a[48:63]\n"
    "ds_read_b64_tr_b16 v[192:193], v216 offset:22528\n"
    "ds_read_b64_tr_b16 v[194:195], v216 offset:23552\n"
    "v_accvgpr_read_b32 v8, a8\n"
    "v_accvgpr_read_b32 v9, a9\n"
    "v_accvgpr_read_b32 v10, a10\n"
    "s_waitcnt lgkmcnt(6)\n"
    "v_mfma_f32_32x32x16_bf16 a[64:79], v[168:171], v[128:131], a[64:79]\n"
    "s_barrier\n"
    "v_mfma_f32_32x32x16_bf16 a[80:95], v[172:175], v[128:131], a[80:95]\n"
    "ds_read_b64_tr_b16 v[196:197], v219 offset:22528\n"
    "ds_read_b64_tr_b16 v[198:199], v219 offset:23552\n"
    "ds_read_b64_tr_b16 v[168:169], v218 offset:8192\n"
    "ds_read_b64_tr_b16 v[170:171], v218 offset:9216\n"
    "s_add_u32 m0, s65, 49152\n"
    "s_add_u32 s84, s61, 4480\n"
    "v_mfma_f32_32x32x16_bf16 a[64:79], v[176:179], v[132:135], a[64:79]\n"
    "ds_read_b64_tr_b16 v[172:173], v221 offset:8192\n"
    "ds_read_b64_tr_b16 v[174:175], v221 offset:9216\n"
    "buffer_load_dwordx4 v243, s[48:51], s84 offen lds\n"
    "v_accvgpr_read_b32 v11, a11\n"
    "v_mfma_f32_32x32x16_bf16 a[80:95], v[180:183], v[132:135], a[80:95]\n"
    "ds_read_b64_tr_b16 v[176:177], v218 offset:14336\n"
    "ds_read_b64_tr_b16 v[178:179], v218 offset:15360\n"
    "buffer_load_dwordx4 v226, s[48:51], s84 offen offset:1024 lds\n"
    "s_add_u32 s84, s59, 223616\n"
    "s_waitcnt lgkmcnt(6)\n"
    "v_mfma_f32_32x32x16_bf16 a[64:79], v[184:187], v[136:139], a[64:79]\n"
    "ds_read_b64_tr_b16 v[180:181], v221 offset:14336\n"
    "ds_read_b64_tr_b16 v[182:183], v221 offset:15360\n"
    "buffer_load_dwordx4 v243, s[48:51], s84 offen offset:2048 lds\n"
    "v_accvgpr_read_b32 v12, a12\n"
    "v_mfma_f32_32x32x16_bf16 a[80:95], v[188:191], v[136:139], a[80:95]\n"
    "ds_read_b64_tr_b16 v[184:185], v218 offset:20480\n"
    "ds_read_b64_tr_b16 v[186:187], v218 offset:21504\n"
    "buffer_load_dwordx4 v226, s[48:51], s84 offen offset:3072 lds\n"
    "s_add_u32 m0, s65, 53248\n"
    "s_add_u32 s84, s60, 225664\n"
    "v_mfma_f32_32x32x16_bf16 a[64:79], v[192:195], v[140:143], a[64:79]\n"
    "ds_read_b64_tr_b16 v[188:189], v221 offset:20480\n"
    "ds_read_b64_tr_b16 v[190:191], v221 offset:21504\n"
    "buffer_load_dwordx4 v243, s[48:51], s84 offen lds\n"
    "v_accvgpr_read_b32 v13, a13\n"
    "v_mfma_f32_32x32x16_bf16 a[80:95], v[196:199], v[140:143], a[80:95]\n"
    "ds_read_b64_tr_b16 v[192:193], v218 offset:26624\n"
    "ds_read_b64_tr_b16 v[194:195], v218 offset:27648\n"
    "buffer_load_dwordx4 v226, s[48:51], s84 offen offset:1024 lds\n"
    "s_waitcnt lgkmcnt(6)\n"
    "v_mfma_f32_32x32x16_bf16 a[64:79], v[168:171], a[144:147], a[64:79]\n"
    "ds_read_b64_tr_b16 v[196:197], v221 offset:26624\n"
    "ds_read_b64_tr_b16 v[198:199], v221 offset:27648\n"
    "v_accvgpr_read_b32 v14, a14\n"
    "v_accvgpr_read_b32 v15, a15\n"
    "v_cvt_pk_bf16_f32 v212, v8, v9\n"
    "v_cvt_pk_bf16_f32 v213, v10, v11\n"
    "v_mfma_f32_32x32x16_bf16 a[80:95], v[172:175], a[144:147], a[80:95]\n"
    "ds_read_b64_tr_b16 v[168:169], v218 offset:10240\n"
    "ds_read_b64_tr_b16 v[170:171], v218 offset:11264\n"
    "v_cvt_pk_bf16_f32 v214, v12, v13\n"
    "v_cvt_pk_bf16_f32 v215, v14, v15\n"
    "v_mfma_f32_32x32x16_bf16 a[64:79], v[176:179], a[148:151], a[64:79]\n"
    "ds_read_b64_tr_b16 v[172:173], v221 offset:10240\n"
    "ds_read_b64_tr_b16 v[174:175], v221 offset:11264\n"
    "s_add_u32 s85, s72, 8192\n"
    "buffer_store_dwordx4 v[212:215], v250, s[52:55], s85 offen sc1\n"
    "v_mfma_f32_32x32x16_bf16 a[80:95], v[180:183], a[148:151], a[80:95]\n"
    "ds_read_b64_tr_b16 v[176:177], v218 offset:16384\n"
    "ds_read_b64_tr_b16 v[178:179], v218 offset:17408\n"
    "v_accvgpr_read_b32 v16, a16\n"
    "v_accvgpr_read_b32 v17, a17\n"
    "v_accvgpr_read_b32 v18, a18\n"
    "s_waitcnt lgkmcnt(6)\n"
    "v_mfma_f32_32x32x16_bf16 a[64:79], v[184:187], a[152:155], a[64:79]\n"
    "ds_read_b64_tr_b16 v[180:181], v221 offset:16384\n"
    "ds_read_b64_tr_b16 v[182:183], v221 offset:17408\n"
    "v_accvgpr_read_b32 v19, a19\n"
    "v_accvgpr_read_b32 v20, a20\n"
    "v_accvgpr_read_b32 v21, a21\n"
    "v_accvgpr_read_b32 v22, a22\n"
    "v_mfma_f32_32x32x16_bf16 a[80:95], v[188:191], a[152:155], a[80:95]\n"
    "ds_read_b64_tr_b16 v[184:185], v218 offset:22528\n"
    "ds_read_b64_tr_b16 v[186:187], v218 offset:23552\n"
    "v_accvgpr_read_b32 v23, a23\n"
    "v_cvt_pk_bf16_f32 v208, v16, v17\n"
    "v_cvt_pk_bf16_f32 v209, v18, v19\n"
    "v_cvt_pk_bf16_f32 v210, v20, v21\n"
    "v_mfma_f32_32x32x16_bf16 a[64:79], v[192:195], a[156:159], a[64:79]\n"
    "ds_read_b64_tr_b16 v[188:189], v221 offset:22528\n"
    "ds_read_b64_tr_b16 v[190:191], v221 offset:23552\n"
    "v_cvt_pk_bf16_f32 v211, v22, v23\n"
    "v_mfma_f32_32x32x16_bf16 a[80:95], v[196:199], a[156:159], a[80:95]\n"
    "ds_read_b64_tr_b16 v[192:193], v218 offset:28672\n"
    "ds_read_b64_tr_b16 v[194:195], v218 offset:29696\n"
    "s_waitcnt lgkmcnt(6)\n"
    "v_mfma_f32_32x32x16_bf16 a[64:79], v[168:171], a[160:163], a[64:79]\n"
    "ds_read_b64_tr_b16 v[196:197], v221 offset:28672\n"
    "ds_read_b64_tr_b16 v[198:199], v221 offset:29696\n"
    "s_add_u32 s85, s72, 16384\n"
    "buffer_store_dwordx4 v[208:211], v250, s[52:55], s85 offen sc1\n"
    "v_mfma_f32_32x32x16_bf16 a[80:95], v[172:175], a[160:163], a[80:95]\n"
    "ds_read_b64_tr_b16 v[168:169], v218 offset:12288\n"
    "ds_read_b64_tr_b16 v[170:171], v218 offset:13312\n"
    "v_accvgpr_read_b32 v24, a24\n"
    "v_accvgpr_read_b32 v25, a25\n"
    "v_accvgpr_read_b32 v26, a26\n"
    "v_accvgpr_read_b32 v27, a27\n"
    "v_mfma_f32_32x32x16_bf16 a[64:79], v[176:179], a[164:167], a[64:79]\n"
    "ds_read_b64_tr_b16 v[172:173], v221 offset:12288\n"
    "ds_read_b64_tr_b16 v[174:175], v221 offset:13312\n"
    "v_accvgpr_read_b32 v28, a28\n"
    "v_accvgpr_read_b32 v29, a29\n"
    "v_accvgpr_read_b32 v30, a30\n"
    "v_accvgpr_read_b32 v31, a31\n"
    "v_mfma_f32_32x32x16_bf16 a[80:95], v[180:183], a[164:167], a[80:95]\n"
    "ds_read_b64_tr_b16 v[176:177], v218 offset:18432\n"
    "ds_read_b64_tr_b16 v[178:179], v218 offset:19456\n"
    "v_cvt_pk_bf16_f32 v212, v24, v25\n"
    "v_cvt_pk_bf16_f32 v213, v26, v27\n"
    "v_cvt_pk_bf16_f32 v214, v28, v29\n"
    "s_waitcnt lgkmcnt(6)\n"
    "v_mfma_f32_32x32x16_bf16 a[64:79], v[184:187], a[168:171], a[64:79]\n"
    "ds_read_b64_tr_b16 v[180:181], v221 offset:18432\n"
    "ds_read_b64_tr_b16 v[182:183], v221 offset:19456\n"
    "v_cvt_pk_bf16_f32 v215, v30, v31\n"
    "v_mfma_f32_32x32x16_bf16 a[80:95], v[188:191], a[168:171], a[80:95]\n"
    "ds_read_b64_tr_b16 v[184:185], v218 offset:24576\n"
    "ds_read_b64_tr_b16 v[186:187], v218 offset:25600\n"
    "s_add_u32 s85, s72, 24576\n"
    "buffer_store_dwordx4 v[212:215], v250, s[52:55], s85 offen sc1\n"
    "v_mfma_f32_32x32x16_bf16 a[64:79], v[192:195], a[172:175], a[64:79]\n"
    "ds_read_b64_tr_b16 v[188:189], v221 offset:24576\n"
    "ds_read_b64_tr_b16 v[190:191], v221 offset:25600\n"
    "v_accvgpr_read_b32 v0, a32\n"
    "v_accvgpr_read_b32 v1, a33\n"
    "v_accvgpr_read_b32 v2, a34\n"
    "v_accvgpr_read_b32 v3, a35\n"
    "v_mfma_f32_32x32x16_bf16 a[80:95], v[196:199], a[172:175], a[80:95]\n"
    "ds_read_b64_tr_b16 v[192:193], v218 offset:30720\n"
    "ds_read_b64_tr_b16 v[194:195], v218 offset:31744\n"
    "v_accvgpr_read_b32 v4, a36\n"
    "v_accvgpr_read_b32 v5, a37\n"
    "v_accvgpr_read_b32 v6, a38\n"
    "s_waitcnt lgkmcnt(6)\n"
    "v_mfma_f32_32x32x16_bf16 a[64:79], v[168:171], a[176:179], a[64:79]\n"
    "s_barrier\n"
    "v_mfma_f32_32x32x16_bf16 a[80:95], v[172:175], a[176:179], a[80:95]\n"
    "ds_read_b64_tr_b16 v[196:197], v221 offset:30720\n"
    "ds_read_b64_tr_b16 v[198:199], v221 offset:31744\n"
    "ds_read_b64_tr_b16 v[168:169], v216 offset:24576\n"
    "ds_read_b64_tr_b16 v[170:171], v216 offset:25600\n"
    "s_add_u32 m0, s65, 0\n"
    "s_add_u32 s84, s62, 4096\n"
    "v_mfma_f32_32x32x16_bf16 a[64:79], v[176:179], a[180:183], a[64:79]\n"
    "ds_read_b64_tr_b16 v[172:173], v219 offset:24576\n"
    "ds_read_b64_tr_b16 v[174:175], v219 offset:25600\n"
    "buffer_load_dwordx4 v245, s[48:51], s84 offen lds\n"
    "v_accvgpr_read_b32 v7, a39\n"
    "v_mfma_f32_32x32x16_bf16 a[80:95], v[180:183], a[180:183], a[80:95]\n"
    "ds_read_b64_tr_b16 v[176:177], v216 offset:30720\n"
    "ds_read_b64_tr_b16 v[178:179], v216 offset:31744\n"
    "buffer_load_dwordx4 v228, s[48:51], s84 offen offset:1024 lds\n"
    "s_add_u32 s84, s63, 2048\n"
    "s_waitcnt lgkmcnt(6)\n"
    "v_mfma_f32_32x32x16_bf16 a[64:79], v[184:187], a[184:187], a[64:79]\n"
    "ds_read_b64_tr_b16 v[180:181], v219 offset:30720\n"
    "ds_read_b64_tr_b16 v[182:183], v219 offset:31744\n"
    "buffer_load_dwordx4 v245, s[48:51], s84 offen offset:2048 lds\n"
    "v_cvt_pk_bf16_f32 v208, v0, v1\n"
    "v_mfma_f32_32x32x16_bf16 a[80:95], v[188:191], a[184:187], a[80:95]\n"
    "ds_read_b64_tr_b16 v[184:185], v216 offset:36864\n"
    "ds_read_b64_tr_b16 v[186:187], v216 offset:37888\n"
    "buffer_load_dwordx4 v228, s[48:51], s84 offen offset:3072 lds\n"
    "s_add_u32 m0, s65, 4096\n"
    "s_add_u32 s84, s62, 151552\n"
    "v_mfma_f32_32x32x16_bf16 a[64:79], v[192:195], a[188:191], a[64:79]\n"
    "ds_read_b64_tr_b16 v[188:189], v219 offset:36864\n"
    "ds_read_b64_tr_b16 v[190:191], v219 offset:37888\n"
    "buffer_load_dwordx4 v245, s[48:51], s84 offen lds\n"
    "v_cvt_pk_bf16_f32 v209, v2, v3\n"
    "v_mfma_f32_32x32x16_bf16 a[80:95], v[196:199], a[188:191], a[80:95]\n"
    "ds_read_b64_tr_b16 v[192:193], v216 offset:43008\n"
    "ds_read_b64_tr_b16 v[194:195], v216 offset:44032\n"
    "buffer_load_dwordx4 v228, s[48:51], s84 offen offset:1024 lds\n"
    "s_waitcnt lgkmcnt(6)\n"
    "v_mfma_f32_32x32x16_bf16 a[64:79], v[168:171], v[160:163], a[64:79]\n"
    "ds_read_b64_tr_b16 v[196:197], v219 offset:43008\n"
    "ds_read_b64_tr_b16 v[198:199], v219 offset:44032\n"
    "v_cvt_pk_bf16_f32 v210, v4, v5\n"
    "v_cvt_pk_bf16_f32 v211, v6, v7\n"
    "v_mfma_f32_32x32x16_bf16 a[80:95], v[172:175], v[160:163], a[80:95]\n"
    "ds_read_b64_tr_b16 v[168:169], v216 offset:26624\n"
    "ds_read_b64_tr_b16 v[170:171], v216 offset:27648\n"
    "s_add_u32 s85, s72, 32768\n"
    "buffer_store_dwordx4 v[208:211], v250, s[52:55], s85 offen sc1\n"
    "v_mfma_f32_32x32x16_bf16 a[64:79], v[176:179], v[164:167], a[64:79]\n"
    "ds_read_b64_tr_b16 v[172:173], v219 offset:26624\n"
    "ds_read_b64_tr_b16 v[174:175], v219 offset:27648\n"
    "v_accvgpr_read_b32 v8, a40\n"
    "v_accvgpr_read_b32 v9, a41\n"
    "v_accvgpr_read_b32 v10, a42\n"
    "v_accvgpr_read_b32 v11, a43\n"
    "v_mfma_f32_32x32x16_bf16 a[80:95], v[180:183], v[164:167], a[80:95]\n"
    "ds_read_b64_tr_b16 v[176:177], v216 offset:32768\n"
    "ds_read_b64_tr_b16 v[178:179], v216 offset:33792\n"
    "v_accvgpr_read_b32 v12, a44\n"
    "v_accvgpr_read_b32 v13, a45\n"
    "v_accvgpr_read_b32 v14, a46\n"
    "s_waitcnt lgkmcnt(6)\n"
    "v_mfma_f32_32x32x16_bf16 a[64:79], v[184:187], a[208:211], a[64:79]\n"
    "ds_read_b64_tr_b16 v[180:181], v219 offset:32768\n"
    "ds_read_b64_tr_b16 v[182:183], v219 offset:33792\n"
    "v_accvgpr_read_b32 v15, a47\n"
    "v_cvt_pk_bf16_f32 v212, v8, v9\n"
    "v_cvt_pk_bf16_f32 v213, v10, v11\n"
    "v_cvt_pk_bf16_f32 v214, v12, v13\n"
    "v_mfma_f32_32x32x16_bf16 a[80:95], v[188:191], a[208:211], a[80:95]\n"
    "ds_read_b64_tr_b16 v[184:185], v216 offset:38912\n"
    "ds_read_b64_tr_b16 v[186:187], v216 offset:39936\n"
    "v_cvt_pk_bf16_f32 v215, v14, v15\n"
    "v_mfma_f32_32x32x16_bf16 a[64:79], v[192:195], a[212:215], a[64:79]\n"
    "ds_read_b64_tr_b16 v[188:189], v219 offset:38912\n"
    "ds_read_b64_tr_b16 v[190:191], v219 offset:39936\n"
    "s_add_u32 s85, s72, 40960\n"
    "buffer_store_dwordx4 v[212:215], v250, s[52:55], s85 offen sc1\n"
    "v_mfma_f32_32x32x16_bf16 a[80:95], v[196:199], a[212:215], a[80:95]\n"
    "ds_read_b64_tr_b16 v[192:193], v216 offset:45056\n"
    "ds_read_b64_tr_b16 v[194:195], v216 offset:46080\n"
    "v_accvgpr_read_b32 v16, a48\n"
    "v_accvgpr_read_b32 v17, a49\n"
    "v_accvgpr_read_b32 v18, a50\n"
    "s_waitcnt lgkmcnt(6)\n"
    "v_mfma_f32_32x32x16_bf16 a[64:79], v[168:171], v[144:147], a[64:79]\n"
    "ds_read_b64_tr_b16 v[196:197], v219 offset:45056\n"
    "ds_read_b64_tr_b16 v[198:199], v219 offset:46080\n"
    "v_accvgpr_read_b32 v19, a51\n"
    "v_accvgpr_read_b32 v20, a52\n"
    "v_accvgpr_read_b32 v21, a53\n"
    "v_accvgpr_read_b32 v22, a54\n"
    "v_mfma_f32_32x32x16_bf16 a[80:95], v[172:175], v[144:147], a[80:95]\n"
    "ds_read_b64_tr_b16 v[168:169], v216 offset:28672\n"
    "ds_read_b64_tr_b16 v[170:171], v216 offset:29696\n"
    "v_accvgpr_read_b32 v23, a55\n"
    "v_cvt_pk_bf16_f32 v208, v16, v17\n"
    "v_cvt_pk_bf16_f32 v209, v18, v19\n"
    "v_cvt_pk_bf16_f32 v210, v20, v21\n"
    "v_mfma_f32_32x32x16_bf16 a[64:79], v[176:179], v[148:151], a[64:79]\n"
    "ds_read_b64_tr_b16 v[172:173], v219 offset:28672\n"
    "ds_read_b64_tr_b16 v[174:175], v219 offset:29696\n"
    "v_cvt_pk_bf16_f32 v211, v22, v23\n"
    "v_mfma_f32_32x32x16_bf16 a[80:95], v[180:183], v[148:151], a[80:95]\n"
    "ds_read_b64_tr_b16 v[176:177], v216 offset:34816\n"
    "ds_read_b64_tr_b16 v[178:179], v216 offset:35840\n"
    "s_waitcnt lgkmcnt(6)\n"
    "v_mfma_f32_32x32x16_bf16 a[64:79], v[184:187], v[152:155], a[64:79]\n"
    "ds_read_b64_tr_b16 v[180:181], v219 offset:34816\n"
    "ds_read_b64_tr_b16 v[182:183], v219 offset:35840\n"
    "s_add_u32 s85, s72, 49152\n"
    "buffer_store_dwordx4 v[208:211], v250, s[52:55], s85 offen sc1\n"
    "v_mfma_f32_32x32x16_bf16 a[80:95], v[188:191], v[152:155], a[80:95]\n"
    "ds_read_b64_tr_b16 v[184:185], v216 offset:40960\n"
    "ds_read_b64_tr_b16 v[186:187], v216 offset:41984\n"
    "v_accvgpr_read_b32 v24, a56\n"
    "v_accvgpr_read_b32 v25, a57\n"
    "v_accvgpr_read_b32 v26, a58\n"
    "v_accvgpr_read_b32 v27, a59\n"
    "v_mfma_f32_32x32x16_bf16 a[64:79], v[192:195], v[156:159], a[64:79]\n"
    "ds_read_b64_tr_b16 v[188:189], v219 offset:40960\n"
    "ds_read_b64_tr_b16 v[190:191], v219 offset:41984\n"
    "v_accvgpr_read_b32 v28, a60\n"
    "v_accvgpr_read_b32 v29, a61\n"
    "v_accvgpr_read_b32 v30, a62\n"
    "v_accvgpr_read_b32 v31, a63\n"
    "v_mfma_f32_32x32x16_bf16 a[80:95], v[196:199], v[156:159], a[80:95]\n"
    "ds_read_b64_tr_b16 v[192:193], v216 offset:47104\n"
    "ds_read_b64_tr_b16 v[194:195], v216 offset:48128\n"
    "v_cvt_pk_bf16_f32 v212, v24, v25\n"
    "v_cvt_pk_bf16_f32 v213, v26, v27\n"
    "v_cvt_pk_bf16_f32 v214, v28, v29\n"
    "s_waitcnt lgkmcnt(6)\n"
    "v_mfma_f32_32x32x16_bf16 a[64:79], v[168:171], a[192:195], a[64:79]\n"
    "s_barrier\n"
    "v_mfma_f32_32x32x16_bf16 a[80:95], v[172:175], a[192:195], a[80:95]\n"
    "ds_read_b64_tr_b16 v[196:197], v219 offset:47104\n"
    "ds_read_b64_tr_b16 v[198:199], v219 offset:48128\n"
    "ds_read_b64_tr_b16 v[168:169], v217 offset:8192\n"
    "ds_read_b64_tr_b16 v[170:171], v217 offset:9216\n"
    "s_add_u32 m0, s65, 139264\n"
    "s_add_u32 s84, s63, 151552\n"
    "v_mfma_f32_32x32x16_bf16 a[64:79], v[176:179], a[196:199], a[64:79]\n"
    "ds_read_b64_tr_b16 v[172:173], v220 offset:8192\n"
    "ds_read_b64_tr_b16 v[174:175], v220 offset:9216\n"
    "buffer_load_dwordx4 v245, s[48:51], s84 offen lds\n"
    "v_cvt_pk_bf16_f32 v215, v30, v31\n"
    "v_mfma_f32_32x32x16_bf16 a[80:95], v[180:183], a[196:199], a[80:95]\n"
    "ds_read_b64_tr_b16 v[176:177], v217 offset:14336\n"
    "ds_read_b64_tr_b16 v[178:179], v217 offset:15360\n"
    "buffer_load_dwordx4 v228, s[48:51], s84 offen offset:1024 lds\n"
    "s_add_u32 s84, s64, 2048\n"
    "s_waitcnt lgkmcnt(6)\n"
    "v_mfma_f32_32x32x16_bf16 a[64:79], v[184:187], a[200:203], a[64:79]\n"
    "ds_read_b64_tr_b16 v[180:181], v220 offset:14336\n"
    "ds_read_b64_tr_b16 v[182:183], v220 offset:15360\n"
    "buffer_load_dwordx4 v246, s[48:51], s84 offen offset:2048 lds\n"
    "v_mfma_f32_32x32x16_bf16 a[80:95], v[188:191], a[200:203], a[80:95]\n"
    "ds_read_b64_tr_b16 v[184:185], v217 offset:20480\n"
    "ds_read_b64_tr_b16 v[186:187], v217 offset:21504\n"
    "s_add_u32 s85, s72, 57344\n"
    "buffer_store_dwordx4 v[212:215], v250, s[52:55], s85 offen sc1\n"
    "v_mfma_f32_32x32x16_bf16 a[64:79], v[192:195], a[204:207], a[64:79]\n"
    "ds_read_b64_tr_b16 v[188:189], v220 offset:20480\n"
    "ds_read_b64_tr_b16 v[190:191], v220 offset:21504\n"
    "v_mfma_f32_32x32x16_bf16 a[80:95], v[196:199], a[204:207], a[80:95]\n"
    "ds_read_b64_tr_b16 v[192:193], v217 offset:26624\n"
    "ds_read_b64_tr_b16 v[194:195], v217 offset:27648\n"
    "s_waitcnt lgkmcnt(6)\n"
    "v_mfma_f32_32x32x16_bf16 a[96:111], v[168:171], v[96:99], 0\n"
    "ds_read_b64_tr_b16 v[196:197], v220 offset:26624\n"
    "ds_read_b64_tr_b16 v[198:199], v220 offset:27648\n"
    "v_mfma_f32_32x32x16_bf16 a[112:127], v[172:175], v[96:99], 0\n"
    "ds_read_b64_tr_b16 v[168:169], v217 offset:10240\n"
    "ds_read_b64_tr_b16 v[170:171], v217 offset:11264\n"
    "v_mfma_f32_32x32x16_bf16 a[96:111], v[176:179], v[100:103], a[96:111]\n"
    "ds_read_b64_tr_b16 v[172:173], v220 offset:10240\n"
    "ds_read_b64_tr_b16 v[174:175], v220 offset:11264\n"
    "v_accvgpr_read_b32 v0, a64\n"
    "v_accvgpr_read_b32 v1, a65\n"
    "v_accvgpr_read_b32 v2, a66\n"
    "v_accvgpr_read_b32 v3, a67\n"
    "v_mfma_f32_32x32x16_bf16 a[112:127], v[180:183], v[100:103], a[112:127]\n"
    "ds_read_b64_tr_b16 v[176:177], v217 offset:16384\n"
    "ds_read_b64_tr_b16 v[178:179], v217 offset:17408\n"
    "v_accvgpr_read_b32 v4, a68\n"
    "v_accvgpr_read_b32 v5, a69\n"
    "v_accvgpr_read_b32 v6, a70\n"
    "s_waitcnt lgkmcnt(6)\n"
    "v_mfma_f32_32x32x16_bf16 a[96:111], v[184:187], v[104:107], a[96:111]\n"
    "ds_read_b64_tr_b16 v[180:181], v220 offset:16384\n"
    "ds_read_b64_tr_b16 v[182:183], v220 offset:17408\n"
    "v_accvgpr_read_b32 v7, a71\n"
    "v_cvt_pk_bf16_f32 v208, v0, v1\n"
    "v_cvt_pk_bf16_f32 v209, v2, v3\n"
    "v_cvt_pk_bf16_f32 v210, v4, v5\n"
    "v_mfma_f32_32x32x16_bf16 a[112:127], v[188:191], v[104:107], a[112:127]\n"
    "ds_read_b64_tr_b16 v[184:185], v217 offset:22528\n"
    "ds_read_b64_tr_b16 v[186:187], v217 offset:23552\n"
    "v_cvt_pk_bf16_f32 v211, v6, v7\n"
    "v_mfma_f32_32x32x16_bf16 a[96:111], v[192:195], v[108:111], a[96:111]\n"
    "ds_read_b64_tr_b16 v[188:189], v220 offset:22528\n"
    "ds_read_b64_tr_b16 v[190:191], v220 offset:23552\n"
    "s_add_u32 s85, s72, 65536\n"
    "buffer_store_dwordx4 v[208:211], v250, s[52:55], s85 offen sc1\n"
    "v_mfma_f32_32x32x16_bf16 a[112:127], v[196:199], v[108:111], a[112:127]\n"
    "ds_read_b64_tr_b16 v[192:193], v217 offset:28672\n"
    "ds_read_b64_tr_b16 v[194:195], v217 offset:29696\n"
    "v_accvgpr_read_b32 v8, a72\n"
    "v_accvgpr_read_b32 v9, a73\n"
    "v_accvgpr_read_b32 v10, a74\n"
    "s_waitcnt lgkmcnt(6)\n"
    "v_mfma_f32_32x32x16_bf16 a[96:111], v[168:171], v[112:115], a[96:111]\n"
    "ds_read_b64_tr_b16 v[196:197], v220 offset:28672\n"
    "ds_read_b64_tr_b16 v[198:199], v220 offset:29696\n"
    "v_accvgpr_read_b32 v11, a75\n"
    "v_accvgpr_read_b32 v12, a76\n"
    "v_accvgpr_read_b32 v13, a77\n"
    "v_accvgpr_read_b32 v14, a78\n"
    "v_mfma_f32_32x32x16_bf16 a[112:127], v[172:175], v[112:115], a[112:127]\n"
    "ds_read_b64_tr_b16 v[168:169], v217 offset:12288\n"
    "ds_read_b64_tr_b16 v[170:171], v217 offset:13312\n"
    "v_accvgpr_read_b32 v15, a79\n"
    "v_cvt_pk_bf16_f32 v212, v8, v9\n"
    "v_cvt_pk_bf16_f32 v213, v10, v11\n"
    "v_cvt_pk_bf16_f32 v214, v12, v13\n"
    "v_mfma_f32_32x32x16_bf16 a[96:111], v[176:179], v[116:119], a[96:111]\n"
    "ds_read_b64_tr_b16 v[172:173], v220 offset:12288\n"
    "ds_read_b64_tr_b16 v[174:175], v220 offset:13312\n"
    "v_cvt_pk_bf16_f32 v215, v14, v15\n"
    "v_mfma_f32_32x32x16_bf16 a[112:127], v[180:183], v[116:119], a[112:127]\n"
    "ds_read_b64_tr_b16 v[176:177], v217 offset:18432\n"
    "ds_read_b64_tr_b16 v[178:179], v217 offset:19456\n"
    "s_waitcnt lgkmcnt(6)\n"
    "v_mfma_f32_32x32x16_bf16 a[96:111], v[184:187], v[120:123], a[96:111]\n"
    "ds_read_b64_tr_b16 v[180:181], v220 offset:18432\n"
    "ds_read_b64_tr_b16 v[182:183], v220 offset:19456\n"
    "s_add_u32 s85, s72, 73728\n"
    "buffer_store_dwordx4 v[212:215], v250, s[52:55], s85 offen sc1\n"
    "v_mfma_f32_32x32x16_bf16 a[112:127], v[188:191], v[120:123], a[112:127]\n"
    "ds_read_b64_tr_b16 v[184:185], v217 offset:24576\n"
    "ds_read_b64_tr_b16 v[186:187], v217 offset:25600\n"
    "v_accvgpr_read_b32 v16, a80\n"
    "v_accvgpr_read_b32 v17, a81\n"
    "v_accvgpr_read_b32 v18, a82\n"
    "v_accvgpr_read_b32 v19, a83\n"
    "v_mfma_f32_32x32x16_bf16 a[96:111], v[192:195], v[124:127], a[96:111]\n"
    "ds_read_b64_tr_b16 v[188:189], v220 offset:24576\n"
    "ds_read_b64_tr_b16 v[190:191], v220 offset:25600\n"
    "v_accvgpr_read_b32 v20, a84\n"
    "v_accvgpr_read_b32 v21, a85\n"
    "v_accvgpr_read_b32 v22, a86\n"
    "v_accvgpr_read_b32 v23, a87\n"
    "v_mfma_f32_32x32x16_bf16 a[112:127], v[196:199], v[124:127], a[112:127]\n"
    "ds_read_b64_tr_b16 v[192:193], v217 offset:30720\n"
    "ds_read_b64_tr_b16 v[194:195], v217 offset:31744\n"
    "v_cvt_pk_bf16_f32 v208, v16, v17\n"
    "v_cvt_pk_bf16_f32 v209, v18, v19\n"
    "v_cvt_pk_bf16_f32 v210, v20, v21\n"
    "s_waitcnt lgkmcnt(6)\n"
    "v_mfma_f32_32x32x16_bf16 a[96:111], v[168:171], v[128:131], a[96:111]\n"
    "s_waitcnt vmcnt(25)\n"
    "s_barrier\n"
    "v_mfma_f32_32x32x16_bf16 a[112:127], v[172:175], v[128:131], a[112:127]\n"
    "ds_read_b64_tr_b16 v[196:197], v220 offset:30720\n"
    "ds_read_b64_tr_b16 v[198:199], v220 offset:31744\n"
    "ds_read_b64_tr_b16 v[168:169], v217 offset:32768\n"
    "ds_read_b64_tr_b16 v[170:171], v217 offset:33792\n"
    "v_cvt_pk_bf16_f32 v211, v22, v23\n"
    "v_mfma_f32_32x32x16_bf16 a[96:111], v[176:179], v[132:135], a[96:111]\n"
    "ds_read_b64_tr_b16 v[172:173], v220 offset:32768\n"
    "ds_read_b64_tr_b16 v[174:175], v220 offset:33792\n"
    "s_add_u32 s85, s72, 81920\n"
    "buffer_store_dwordx4 v[208:211], v250, s[52:55], s85 offen sc1\n"
    "v_mfma_f32_32x32x16_bf16 a[112:127], v[180:183], v[132:135], a[112:127]\n"
    "ds_read_b64_tr_b16 v[176:177], v217 offset:38912\n"
    "ds_read_b64_tr_b16 v[178:179], v217 offset:39936\n"
    "v_accvgpr_read_b32 v24, a88\n"
    "v_accvgpr_read_b32 v25, a89\n"
    "v_accvgpr_read_b32 v26, a90\n"
    "s_waitcnt lgkmcnt(6)\n"
    "v_mfma_f32_32x32x16_bf16 a[96:111], v[184:187], v[136:139], a[96:111]\n"
    "ds_read_b64_tr_b16 v[180:181], v220 offset:38912\n"
    "ds_read_b64_tr_b16 v[182:183], v220 offset:39936\n"
    "v_accvgpr_read_b32 v27, a91\n"
    "v_accvgpr_read_b32 v28, a92\n"
    "v_accvgpr_read_b32 v29, a93\n"
    "v_accvgpr_read_b32 v30, a94\n"
    "v_mfma_f32_32x32x16_bf16 a[112:127], v[188:191], v[136:139], a[112:127]\n"
    "ds_read_b64_tr_b16 v[184:185], v217 offset:45056\n"
    "ds_read_b64_tr_b16 v[186:187], v217 offset:46080\n"
    "v_accvgpr_read_b32 v31, a95\n"
    "v_cvt_pk_bf16_f32 v212, v24, v25\n"
    "v_cvt_pk_bf16_f32 v213, v26, v27\n"
    "v_cvt_pk_bf16_f32 v214, v28, v29\n"
    "v_mfma_f32_32x32x16_bf16 a[96:111], v[192:195], v[140:143], a[96:111]\n"
    "ds_read_b64_tr_b16 v[188:189], v220 offset:45056\n"
    "ds_read_b64_tr_b16 v[190:191], v220 offset:46080\n"
    "v_cvt_pk_bf16_f32 v215, v30, v31\n"
    "v_mfma_f32_32x32x16_bf16 a[112:127], v[196:199], v[140:143], a[112:127]\n"
    "ds_read_b64_tr_b16 v[192:193], v217 offset:51200\n"
    "ds_read_b64_tr_b16 v[194:195], v217 offset:52224\n"
    "s_waitcnt lgkmcnt(6)\n"
    "v_mfma_f32_32x32x16_bf16 a[96:111], v[168:171], a[144:147], a[96:111]\n"
    "ds_read_b64_tr_b16 v[196:197], v220 offset:51200\n"
    "ds_read_b64_tr_b16 v[198:199], v220 offset:52224\n"
    "s_add_u32 s85, s72, 90112\n"
    "buffer_store_dwordx4 v[212:215], v250, s[52:55], s85 offen sc1\n"
    "v_mfma_f32_32x32x16_bf16 a[112:127], v[172:175], a[144:147], a[112:127]\n"
    "ds_read_b64_tr_b16 v[168:169], v217 offset:34816\n"
    "ds_read_b64_tr_b16 v[170:171], v217 offset:35840\n"
    "v_mfma_f32_32x32x16_bf16 a[96:111], v[176:179], a[148:151], a[96:111]\n"
    "ds_read_b64_tr_b16 v[172:173], v220 offset:34816\n"
    "ds_read_b64_tr_b16 v[174:175], v220 offset:35840\n"
    "v_mfma_f32_32x32x16_bf16 a[112:127], v[180:183], a[148:151], a[112:127]\n"
    "ds_read_b64_tr_b16 v[176:177], v217 offset:40960\n"
    "ds_read_b64_tr_b16 v[178:179], v217 offset:41984\n"
    "s_waitcnt lgkmcnt(6)\n"
    "v_mfma_f32_32x32x16_bf16 a[96:111], v[184:187], a[152:155], a[96:111]\n"
    "ds_read_b64_tr_b16 v[180:181], v220 offset:40960\n"
    "ds_read_b64_tr_b16 v[182:183], v220 offset:41984\n"
    "v_mfma_f32_32x32x16_bf16 a[112:127], v[188:191], a[152:155], a[112:127]\n"
    "ds_read_b64_tr_b16 v[184:185], v217 offset:47104\n"
    "ds_read_b64_tr_b16 v[186:187], v217 offset:48128\n"
    "v_mfma_f32_32x32x16_bf16 a[96:111], v[192:195], a[156:159], a[96:111]\n"
    "ds_read_b64_tr_b16 v[188:189], v220 offset:47104\n"
    "ds_read_b64_tr_b16 v[190:191], v220 offset:48128\n"
    "v_mfma_f32_32x32x16_bf16 a[112:127], v[196:199], a[156:159], a[112:127]\n"
    "ds_read_b64_tr_b16 v[192:193], v217 offset:53248\n"
    "ds_read_b64_tr_b16 v[194:195], v217 offset:54272\n"
    "s_waitcnt lgkmcnt(6)\n"
    "v_mfma_f32_32x32x16_bf16 a[96:111], v[168:171], a[160:163], a[96:111]\n"
    "ds_read_b64_tr_b16 v[196:197], v220 offset:53248\n"
    "ds_read_b64_tr_b16 v[198:199], v220 offset:54272\n"
    "v_mfma_f32_32x32x16_bf16 a[112:127], v[172:175], a[160:163], a[112:127]\n"
    "ds_read_b64_tr_b16 v[168:169], v217 offset:36864\n"
    "ds_read_b64_tr_b16 v[170:171], v217 offset:37888\n"
    "v_mfma_f32_32x32x16_bf16 a[96:111], v[176:179], a[164:167], a[96:111]\n"
    "ds_read_b64_tr_b16 v[172:173], v220 offset:36864\n"
    "ds_read_b64_tr_b16 v[174:175], v220 offset:37888\n"
    "v_mfma_f32_32x32x16_bf16 a[112:127], v[180:183], a[164:167], a[112:127]\n"
    "ds_read_b64_tr_b16 v[176:177], v217 offset:43008\n"
    "ds_read_b64_tr_b16 v[178:179], v217 offset:44032\n"
    "s_waitcnt lgkmcnt(6)\n"
    "v_mfma_f32_32x32x16_bf16 a[96:111], v[184:187], a[168:171], a[96:111]\n"
    "ds_read_b64_tr_b16 v[180:181], v220 offset:43008\n"
    "ds_read_b64_tr_b16 v[182:183], v220 offset:44032\n"
    "v_mfma_f32_32x32x16_bf16 a[112:127], v[188:191], a[168:171], a[112:127]\n"
    "ds_read_b64_tr_b16 v[184:185], v217 offset:49152\n"
    "ds_read_b64_tr_b16 v[186:187], v217 offset:50176\n"
    "v_mfma_f32_32x32x16_bf16 a[96:111], v[192:195], a[172:175], a[96:111]\n"
    "ds_read_b64_tr_b16 v[188:189], v220 offset:49152\n"
    "ds_read_b64_tr_b16 v[190:191], v220 offset:50176\n"
    "v_mfma_f32_32x32x16_bf16 a[112:127], v[196:199], a[172:175], a[112:127]\n"
    "ds_read_b64_tr_b16 v[192:193], v217 offset:55296\n"
    "ds_read_b64_tr_b16 v[194:195], v217 offset:56320\n"
    "s_waitcnt lgkmcnt(6)\n"
    "v_mfma_f32_32x32x16_bf16 a[96:111], v[168:171], a[176:179], a[96:111]\n"
    "s_waitcnt vmcnt(20)\n"
    "s_barrier\n"
    "v_mfma_f32_32x32x16_bf16 a[112:127], v[172:175], a[176:179], a[112:127]\n"
    "ds_read_b64_tr_b16 v[196:197], v220 offset:55296\n"
    "ds_read_b64_tr_b16 v[198:199], v220 offset:56320\n"
    "ds_read_b64_tr_b16 v[168:169], v216 offset:49152\n"
    "ds_read_b64_tr_b16 v[170:171], v216 offset:50176\n"
    "v_mfma_f32_32x32x16_bf16 a[96:111], v[176:179], a[180:183], a[96:111]\n"
    "ds_read_b64_tr_b16 v[172:173], v219 offset:49152\n"
    "ds_read_b64_tr_b16 v[174:175], v219 offset:50176\n"
    "v_mfma_f32_32x32x16_bf16 a[112:127], v[180:183], a[180:183], a[112:127]\n"
    "ds_read_b64_tr_b16 v[176:177], v216 offset:55296\n"
    "ds_read_b64_tr_b16 v[178:179], v216 offset:56320\n"
    "s_waitcnt lgkmcnt(6)\n"
    "v_mfma_f32_32x32x16_bf16 a[96:111], v[184:187], a[184:187], a[96:111]\n"
    "ds_read_b64_tr_b16 v[180:181], v219 offset:55296\n"
    "ds_read_b64_tr_b16 v[182:183], v219 offset:56320\n"
    "v_mfma_f32_32x32x16_bf16 a[112:127], v[188:191], a[184:187], a[112:127]\n"
    "ds_read_b64_tr_b16 v[184:185], v216 offset:61440\n"
    "ds_read_b64_tr_b16 v[186:187], v216 offset:62464\n"
    "v_mfma_f32_32x32x16_bf16 a[96:111], v[192:195], a[188:191], a[96:111]\n"
    "ds_read_b64_tr_b16 v[188:189], v219 offset:61440\n"
    "ds_read_b64_tr_b16 v[190:191], v219 offset:62464\n"
    "s_waitcnt vmcnt(0)\n"
    "v_add_u32 v238, 1, v239\n"
    "buffer_store_dword v238, off, s[52:55], s81 sc1\n"
    "v_mfma_f32_32x32x16_bf16 a[112:127], v[196:199], a[188:191], a[112:127]\n"
    "ds_read_b64_tr_b16 v[192:193], v217 offset:2048\n"
    "ds_read_b64_tr_b16 v[194:195], v217 offset:3072\n"
    "s_waitcnt lgkmcnt(6)\n"
    "v_mfma_f32_32x32x16_bf16 a[96:111], v[168:171], v[160:163], a[96:111]\n"
    "ds_read_b64_tr_b16 v[196:197], v220 offset:2048\n"
    "ds_read_b64_tr_b16 v[198:199], v220 offset:3072\n"
    "v_mfma_f32_32x32x16_bf16 a[112:127], v[172:175], v[160:163], a[112:127]\n"
    "ds_read_b64_tr_b16 v[168:169], v216 offset:51200\n"
    "ds_read_b64_tr_b16 v[170:171], v216 offset:52224\n"
    "v_mfma_f32_32x32x16_bf16 a[96:111], v[176:179], v[164:167], a[96:111]\n"
    "ds_read_b64_tr_b16 v[172:173], v219 offset:51200\n"
    "ds_read_b64_tr_b16 v[174:175], v219 offset:52224\n"
    "v_mfma_f32_32x32x16_bf16 a[112:127], v[180:183], v[164:167], a[112:127]\n"
    "ds_read_b64_tr_b16 v[176:177], v216 offset:57344\n"
    "ds_read_b64_tr_b16 v[178:179], v216 offset:58368\n"
    "s_waitcnt lgkmcnt(6)\n"
    "v_mfma_f32_32x32x16_bf16 a[96:111], v[184:187], a[208:211], a[96:111]\n"
    "ds_read_b64_tr_b16 v[180:181], v219 offset:57344\n"
    "ds_read_b64_tr_b16 v[182:183], v219 offset:58368\n"
    "v_mfma_f32_32x32x16_bf16 a[112:127], v[188:191], a[208:211], a[112:127]\n"
    "ds_read_b64_tr_b16 v[184:185], v216 offset:63488\n"
    "ds_read_b64_tr_b16 v[186:187], v216 offset:64512\n"
    "v_mfma_f32_32x32x16_bf16 a[96:111], v[192:195], a[212:215], a[96:111]\n"
    "ds_read_b64_tr_b16 v[188:189], v219 offset:63488\n"
    "ds_read_b64_tr_b16 v[190:191], v219 offset:64512\n"
    "v_mfma_f32_32x32x16_bf16 a[112:127], v[196:199], a[212:215], a[112:127]\n"
    "ds_read_b64_tr_b16 v[192:193], v217 offset:4096\n"
    "ds_read_b64_tr_b16 v[194:195], v217 offset:5120\n"
    "s_waitcnt lgkmcnt(6)\n"
    "v_mfma_f32_32x32x16_bf16 a[96:111], v[168:171], v[144:147], a[96:111]\n"
    "ds_read_b64_tr_b16 v[196:197], v220 offset:4096\n"
    "ds_read_b64_tr_b16 v[198:199], v220 offset:5120\n"
    "v_mfma_f32_32x32x16_bf16 a[112:127], v[172:175], v[144:147], a[112:127]\n"
    "ds_read_b64_tr_b16 v[168:169], v216 offset:53248\n"
    "ds_read_b64_tr_b16 v[170:171], v216 offset:54272\n"
    "v_mfma_f32_32x32x16_bf16 a[96:111], v[176:179], v[148:151], a[96:111]\n"
    "ds_read_b64_tr_b16 v[172:173], v219 offset:53248\n"
    "ds_read_b64_tr_b16 v[174:175], v219 offset:54272\n"
    "v_mfma_f32_32x32x16_bf16 a[112:127], v[180:183], v[148:151], a[112:127]\n"
    "ds_read_b64_tr_b16 v[176:177], v216 offset:59392\n"
    "ds_read_b64_tr_b16 v[178:179], v216 offset:60416\n"
    "s_waitcnt lgkmcnt(6)\n"
    "v_mfma_f32_32x32x16_bf16 a[96:111], v[184:187], v[152:155], a[96:111]\n"
    "ds_read_b64_tr_b16 v[180:181], v219 offset:59392\n"
    "ds_read_b64_tr_b16 v[182:183], v219 offset:60416\n"
    "v_mfma_f32_32x32x16_bf16 a[112:127], v[188:191], v[152:155], a[112:127]\n"
    "ds_read_b64_tr_b16 v[184:185], v217 offset:0\n"
    "ds_read_b64_tr_b16 v[186:187], v217 offset:1024\n"
    "v_mfma_f32_32x32x16_bf16 a[96:111], v[192:195], v[156:159], a[96:111]\n"
    "ds_read_b64_tr_b16 v[188:189], v220 offset:0\n"
    "ds_read_b64_tr_b16 v[190:191], v220 offset:1024\n"
    "v_mfma_f32_32x32x16_bf16 a[112:127], v[196:199], v[156:159], a[112:127]\n"
    "ds_read_b64_tr_b16 v[192:193], v217 offset:6144\n"
    "ds_read_b64_tr_b16 v[194:195], v217 offset:7168\n"
    "s_waitcnt lgkmcnt(6)\n"
    "v_mfma_f32_32x32x16_bf16 a[96:111], v[168:171], a[192:195], a[96:111]\n"
    "s_barrier\n"
    "v_mfma_f32_32x32x16_bf16 a[112:127], v[172:175], a[192:195], a[112:127]\n"
    "ds_read_b64_tr_b16 v[196:197], v220 offset:6144\n"
    "ds_read_b64_tr_b16 v[198:199], v220 offset:7168\n"
    "ds_read_b64_tr_b16 v[168:169], v222 offset:0\n"
    "ds_read_b64_tr_b16 v[170:171], v222 offset:512\n"
    "v_mfma_f32_32x32x16_bf16 a[96:111], v[176:179], a[196:199], a[96:111]\n"
    "ds_read_b64_tr_b16 v[172:173], v222 offset:1024\n"
    "ds_read_b64_tr_b16 v[174:175], v222 offset:1536\n"
    "v_mfma_f32_32x32x16_bf16 a[112:127], v[180:183], a[196:199], a[112:127]\n"
    "ds_read_b64_tr_b16 v[176:177], v222 offset:6144\n"
    "ds_read_b64_tr_b16 v[178:179], v222 offset:6656\n"
    "s_waitcnt lgkmcnt(6)\n"
    "v_mfma_f32_32x32x16_bf16 a[96:111], v[184:187], a[200:203], a[96:111]\n"
    "ds_read_b64_tr_b16 v[180:181], v222 offset:7168\n"
    "ds_read_b64_tr_b16 v[182:183], v222 offset:7680\n"
    "v_mfma_f32_32x32x16_bf16 a[112:127], v[188:191], a[200:203], a[112:127]\n"
    "ds_read_b64_tr_b16 v[184:185], v222 offset:12288\n"
    "ds_read_b64_tr_b16 v[186:187], v222 offset:12800\n"
    "v_mfma_f32_32x32x16_bf16 a[96:111], v[192:195], a[204:207], a[96:111]\n"
    "ds_read_b64_tr_b16 v[188:189], v222 offset:13312\n"
    "ds_read_b64_tr_b16 v[190:191], v222 offset:13824\n"
    "v_mfma_f32_32x32x16_bf16 a[112:127], v[196:199], a[204:207], a[112:127]\n"
    "ds_read_b64_tr_b16 v[192:193], v222 offset:18432\n"
    "ds_read_b64_tr_b16 v[194:195], v222 offset:18944\n"
    "s_waitcnt lgkmcnt(6)\n"
    "v_mfma_f32_32x32x16_bf16 a[128:143], v[168:171], v[96:99], 0\n"
    "ds_read_b64_tr_b16 v[196:197], v222 offset:19456\n"
    "ds_read_b64_tr_b16 v[198:199], v222 offset:19968\n"
    "v_mfma_f32_32x32x16_bf16 a[128:143], v[172:175], v[100:103], a[128:143]\n"
    "ds_read_b64_tr_b16 v[168:169], v222 offset:2048\n"
    "ds_read_b64_tr_b16 v[170:171], v222 offset:2560\n"
    "v_mfma_f32_32x32x16_bf16 a[128:143], v[176:179], v[104:107], a[128:143]\n"
    "ds_read_b64_tr_b16 v[172:173], v222 offset:3072\n"
    "ds_read_b64_tr_b16 v[174:175], v222 offset:3584\n"
    "v_accvgpr_read_b32 v0, a96\n"
    "v_accvgpr_read_b32 v1, a97\n"
    "v_accvgpr_read_b32 v2, a98\n"
    "v_accvgpr_read_b32 v3, a99\n"
    "v_mfma_f32_32x32x16_bf16 a[128:143], v[180:183], v[108:111], a[128:143]\n"
    "ds_read_b64_tr_b16 v[176:177], v222 offset:8192\n"
    "ds_read_b64_tr_b16 v[178:179], v222 offset:8704\n"
    "v_accvgpr_read_b32 v4, a100\n"
    "v_accvgpr_read_b32 v5, a101\n"
    "v_accvgpr_read_b32 v6, a102\n"
    "s_waitcnt lgkmcnt(6)\n"
    "v_mfma_f32_32x32x16_bf16 a[128:143], v[184:187], v[112:115], a[128:143]\n"
    "ds_read_b64_tr_b16 v[180:181], v222 offset:9216\n"
    "ds_read_b64_tr_b16 v[182:183], v222 offset:9728\n"
    "v_accvgpr_read_b32 v7, a103\n"
    "v_cvt_pk_bf16_f32 v208, v0, v1\n"
    "v_cvt_pk_bf16_f32 v209, v2, v3\n"
    "v_cvt_pk_bf16_f32 v210, v4, v5\n"
    "v_mfma_f32_32x32x16_bf16 a[128:143], v[188:191], v[116:119], a[128:143]\n"
    "ds_read_b64_tr_b16 v[184:185], v222 offset:14336\n"
    "ds_read_b64_tr_b16 v[186:187], v222 offset:14848\n"
    "v_cvt_pk_bf16_f32 v211, v6, v7\n"
    "v_mfma_f32_32x32x16_bf16 a[128:143], v[192:195], v[120:123], a[128:143]\n"
    "ds_read_b64_tr_b16 v[188:189], v222 offset:15360\n"
    "ds_read_b64_tr_b16 v[190:191], v222 offset:15872\n"
    "s_add_u32 s85, s72, 98304\n"
    "buffer_store_dwordx4 v[208:211], v250, s[52:55], s85 offen sc1\n"
    "v_mfma_f32_32x32x16_bf16 a[128:143], v[196:199], v[124:127], a[128:143]\n"
    "ds_read_b64_tr_b16 v[192:193], v222 offset:20480\n"
    "ds_read_b64_tr_b16 v[194:195], v222 offset:20992\n"
    "v_accvgpr_read_b32 v8, a104\n"
    "v_accvgpr_read_b32 v9, a105\n"
    "v_accvgpr_read_b32 v10, a106\n"
    "s_waitcnt lgkmcnt(6)\n"
    "v_mfma_f32_32x32x16_bf16 a[128:143], v[168:171], a[144:147], a[128:143]\n"
    "ds_read_b64_tr_b16 v[196:197], v222 offset:21504\n"
    "ds_read_b64_tr_b16 v[198:199], v222 offset:22016\n"
    "v_accvgpr_read_b32 v11, a107\n"
    "v_accvgpr_read_b32 v12, a108\n"
    "v_accvgpr_read_b32 v13, a109\n"
    "v_accvgpr_read_b32 v14, a110\n"
    "v_mfma_f32_32x32x16_bf16 a[128:143], v[172:175], a[148:151], a[128:143]\n"
    "ds_read_b64_tr_b16 v[168:169], v222 offset:4096\n"
    "ds_read_b64_tr_b16 v[170:171], v222 offset:4608\n"
    "v_accvgpr_read_b32 v15, a111\n"
    "v_cvt_pk_bf16_f32 v212, v8, v9\n"
    "v_cvt_pk_bf16_f32 v213, v10, v11\n"
    "v_cvt_pk_bf16_f32 v214, v12, v13\n"
    "v_mfma_f32_32x32x16_bf16 a[128:143], v[176:179], a[152:155], a[128:143]\n"
    "ds_read_b64_tr_b16 v[172:173], v222 offset:5120\n"
    "ds_read_b64_tr_b16 v[174:175], v222 offset:5632\n"
    "v_cvt_pk_bf16_f32 v215, v14, v15\n"
    "v_mfma_f32_32x32x16_bf16 a[128:143], v[180:183], a[156:159], a[128:143]\n"
    "ds_read_b64_tr_b16 v[176:177], v222 offset:10240\n"
    "ds_read_b64_tr_b16 v[178:179], v222 offset:10752\n"
    "s_waitcnt lgkmcnt(6)\n"
    "v_mfma_f32_32x32x16_bf16 a[128:143], v[184:187], a[160:163], a[128:143]\n"
    "ds_read_b64_tr_b16 v[180:181], v222 offset:11264\n"
    "ds_read_b64_tr_b16 v[182:183], v222 offset:11776\n"
    "s_add_u32 s85, s72, 106496\n"
    "buffer_store_dwordx4 v[212:215], v250, s[52:55], s85 offen sc1\n"
    "v_mfma_f32_32x32x16_bf16 a[128:143], v[188:191], a[164:167], a[128:143]\n"
    "ds_read_b64_tr_b16 v[184:185], v222 offset:16384\n"
    "ds_read_b64_tr_b16 v[186:187], v222 offset:16896\n"
    "v_accvgpr_read_b32 v16, a112\n"
    "v_accvgpr_read_b32 v17, a113\n"
    "v_accvgpr_read_b32 v18, a114\n"
    "v_accvgpr_read_b32 v19, a115\n"
    "v_mfma_f32_32x32x16_bf16 a[128:143], v[192:195], a[168:171], a[128:143]\n"
    "ds_read_b64_tr_b16 v[188:189], v222 offset:17408\n"
    "ds_read_b64_tr_b16 v[190:191], v222 offset:17920\n"
    "v_accvgpr_read_b32 v20, a116\n"
    "v_accvgpr_read_b32 v21, a117\n"
    "v_accvgpr_read_b32 v22, a118\n"
    "v_accvgpr_read_b32 v23, a119\n"
    "v_mfma_f32_32x32x16_bf16 a[128:143], v[196:199], a[172:175], a[128:143]\n"
    "ds_read_b64_tr_b16 v[192:193], v222 offset:22528\n"
    "ds_read_b64_tr_b16 v[194:195], v222 offset:23040\n"
    "v_cvt_pk_bf16_f32 v208, v16, v17\n"
    "v_cvt_pk_bf16_f32 v209, v18, v19\n"
    "v_cvt_pk_bf16_f32 v210, v20, v21\n"
    "s_waitcnt lgkmcnt(6)\n"
    "v_mfma_f32_32x32x16_bf16 a[128:143], v[168:171], v[128:131], a[128:143]\n"
    "s_barrier\n"
    "v_mfma_f32_32x32x16_bf16 a[128:143], v[172:175], v[132:135], a[128:143]\n"
    "ds_read_b64_tr_b16 v[196:197], v222 offset:23552\n"
    "ds_read_b64_tr_b16 v[198:199], v222 offset:24064\n"
    "ds_read_b64_tr_b16 v[168:169], v224 offset:8192\n"
    "ds_read_b64_tr_b16 v[170:171], v224 offset:8704\n"
    "v_cvt_pk_bf16_f32 v211, v22, v23\n"
    "v_mfma_f32_32x32x16_bf16 a[128:143], v[176:179], v[136:139], a[128:143]\n"
    "ds_read_b64_tr_b16 v[172:173], v224 offset:9216\n"
    "ds_read_b64_tr_b16 v[174:175], v224 offset:9728\n"
    "s_add_u32 s85, s72, 114688\n"
    "buffer_store_dwordx4 v[208:211], v250, s[52:55], s85 offen sc1\n"
    "v_mfma_f32_32x32x16_bf16 a[128:143], v[180:183], v[140:143], a[128:143]\n"
    "ds_read_b64_tr_b16 v[176:177], v224 offset:14336\n"
    "ds_read_b64_tr_b16 v[178:179], v224 offset:14848\n"
    "v_accvgpr_read_b32 v24, a120\n"
    "v_accvgpr_read_b32 v25, a121\n"
    "v_accvgpr_read_b32 v26, a122\n"
    "s_waitcnt lgkmcnt(6)\n"
    "v_mfma_f32_32x32x16_bf16 a[128:143], v[184:187], v[144:147], a[128:143]\n"
    "ds_read_b64_tr_b16 v[180:181], v224 offset:15360\n"
    "ds_read_b64_tr_b16 v[182:183], v224 offset:15872\n"
    "v_accvgpr_read_b32 v27, a123\n"
    "v_accvgpr_read_b32 v28, a124\n"
    "v_accvgpr_read_b32 v29, a125\n"
    "v_accvgpr_read_b32 v30, a126\n"
    "v_mfma_f32_32x32x16_bf16 a[128:143], v[188:191], v[148:151], a[128:143]\n"
    "ds_read_b64_tr_b16 v[184:185], v224 offset:20480\n"
    "ds_read_b64_tr_b16 v[186:187], v224 offset:20992\n"
    "v_accvgpr_read_b32 v31, a127\n"
    "v_cvt_pk_bf16_f32 v212, v24, v25\n"
    "v_cvt_pk_bf16_f32 v213, v26, v27\n"
    "v_cvt_pk_bf16_f32 v214, v28, v29\n"
    "v_mfma_f32_32x32x16_bf16 a[128:143], v[192:195], v[152:155], a[128:143]\n"
    "ds_read_b64_tr_b16 v[188:189], v224 offset:21504\n"
    "ds_read_b64_tr_b16 v[190:191], v224 offset:22016\n"
    "v_cvt_pk_bf16_f32 v215, v30, v31\n"
    "v_mfma_f32_32x32x16_bf16 a[128:143], v[196:199], v[156:159], a[128:143]\n"
    "ds_read_b64_tr_b16 v[192:193], v224 offset:26624\n"
    "ds_read_b64_tr_b16 v[194:195], v224 offset:27136\n"
    "s_waitcnt lgkmcnt(6)\n"
    "v_mfma_f32_32x32x16_bf16 a[128:143], v[168:171], a[176:179], a[128:143]\n"
    "ds_read_b64_tr_b16 v[196:197], v224 offset:27648\n"
    "ds_read_b64_tr_b16 v[198:199], v224 offset:28160\n"
    "s_add_u32 s85, s72, 122880\n"
    "buffer_store_dwordx4 v[212:215], v250, s[52:55], s85 offen sc1\n"
    "v_mfma_f32_32x32x16_bf16 a[128:143], v[172:175], a[180:183], a[128:143]\n"
    "ds_read_b64_tr_b16 v[168:169], v224 offset:10240\n"
    "ds_read_b64_tr_b16 v[170:171], v224 offset:10752\n"
    "v_mfma_f32_32x32x16_bf16 a[128:143], v[176:179], a[184:187], a[128:143]\n"
    "ds_read_b64_tr_b16 v[172:173], v224 offset:16384\n"
    "ds_read_b64_tr_b16 v[174:175], v224 offset:16896\n"
    "v_mfma_f32_32x32x16_bf16 a[128:143], v[180:183], a[188:191], a[128:143]\n"
    "ds_read_b64_tr_b16 v[176:177], v224 offset:22528\n"
    "ds_read_b64_tr_b16 v[178:179], v224 offset:23040\n"
    "s_waitcnt lgkmcnt(6)\n"
    "v_mfma_f32_32x32x16_bf16 a[128:143], v[184:187], a[192:195], a[128:143]\n"
    "ds_read_b64_tr_b16 v[180:181], v224 offset:28672\n"
    "ds_read_b64_tr_b16 v[182:183], v224 offset:29184\n"
    "v_mfma_f32_32x32x16_bf16 a[128:143], v[188:191], a[196:199], a[128:143]\n"
    "v_mfma_f32_32x32x16_bf16 a[128:143], v[192:195], a[200:203], a[128:143]\n"
    "v_mfma_f32_32x32x16_bf16 a[128:143], v[196:199], a[204:207], a[128:143]\n"
    "s_waitcnt lgkmcnt(0)\n"
    "v_mfma_f32_32x32x16_bf16 a[128:143], v[168:171], v[160:163], a[128:143]\n"
    "v_mfma_f32_32x32x16_bf16 a[128:143], v[172:175], v[164:167], a[128:143]\n"
    "v_mfma_f32_32x32x16_bf16 a[128:143], v[176:179], a[208:211], a[128:143]\n"
    "v_mfma_f32_32x32x16_bf16 a[128:143], v[180:183], a[212:215], a[128:143]\n"
    "s_nop 15\n"
    "v_accvgpr_read_b32 v0, a128\n"
    "v_accvgpr_read_b32 v1, a129\n"
    "v_accvgpr_read_b32 v2, a130\n"
    "v_accvgpr_read_b32 v3, a131\n"
    "v_accvgpr_read_b32 v4, a132\n"
    "v_accvgpr_read_b32 v5, a133\n"
    "v_accvgpr_read_b32 v6, a134\n"
    "v_accvgpr_read_b32 v7, a135\n"
    "v_cvt_pk_bf16_f32 v208, v0, v1\n"
    "v_cvt_pk_bf16_f32 v209, v2, v3\n"
    "v_cvt_pk_bf16_f32 v210, v4, v5\n"
    "v_cvt_pk_bf16_f32 v211, v6, v7\n"
    "s_add_u32 s85, s72, 131072\n"
    "buffer_store_dwordx4 v[208:211], v250, s[52:55], s85 offen sc1\n"
    "v_accvgpr_read_b32 v8, a136\n"
    "v_accvgpr_read_b32 v9, a137\n"
    "v_accvgpr_read_b32 v10, a138\n"
    "v_accvgpr_read_b32 v11, a139\n"
    "v_accvgpr_read_b32 v12, a140\n"
    "v_accvgpr_read_b32 v13, a141\n"
    "v_accvgpr_read_b32 v14, a142\n"
    "v_accvgpr_read_b32 v15, a143\n"
    "v_cvt_pk_bf16_f32 v212, v8, v9\n"
    "v_cvt_pk_bf16_f32 v213, v10, v11\n"
    "v_cvt_pk_bf16_f32 v214, v12, v13\n"
    "v_cvt_pk_bf16_f32 v215, v14, v15\n"
    "s_add_u32 s85, s72, 139264\n"
    "buffer_store_dwordx4 v[212:215], v250, s[52:55], s85 offen sc1\n"
    "buffer_load_dword v229, v234, s[52:55], s80 offen sc1\n"
    "s_waitcnt vmcnt(0)\n"
    "v_add_u32 v238, 2, v239\n"
    "buffer_store_dword v238, off, s[52:55], s81 sc1\n"
    "v_sub_u32 v229, v229, v239\n"
    "v_subrev_u32 v229, 1, v229\n"
    "v_cmp_ge_u32 vcc, 1, v229\n"
    "s_nop 4\n"
    "s_cmp_eq_u64 vcc, -1\n"
    "s_cbranch_scc0 Lslow2_%=\n"
    "Lback2_%=:\n"
    "s_add_u32 s85, s82, 0\n"
    "buffer_load_dwordx4 v[32:35], v232, s[52:55], s85 offen sc1\n"
    "s_add_u32 s85, s82, 4096\n"
    "buffer_load_dwordx4 v[36:39], v232, s[52:55], s85 offen sc1\n"
    "s_add_u32 s85, s82, 16384\n"
    "buffer_load_dwordx4 v[40:43], v232, s[52:55], s85 offen sc1\n"
    "s_add_u32 s85, s82, 20480\n"
    "buffer_load_dwordx4 v[44:47], v232, s[52:55], s85 offen sc1\n"
    "s_add_u32 s85, s82, 32768\n"
    "buffer_load_dwordx4 v[48:51], v232, s[52:55], s85 offen sc1\n"
    "s_add_u32 s85, s82, 36864\n"
    "buffer_load_dwordx4 v[52:55], v232, s[52:55], s85 offen sc1\n"
    "s_add_u32 s85, s82, 49152\n"
    "buffer_load_dwordx4 v[56:59], v232, s[52:55], s85 offen sc1\n"
    "s_add_u32 s85, s82, 53248\n"
    "buffer_load_dwordx4 v[60:63], v232, s[52:55], s85 offen sc1\n"
    "s_add_u32 s85, s82, 65536\n"
    "buffer_load_dwordx4 v[64:67], v232, s[52:55], s85 offen sc1\n"
    "s_add_u32 s85, s82, 69632\n"
    "buffer_load_dwordx4 v[68:71], v232, s[52:55], s85 offen sc1\n"
    "s_add_u32 s85, s82, 81920\n"
    "buffer_load_dwordx4 v[72:75], v232, s[52:55], s85 offen sc1\n"
    "s_add_u32 s85, s82, 86016\n"
    "buffer_load_dwordx4 v[76:79], v232, s[52:55], s85 offen sc1\n"
    "buffer_load_dword v229, v234, s[52:55], s80 offen sc1\n"
    "s_waitcnt vmcnt(0)\n"
    "v_sub_u32 v229, v229, v239\n"
    "v_subrev_u32 v229, 2, v229\n"
    "v_cmp_ge_u32 vcc, 0, v229\n"
    "s_nop 4\n"
    "s_cmp_eq_u64 vcc, -1\n"
    "s_cbranch_scc0 Lslow3_%=\n"
    "Lback3_%=:\n"
    "buffer_store_dword v239, off, s[52:55], s80 sc1\n"
    "s_add_u32 s85, s82, 98304\n"
    "buffer_load_dwordx4 v[80:83], v232, s[52:55], s85 offen sc1\n"
    "s_add_u32 s85, s82, 102400\n"
    "buffer_load_dwordx4 v[84:87], v232, s[52:55], s85 offen sc1\n"
    "s_add_u32 s85, s82, 114688\n"
    "buffer_load_dwordx4 v[88:91], v232, s[52:55], s85 offen sc1\n"
    "s_add_u32 s85, s82, 118784\n"
    "buffer_load_dwordx4 v[92:95], v232, s[52:55], s85 offen sc1\n"
    "s_add_u32 s85, s82, 131072\n"
    "buffer_load_dwordx4 v[96:99], v232, s[52:55], s85 offen sc1\n"
    "s_add_u32 s85, s82, 135168\n"
    "buffer_load_dwordx4 v[100:103], v232, s[52:55], s85 offen sc1\n"
    "s_mov_b32 s86, 0xffff0000\n"
    "v_lshlrev_b32 v200, 16, v32\n"
    "v_lshlrev_b32 v208, 16, v36\n"
    "v_lshlrev_b32 v201, 16, v33\n"
    "v_lshlrev_b32 v209, 16, v37\n"
    "v_lshlrev_b32 v202, 16, v34\n"
    "v_lshlrev_b32 v210, 16, v38\n"
    "v_lshlrev_b32 v203, 16, v35\n"
    "v_lshlrev_b32 v211, 16, v39\n"
    "v_and_b32 v32, s86, v32\n"
    "v_and_b32 v36, s86, v36\n"
    "v_and_b32 v33, s86, v33\n"
    "v_and_b32 v37, s86, v37\n"
    "v_and_b32 v34, s86, v34\n"
    "v_and_b32 v38, s86, v38\n"
    "v_and_b32 v35, s86, v35\n"
    "v_and_b32 v39, s86, v39\n"
    "v_add_f32 v200, v200, v208\n"
    "v_add_f32 v201, v201, v209\n"
    "v_add_f32 v202, v202, v210\n"
    "v_add_f32 v203, v203, v211\n"
    "v_add_f32 v32, v32, v36\n"
    "v_add_f32 v33, v33, v37\n"
    "v_add_f32 v34, v34, v38\n"
    "v_add_f32 v35, v35, v39\n"
    "s_nop 1\n"
    "v_permlane32_swap_b32 v200, v202\n"
    "v_permlane32_swap_b32 v32, v34\n"
    "v_permlane32_swap_b32 v201, v203\n"
    "v_permlane32_swap_b32 v33, v35\n"
    "v_add_f32 v200, v200, v202\n"
    "v_add_f32 v32, v32, v34\n"
    "v_add_f32 v201, v201, v203\n"
    "v_add_f32 v33, v33, v35\n"
    "s_nop 1\n"
    "v_permlane16_swap_b32 v200, v201\n"
    "v_permlane16_swap_b32 v32, v33\n"
    "s_nop 0\n"
    "v_add_f32 v200, v200, v201\n"
    "v_add_f32 v32, v32, v33\n"
    "v_cvt_pk_bf16_f32 v36, v200, v32\n"
    "s_add_u32 s85, s83, 0\n"
    "buffer_store_dword v36, v233, s[76:79], s85 offen sc1\n"
    "v_lshlrev_b32 v200, 16, v40\n"
    "v_lshlrev_b32 v208, 16, v44\n"
    "v_lshlrev_b32 v201, 16, v41\n"
    "v_lshlrev_b32 v209, 16, v45\n"
    "v_lshlrev_b32 v202, 16, v42\n"
    "v_lshlrev_b32 v210, 16, v46\n"
    "v_lshlrev_b32 v203, 16, v43\n"
    "v_lshlrev_b32 v211, 16, v47\n"
    "v_and_b32 v40, s86, v40\n"
    "v_and_b32 v44, s86, v44\n"
    "v_and_b32 v41, s86, v41\n"
    "v_and_b32 v45, s86, v45\n"
    "v_and_b32 v42, s86, v42\n"
    "v_and_b32 v46, s86, v46\n"
    "v_and_b32 v43, s86, v43\n"
    "v_and_b32 v47, s86, v47\n"
    "v_add_f32 v200, v200, v208\n"
    "v_add_f32 v201, v201, v209\n"
    "v_add_f32 v202, v202, v210\n"
    "v_add_f32 v203, v203, v211\n"
    "v_add_f32 v40, v40, v44\n"
    "v_add_f32 v41, v41, v45\n"
    "v_add_f32 v42, v42, v46\n"
    "v_add_f32 v43, v43, v47\n"
    "s_nop 1\n"
    "v_permlane32_swap_b32 v200, v202\n"
    "v_permlane32_swap_b32 v40, v42\n"
    "v_permlane32_swap_b32 v201, v203\n"
    "v_permlane32_swap_b32 v41, v43\n"
    "v_add_f32 v200, v200, v202\n"
    "v_add_f32 v40, v40, v42\n"
    "v_add_f32 v201, v201, v203\n"
    "v_add_f32 v41, v41, v43\n"
    "s_nop 1\n"
    "v_permlane16_swap_b32 v200, v201\n"
    "v_permlane16_swap_b32 v40, v41\n"
    "s_nop 0\n"
    "v_add_f32 v200, v200, v201\n"
    "v_add_f32 v40, v40, v41\n"
    "v_cvt_pk_bf16_f32 v44, v200, v40\n"
    "s_add_u32 s85, s83, 64\n"
    "buffer_store_dword v44, v233, s[76:79], s85 offen sc1\n"
    "v_lshlrev_b32 v200, 16, v48\n"
    "v_lshlrev_b32 v208, 16, v52\n"
    "v_lshlrev_b32 v201, 16, v49\n"
    "v_lshlrev_b32 v209, 16, v53\n"
    "v_lshlrev_b32 v202, 16, v50\n"
    "v_lshlrev_b32 v210, 16, v54\n"
    "v_lshlrev_b32 v203, 16, v51\n"
    "v_lshlrev_b32 v211, 16, v55\n"
    "v_and_b32 v48, s86, v48\n"
    "v_and_b32 v52, s86, v52\n"
    "v_and_b32 v49, s86, v49\n"
    "v_and_b32 v53, s86, v53\n"
    "v_and_b32 v50, s86, v50\n"
    "v_and_b32 v54, s86, v54\n"
    "v_and_b32 v51, s86, v51\n"
    "v_and_b32 v55, s86, v55\n"
    "v_add_f32 v200, v200, v208\n"
    "v_add_f32 v201, v201, v209\n"
    "v_add_f32 v202, v202, v210\n"
    "v_add_f32 v203, v203, v211\n"
    "v_add_f32 v48, v48, v52\n"
    "v_add_f32 v49, v49, v53\n"
    "v_add_f32 v50, v50, v54\n"
    "v_add_f32 v51, v51, v55\n"
    "s_nop 1\n"
    "v_permlane32_swap_b32 v200, v202\n"
    "v_permlane32_swap_b32 v48, v50\n"
    "v_permlane32_swap_b32 v201, v203\n"
    "v_permlane32_swap_b32 v49, v51\n"
    "v_add_f32 v200, v200, v202\n"
    "v_add_f32 v48, v48, v50\n"
    "v_add_f32 v201, v201, v203\n"
    "v_add_f32 v49, v49, v51\n"
    "s_nop 1\n"
    "v_permlane16_swap_b32 v200, v201\n"
    "v_permlane16_swap_b32 v48, v49\n"
    "s_nop 0\n"
    "v_add_f32 v200, v200, v201\n"
    "v_add_f32 v48, v48, v49\n"
    "v_cvt_pk_bf16_f32 v52, v200, v48\n"
    "s_add_u32 s85, s83, 128\n"
    "buffer_store_dword v52, v233, s[76:79], s85 offen sc1\n"
    "v_lshlrev_b32 v200, 16, v56\n"
    "v_lshlrev_b32 v208, 16, v60\n"
    "v_lshlrev_b32 v201, 16, v57\n"
    "v_lshlrev_b32 v209, 16, v61\n"
    "v_lshlrev_b32 v202, 16, v58\n"
    "v_lshlrev_b32 v210, 16, v62\n"
    "v_lshlrev_b32 v203, 16, v59\n"
    "v_lshlrev_b32 v211, 16, v63\n"
    "v_and_b32 v56, s86, v56\n"
    "v_and_b32 v60, s86, v60\n"
    "v_and_b32 v57, s86, v57\n"
    "v_and_b32 v61, s86, v61\n"
    "v_and_b32 v58, s86, v58\n"
    "v_and_b32 v62, s86, v62\n"
    "v_and_b32 v59, s86, v59\n"
    "v_and_b32 v63, s86, v63\n"
    "v_add_f32 v200, v200, v208\n"
    "v_add_f32 v201, v201, v209\n"
    "v_add_f32 v202, v202, v210\n"
    "v_add_f32 v203, v203, v211\n"
    "v_add_f32 v56, v56, v60\n"
    "v_add_f32 v57, v57, v61\n"
    "v_add_f32 v58, v58, v62\n"
    "v_add_f32 v59, v59, v63\n"
    "s_nop 1\n"
    "v_permlane32_swap_b32 v200, v202\n"
    "v_permlane32_swap_b32 v56, v58\n"
    "v_permlane32_swap_b32 v201, v203\n"
    "v_permlane32_swap_b32 v57, v59\n"
    "v_add_f32 v200, v200, v202\n"
    "v_add_f32 v56, v56, v58\n"
    "v_add_f32 v201, v201, v203\n"
    "v_add_f32 v57, v57, v59\n"
    "s_nop 1\n"
    "v_permlane16_swap_b32 v200, v201\n"
    "v_permlane16_swap_b32 v56, v57\n"
    "s_nop 0\n"
    "v_add_f32 v200, v200, v201\n"
    "v_add_f32 v56, v56, v57\n"
    "v_cvt_pk_bf16_f32 v60, v200, v56\n"
    "s_add_u32 s85, s83, 192\n"
    "buffer_store_dword v60, v233, s[76:79], s85 offen sc1\n"
    "v_lshlrev_b32 v200, 16, v64\n"
    "v_lshlrev_b32 v208, 16, v68\n"
    "v_lshlrev_b32 v201, 16, v65\n"
    "v_lshlrev_b32 v209, 16, v69\n"
    "v_lshlrev_b32 v202, 16, v66\n"
    "v_lshlrev_b32 v210, 16, v70\n"
    "v_lshlrev_b32 v203, 16, v67\n"
    "v_lshlrev_b32 v211, 16, v71\n"
    "v_and_b32 v64, s86, v64\n"
    "v_and_b32 v68, s86, v68\n"
    "v_and_b32 v65, s86, v65\n"
    "v_and_b32 v69, s86, v69\n"
    "v_and_b32 v66, s86, v66\n"
    "v_and_b32 v70, s86, v70\n"
    "v_and_b32 v67, s86, v67\n"
    "v_and_b32 v71, s86, v71\n"
    "v_add_f32 v200, v200, v208\n"
    "v_add_f32 v201, v201, v209\n"
    "v_add_f32 v202, v202, v210\n"
    "v_add_f32 v203, v203, v211\n"
    "v_add_f32 v64, v64, v68\n"
    "v_add_f32 v65, v65, v69\n"
    "v_add_f32 v66, v66, v70\n"
    "v_add_f32 v67, v67, v71\n"
    "s_nop 1\n"
    "v_permlane32_swap_b32 v200, v202\n"
    "v_permlane32_swap_b32 v64, v66\n"
    "v_permlane32_swap_b32 v201, v203\n"
    "v_permlane32_swap_b32 v65, v67\n"
    "v_add_f32 v200, v200, v202\n"
    "v_add_f32 v64, v64, v66\n"
    "v_add_f32 v201, v201, v203\n"
    "v_add_f32 v65, v65, v67\n"
    "s_nop 1\n"
    "v_permlane16_swap_b32 v200, v201\n"
    "v_permlane16_swap_b32 v64, v65\n"
    "s_nop 0\n"
    "v_add_f32 v200, v200, v201\n"
    "v_add_f32 v64, v64, v65\n"
    "v_cvt_pk_bf16_f32 v68, v200, v64\n"
    "s_add_u32 s85, s83, 256\n"
    "buffer_store_dword v68, v233, s[76:79], s85 offen sc1\n"
    "v_lshlrev_b32 v200, 16, v72\n"
    "v_lshlrev_b32 v208, 16, v76\n"
    "v_lshlrev_b32 v201, 16, v73\n"
    "v_lshlrev_b32 v209, 16, v77\n"
    "v_lshlrev_b32 v202, 16, v74\n"
    "v_lshlrev_b32 v210, 16, v78\n"
    "v_lshlrev_b32 v203, 16, v75\n"
    "v_lshlrev_b32 v211, 16, v79\n"
    "v_and_b32 v72, s86, v72\n"
    "v_and_b32 v76, s86, v76\n"
    "v_and_b32 v73, s86, v73\n"
    "v_and_b32 v77, s86, v77\n"
    "v_and_b32 v74, s86, v74\n"
    "v_and_b32 v78, s86, v78\n"
    "v_and_b32 v75, s86, v75\n"
    "v_and_b32 v79, s86, v79\n"
    "v_add_f32 v200, v200, v208\n"
    "v_add_f32 v201, v201, v209\n"
    "v_add_f32 v202, v202, v210\n"
    "v_add_f32 v203, v203, v211\n"
    "v_add_f32 v72, v72, v76\n"
    "v_add_f32 v73, v73, v77\n"
    "v_add_f32 v74, v74, v78\n"
    "v_add_f32 v75, v75, v79\n"
    "s_nop 1\n"
    "v_permlane32_swap_b32 v200, v202\n"
    "v_permlane32_swap_b32 v72, v74\n"
    "v_permlane32_swap_b32 v201, v203\n"
    "v_permlane32_swap_b32 v73, v75\n"
    "v_add_f32 v200, v200, v202\n"
    "v_add_f32 v72, v72, v74\n"
    "v_add_f32 v201, v201, v203\n"
    "v_add_f32 v73, v73, v75\n"
    "s_nop 1\n"
    "v_permlane16_swap_b32 v200, v201\n"
    "v_permlane16_swap_b32 v72, v73\n"
    "s_nop 0\n"
    "v_add_f32 v200, v200, v201\n"
    "v_add_f32 v72, v72, v73\n"
    "v_cvt_pk_bf16_f32 v76, v200, v72\n"
    "s_add_u32 s85, s83, 320\n"
    "buffer_store_dword v76, v233, s[76:79], s85 offen sc1\n"
    "s_waitcnt vmcnt(10)\n"
    "v_lshlrev_b32 v200, 16, v80\n"
    "v_lshlrev_b32 v208, 16, v84\n"
    "v_lshlrev_b32 v201, 16, v81\n"
    "v_lshlrev_b32 v209, 16, v85\n"
    "v_lshlrev_b32 v202, 16, v82\n"
    "v_lshlrev_b32 v210, 16, v86\n"
    "v_lshlrev_b32 v203, 16, v83\n"
    "v_lshlrev_b32 v211, 16, v87\n"
    "v_and_b32 v80, s86, v80\n"
    "v_and_b32 v84, s86, v84\n"
    "v_and_b32 v81, s86, v81\n"
    "v_and_b32 v85, s86, v85\n"
    "v_and_b32 v82, s86, v82\n"
    "v_and_b32 v86, s86, v86\n"
    "v_and_b32 v83, s86, v83\n"
    "v_and_b32 v87, s86, v87\n"
    "v_add_f32 v200, v200, v208\n"
    "v_add_f32 v201, v201, v209\n"
    "v_add_f32 v202, v202, v210\n"
    "v_add_f32 v203, v203, v211\n"
    "v_add_f32 v80, v80, v84\n"
    "v_add_f32 v81, v81, v85\n"
    "v_add_f32 v82, v82, v86\n"
    "v_add_f32 v83, v83, v87\n"
    "s_nop 1\n"
    "v_permlane32_swap_b32 v200, v202\n"
    "v_permlane32_swap_b32 v80, v82\n"
    "v_permlane32_swap_b32 v201, v203\n"
    "v_permlane32_swap_b32 v81, v83\n"
    "v_add_f32 v200, v200, v202\n"
    "v_add_f32 v80, v80, v82\n"
    "v_add_f32 v201, v201, v203\n"
    "v_add_f32 v81, v81, v83\n"
    "s_nop 1\n"
    "v_permlane16_swap_b32 v200, v201\n"
    "v_permlane16_swap_b32 v80, v81\n"
    "s_nop 0\n"
    "v_add_f32 v200, v200, v201\n"
    "v_add_f32 v80, v80, v81\n"
    "v_cvt_pk_bf16_f32 v84, v200, v80\n"
    "s_add_u32 s85, s83, 384\n"
    "buffer_store_dword v84, v233, s[76:79], s85 offen sc1\n"
    "s_waitcnt vmcnt(9)\n"
    "v_lshlrev_b32 v200, 16, v88\n"
    "v_lshlrev_b32 v208, 16, v92\n"
    "v_lshlrev_b32 v201, 16, v89\n"
    "v_lshlrev_b32 v209, 16, v93\n"
    "v_lshlrev_b32 v202, 16, v90\n"
    "v_lshlrev_b32 v210, 16, v94\n"
    "v_lshlrev_b32 v203, 16, v91\n"
    "v_lshlrev_b32 v211, 16, v95\n"
    "v_and_b32 v88, s86, v88\n"
    "v_and_b32 v92, s86, v92\n"
    "v_and_b32 v89, s86, v89\n"
    "v_and_b32 v93, s86, v93\n"
    "v_and_b32 v90, s86, v90\n"
    "v_and_b32 v94, s86, v94\n"
    "v_and_b32 v91, s86, v91\n"
    "v_and_b32 v95, s86, v95\n"
    "v_add_f32 v200, v200, v208\n"
    "v_add_f32 v201, v201, v209\n"
    "v_add_f32 v202, v202, v210\n"
    "v_add_f32 v203, v203, v211\n"
    "v_add_f32 v88, v88, v92\n"
    "v_add_f32 v89, v89, v93\n"
    "v_add_f32 v90, v90, v94\n"
    "v_add_f32 v91, v91, v95\n"
    "s_nop 1\n"
    "v_permlane32_swap_b32 v200, v202\n"
    "v_permlane32_swap_b32 v88, v90\n"
    "v_permlane32_swap_b32 v201, v203\n"
    "v_permlane32_swap_b32 v89, v91\n"
    "v_add_f32 v200, v200, v202\n"
    "v_add_f32 v88, v88, v90\n"
    "v_add_f32 v201, v201, v203\n"
    "v_add_f32 v89, v89, v91\n"
    "s_nop 1\n"
    "v_permlane16_swap_b32 v200, v201\n"
    "v_permlane16_swap_b32 v88, v89\n"
    "s_nop 0\n"
    "v_add_f32 v200, v200, v201\n"
    "v_add_f32 v88, v88, v89\n"
    "v_cvt_pk_bf16_f32 v92, v200, v88\n"
    "s_add_u32 s85, s83, 448\n"
    "buffer_store_dword v92, v233, s[76:79], s85 offen sc1\n"
    "s_waitcnt vmcnt(8)\n"
    "v_lshlrev_b32 v200, 16, v96\n"
    "v_lshlrev_b32 v208, 16, v100\n"
    "v_lshlrev_b32 v201, 16, v97\n"
    "v_lshlrev_b32 v209, 16, v101\n"
    "v_lshlrev_b32 v202, 16, v98\n"
    "v_lshlrev_b32 v210, 16, v102\n"
    "v_lshlrev_b32 v203, 16, v99\n"
    "v_lshlrev_b32 v211, 16, v103\n"
    "v_and_b32 v96, s86, v96\n"
    "v_and_b32 v100, s86, v100\n"
    "v_and_b32 v97, s86, v97\n"
    "v_and_b32 v101, s86, v101\n"
    "v_and_b32 v98, s86, v98\n"
    "v_and_b32 v102, s86, v102\n"
    "v_and_b32 v99, s86, v99\n"
    "v_and_b32 v103, s86, v103\n"
    "v_add_f32 v200, v200, v208\n"
    "v_add_f32 v201, v201, v209\n"
    "v_add_f32 v202, v202, v210\n"
    "v_add_f32 v203, v203, v211\n"
    "v_add_f32 v96, v96, v100\n"
    "v_add_f32 v97, v97, v101\n"
    "v_add_f32 v98, v98, v102\n"
    "v_add_f32 v99, v99, v103\n"
    "s_nop 1\n"
    "v_permlane32_swap_b32 v200, v202\n"
    "v_permlane32_swap_b32 v96, v98\n"
    "v_permlane32_swap_b32 v201, v203\n"
    "v_permlane32_swap_b32 v97, v99\n"
    "v_add_f32 v200, v200, v202\n"
    "v_add_f32 v96, v96, v98\n"
    "v_add_f32 v201, v201, v203\n"
    "v_add_f32 v97, v97, v99\n"
    "s_nop 1\n"
    "v_permlane16_swap_b32 v200, v201\n"
    "v_permlane16_swap_b32 v96, v97\n"
    "s_nop 0\n"
    "v_add_f32 v200, v200, v201\n"
    "v_add_f32 v96, v96, v97\n"
    "v_cvt_pk_bf16_f32 v100, v200, v96\n"
    "s_mov_b32 s85, s75\n"
    "buffer_store_dword v100, v233, s[76:79], s85 offen sc1\n"
    "s_branch Lend_%=\n"
    "Lslow0_%=:\n"
    "s_mov_b32 s88, 16384\n"
    "Lspin0_%=:\n"
    "s_sleep 1\n"
    "buffer_load_dword v229, off, s[52:55], s70 sc1\n"
    "s_waitcnt vmcnt(0)\n"
    "v_sub_u32 v229, v229, v230\n"
    "s_nop 0\n"
    "v_readfirstlane_b32 s86, v229\n"
    "s_nop 1\n"
    "s_sub_u32 s86, s86, 1\n"
    "s_cmp_le_u32 s86, 1\n"
    "s_cbranch_scc1 Lback0_%=\n"
    "s_sub_u32 s88, s88, 1\n"
    "s_cmp_lg_u32 s88, 0\n"
    "s_cbranch_scc1 Lspin0_%=\n"
    "s_branch Lback0_%=\n"
    "Lslow1_%=:\n"
    "s_mov_b32 s88, 16384\n"
    "Lspin1_%=:\n"
    "s_sleep 1\n"
    "buffer_load_dword v229, off, s[52:55], s70 sc1\n"
    "s_waitcnt vmcnt(0)\n"
    "v_sub_u32 v229, v229, v230\n"
    "s_nop 0\n"
    "v_readfirstlane_b32 s86, v229\n"
    "s_nop 1\n"
    "s_sub_u32 s86, s86, 2\n"
    "s_cmp_le_u32 s86, 0\n"
    "s_cbranch_scc1 Lback1_%=\n"
    "s_sub_u32 s88, s88, 1\n"
    "s_cmp_lg_u32 s88, 0\n"
    "s_cbranch_scc1 Lspin1_%=\n"
    "s_branch Lback1_%=\n"
    "Lslow2_%=:\n"
    "s_mov_b32 s88, 16384\n"
    "Lspin2_%=:\n"
    "s_sleep 1\n"
    "buffer_load_dword v229, v234, s[52:55], s80 offen sc1\n"
    "s_waitcnt vmcnt(0)\n"
    "v_sub_u32 v229, v229, v239\n"
    "v_subrev_u32 v229, 1, v229\n"
    "v_cmp_ge_u32 vcc, 1, v229\n"
    "s_nop 4\n"
    "s_cmp_eq_u64 vcc, -1\n"
    "s_cbranch_scc1 Lback2_%=\n"
    "s_sub_u32 s88, s88, 1\n"
    "s_cmp_lg_u32 s88, 0\n"
    "s_cbranch_scc1 Lspin2_%=\n"
    "s_branch Lback2_%=\n"
    "Lslow3_%=:\n"
    "s_mov_b32 s88, 16384\n"
    "Lspin3_%=:\n"
    "s_sleep 1\n"
    "buffer_load_dword v229, v234, s[52:55], s80 offen sc1\n"
    "s_waitcnt vmcnt(0)\n"
    "v_sub_u32 v229, v229, v239\n"
    "v_subrev_u32 v229, 2, v229\n"
    "v_cmp_ge_u32 vcc, 0, v229\n"
    "s_nop 4\n"
    "s_cmp_eq_u64 vcc, -1\n"
    "s_cbranch_scc1 Lback3_%=\n"
    "s_sub_u32 s88, s88, 1\n"
    "s_cmp_lg_u32 s88, 0\n"
    "s_cbranch_scc1 Lspin3_%=\n"
    "s_branch Lback3_%=\n"
    "Lend_%=:\n"
    ".Lgeglu_code_end:\n"
    : "+{s[36:51]}"(s0), "+{s[52:67]}"(s1), "+{s[68:75]}"(s2), "+{v[240:255]}"(v), "+{s[76:83]}"(s3), "+{v[232:239]}"(v2), "+{v229}"(pfd) TPRE_OP
    :
    : "v0","v1","v2","v3","v4","v5","v6","v7","v8","v9","v10","v11","v12","v13","v14","v15","v16","v17","v18","v19","v20","v21","v22","v23","v24","v25","v26","v27","v28","v29","v30","v31","v32","v33","v34","v35","v36","v37","v38","v39","v40","v41","v42","v43","v44","v45","v46","v47","v48","v49","v50","v51","v52","v53","v54","v55","v56","v57","v58","v59","v60","v61","v62","v63","v64","v65","v66","v67","v68","v69","v70","v71","v72","v73","v74","v75","v76","v77","v78","v79","v80","v81","v82","v83","v84","v85","v86","v87","v88","v89","v90","v91","v92","v93","v94","v95","v96","v97","v98","v99","v100","v101","v102","v103","v104","v105","v106","v107","v108","v109","v110","v111","v112","v113","v114","v115","v116","v117","v118","v119","v120","v121","v122","v123","v124","v125","v126","v127","v128","v129","v130","v131","v132","v133","v134","v135","v136","v137","v138","v139","v140","v141","v142","v143","v144","v145","v146","v147","v148","v149","v150","v151","v152","v153","v154","v155","v156","v157","v158","v159","v160","v161","v162","v163","v164","v165","v166","v167","v168","v169","v170","v171","v172","v173","v174","v175","v176","v177","v178","v179","v180","v181","v182","v183","v184","v185","v186","v187","v188","v189","v190","v191","v192","v193","v194","v195","v196","v197","v198","v199","v200","v201","v202","v203","v204","v205","v206","v207","v208","v209","v210","v211","v212","v213","v214","v215","v216","v217","v218","v219","v220","v221","v222","v223","v224","v225","v226","v227","v228","v230","v231", "a0","a1","a2","a3","a4","a5","a6","a7","a8","a9","a10","a11","a12","a13","a14","a15","a16","a17","a18","a19","a20","a21","a22","a23","a24","a25","a26","a27","a28","a29","a30","a31","a32","a33","a34","a35","a36","a37","a38","a39","a40","a41","a42","a43","a44","a45","a46","a47","a48","a49","a50","a51","a52","a53","a54","a55","a56","a57","a58","a59","a60","a61","a62","a63","a64","a65","a66","a67","a68","a69","a70","a71","a72","a73","a74","a75","a76","a77","a78","a79","a80","a81","a82","a83","a84","a85","a86","a87","a88","a89","a90","a91","a92","a93","a94","a95","a96","a97","a98","a99","a100","a101","a102","a103","a104","a105","a106","a107","a108","a109","a110","a111","a112","a113","a114","a115","a116","a117","a118","a119","a120","a121","a122","a123","a124","a125","a126","a127","a128","a129","a130","a131","a132","a133","a134","a135","a136","a137","a138","a139","a140","a141","a142","a143","a144","a145","a146","a147","a148","a149","a150","a151","a152","a153","a154","a155","a156","a157","a158","a159","a160","a161","a162","a163","a164","a165","a166","a167","a168","a169","a170","a171","a172","a173","a174","a175","a176","a177","a178","a179","a180","a181","a182","a183","a184","a185","a186","a187","a188","a189","a190","a191","a192","a193","a194","a195","a196","a197","a198","a199","a200","a201","a202","a203","a204","a205","a206","a207","a208","a209","a210","a211","a212","a213","a214","a215","a216","a217","a218","a219","a220","a221","a222","a223","a224","a225","a226","a227","a228","a229","a230","a231","a232","a233","a234","a235","a236","a237","a238","a239","a240","a241","a242","a243","a244","a245","a246","a247","a248","a249","a250","a251","a252","a253","a254","a255", "s84","s85","s86","s87","s88","s89","s90","s91","s92","s93","s94","s95","s96","s97","s98","s99","s100","s101", "vcc", "memory");
}

extern "C" void kernel_launch(void* const* d_in, const int* in_sizes, int n_in, void* d_out, int out_size, void* d_ws,
                              size_t ws_size, hipStream_t stream) {
  geglu_k1<<<256, 256, 0, stream>>>((const char*)d_in[0], (const char*)d_in[1], (const char*)d_in[2], (const char*)d_in[3], (char*)d_ws, (char*)d_out);
}
```
